```python
import torch

def _softmax(x, axis=-1):
    return torch.softmax(x, dim=axis)
def _gelu(x):
    return torch.nn.functional.gelu(x, approximate="tanh")
def _silu(x):
    return torch.nn.functional.silu(x)
def _sigmoid(x):
    return torch.sigmoid(x)
def _layernorm(x, axis=-1, eps=1e-5):
    m = x.mean(dim=axis, keepdim=True)
    v = x.var(dim=axis, keepdim=True, unbiased=False)
    return (x - m) / torch.sqrt(v + eps)
def _scatter_add(tgt, idx, src, axis=0):
    return tgt.clone().index_add_(axis, idx, src)
def _segment_sum(src, seg, n):
    out = torch.zeros((n,) + src.shape[1:], dtype=src.dtype)
    return out.index_add_(0, seg, src)
def _pad(x, pw):
    flat = tuple(v for pair in reversed(pw) for v in pair)
    return torch.nn.functional.pad(x, flat)

kernel_name = 'concat_heads__sliding_block__sliding_block__prompt'

def setup_inputs():

    _w = (lambda a: (lambda t: t.to(torch.bfloat16) if t.is_floating_point() else t)(torch.as_tensor(a)))
    def _A_setup():
        B, S, D = 1, 2048, 1024
        return {
            "x": _w((lambda *s: torch.randn(*s, dtype=torch.bfloat16) * 0.2)(B, S, D)),
            "wqkv": _w((lambda *s: torch.randn(*s, dtype=torch.bfloat16) * 0.2)(D, 3 * D)),
            "wo": _w((lambda *s: torch.randn(*s, dtype=torch.bfloat16) * 0.05)(D, D)),
        }
    _A_in = _A_setup()

    def _B_setup():
        B, S, D = 1, 2048, 1024
        return {
            "x": _w((lambda *s: torch.randn(*s, dtype=torch.bfloat16) * 0.2)(B, S, D)),
            "wqkv": _w((lambda *s: torch.randn(*s, dtype=torch.bfloat16) * 0.2)(D, 3 * D)),
            "wo": _w((lambda *s: torch.randn(*s, dtype=torch.bfloat16) * 0.05)(D, D)),
        }
    _B_in = _B_setup()

    return {
        "A_x": _A_in["x"],
        "A_wqkv": _A_in["wqkv"],
        "A_wo": _A_in["wo"],
        "B_x": _B_in["x"],
        "B_wqkv": _B_in["wqkv"],
        "B_wo": _B_in["wo"],
    }

def reference(A_x, A_wqkv, A_wo, B_x, B_wqkv, B_wo):
    def _A_ref(x, wqkv, wo):
        B, S, D = x.shape
        H = 32; Dh = D // H; W = 256
        qkv = (lambda x, *d: x.permute(*d))((x @ wqkv).reshape(B, S, 3, H, Dh), 2, 0, 3, 1, 4)
        q, k, v = qkv[0], qkv[1], qkv[2]
        scores = torch.einsum("bhqd,bhkd->bhqk", q, k) * (Dh ** -0.5)
        idx = torch.arange(S)
        mask = (idx[:, None] - idx[None, :] >= 0) & (idx[:, None] - idx[None, :] < W)
        scores = torch.where(mask[None, None], scores, -1e9)
        p = _softmax(scores, axis=-1)
        o = torch.einsum("bhqk,bhkd->bhqd", p, v)
        return (lambda x, *d: x.permute(*d))(o, 0, 2, 1, 3).reshape(B, S, D) @ wo

    def _B_ref(x, wqkv, wo):
        B, S, D = x.shape
        H = 32; Dh = D // H; W = 256
        qkv = (lambda x, *d: x.permute(*d))((x @ wqkv).reshape(B, S, 3, H, Dh), 2, 0, 3, 1, 4)
        q, k, v = qkv[0], qkv[1], qkv[2]
        scores = torch.einsum("bhqd,bhkd->bhqk", q, k) * (Dh ** -0.5)
        idx = torch.arange(S)
        mask = (idx[:, None] - idx[None, :] >= 0) & (idx[:, None] - idx[None, :] < W)
        scores = torch.where(mask[None, None], scores, -1e9)
        p = _softmax(scores, axis=-1)
        o = torch.einsum("bhqk,bhkd->bhqd", p, v)
        return (lambda x, *d: x.permute(*d))(o, 0, 2, 1, 3).reshape(B, S, D) @ wo

    a = _A_ref(A_x, A_wqkv, A_wo)
    b = _B_ref(B_x, B_wqkv, B_wo)
    return torch.concatenate([a, b], axis=-1)
```

```cpp
#include <hip/hip_runtime.h>
#include <hip/hip_bf16.h>
typedef __hip_bfloat16 bf16;
__global__ void gemm_naive(const bf16* A, const bf16* B, bf16* C, int M, int N, int K, int ldc) {
    __shared__ float As[32][33], Bs[32][33];
    int tx = threadIdx.x & 31, ty = threadIdx.x >> 5;
    int m0 = blockIdx.y * 32, n0 = blockIdx.x * 32;
    float acc[4] = {0, 0, 0, 0};
    for (int k0 = 0; k0 < K; k0 += 32) {
        for (int i = 0; i < 4; ++i) {
            As[ty + 8 * i][tx] = __bfloat162float(A[(size_t)(m0 + ty + 8 * i) * K + k0 + tx]);
            Bs[ty + 8 * i][tx] = __bfloat162float(B[(size_t)(k0 + ty + 8 * i) * N + n0 + tx]);
        }
        __syncthreads();
        for (int k = 0; k < 32; ++k)
            for (int i = 0; i < 4; ++i) acc[i] += As[ty + 8 * i][k] * Bs[k][tx];
        __syncthreads();
    }
    for (int i = 0; i < 4; ++i) C[(size_t)(m0 + ty + 8 * i) * ldc + n0 + tx] = __float2bfloat16(acc[i]);
}
__global__ void attn_naive(const bf16* qkv, bf16* o) {
    int q = blockIdx.x, h = blockIdx.y, lane = threadIdx.x;
    const bf16* qp = qkv + (size_t)q * 3072 + h * 32;
    float s[4]; float m = -1e30f;
    for (int i = 0; i < 4; ++i) {
        int key = q - (lane + 64 * i);
        float a = -1e30f;
        if (key >= 0) {
            const bf16* kp = qkv + (size_t)key * 3072 + 1024 + h * 32;
            a = 0;
            for (int d = 0; d < 32; ++d) a += __bfloat162float(qp[d]) * __bfloat162float(kp[d]);
            a *= 0.17677669529663687f;
        }
        s[i] = a; m = fmaxf(m, a);
    }
    for (int off = 32; off; off >>= 1) m = fmaxf(m, __shfl_xor(m, off));
    float l = 0;
    for (int i = 0; i < 4; ++i) { int key = q - (lane + 64 * i); s[i] = key >= 0 ? __expf(s[i] - m) : 0.f; l += s[i]; }
    for (int off = 32; off; off >>= 1) l += __shfl_xor(l, off);
    for (int d = 0; d < 32; ++d) {
        float a = 0;
        for (int i = 0; i < 4; ++i) { int key = q - (lane + 64 * i); if (key >= 0) a += s[i] * __bfloat162float(qkv[(size_t)key * 3072 + 2048 + h * 32 + d]); }
        for (int off = 32; off; off >>= 1) a += __shfl_xor(a, off);
        if (lane == 0) o[(size_t)q * 1024 + h * 32 + d] = __float2bfloat16(a / l);
    }
}
extern "C" void kernel_launch(void* const* d_in, const int* in_sizes, int n_in, void* d_out, int out_size, void* d_ws, size_t ws_size, hipStream_t stream) {
    bf16* out = (bf16*)d_out;
    for (int b = 0; b < 2; ++b) {
        const bf16* x = (const bf16*)d_in[3 * b]; const bf16* wqkv = (const bf16*)d_in[3 * b + 1]; const bf16* wo = (const bf16*)d_in[3 * b + 2];
        bf16* qkv = (bf16*)d_ws + (size_t)b * 2048 * 4096; bf16* o = qkv + (size_t)2048 * 3072;
        gemm_naive<<<dim3(96, 64), 256, 0, stream>>>(x, wqkv, qkv, 2048, 3072, 1024, 3072);
        attn_naive<<<dim3(2048, 32), 64, 0, stream>>>(qkv, o);
        gemm_naive<<<dim3(32, 64), 256, 0, stream>>>(o, wo, out + b * 1024, 2048, 1024, 1024, 2048);
    }
}
```

```cpp
#include <hip/hip_runtime.h>
#include <hip/hip_bf16.h>
#include <type_traits>
typedef short bf16x8 __attribute__((ext_vector_type(8)));
typedef short s16x4 __attribute__((ext_vector_type(4)));
typedef float f32x16 __attribute__((ext_vector_type(16)));
typedef float f32x4 __attribute__((ext_vector_type(4)));
typedef float f32x2 __attribute__((ext_vector_type(2)));
typedef __bf16 bf16x2 __attribute__((ext_vector_type(2)));
typedef unsigned u32x4 __attribute__((ext_vector_type(4)));
typedef unsigned short u16;
typedef unsigned long long u64;
#define LAS __attribute__((address_space(3)))
#define GAS __attribute__((address_space(1)))
#define DI __device__ __forceinline__
#define MFMA16(a, b, c) __builtin_amdgcn_mfma_f32_16x16x32_bf16((a), (b), (c), 0, 0, 0)
#define MFMA32(a, b, c) __builtin_amdgcn_mfma_f32_32x32x16_bf16((a), (b), (c), 0, 0, 0)
#define SBAR() __builtin_amdgcn_sched_barrier(0)
#define BAR() do { SBAR(); asm volatile("s_barrier" ::: "memory"); SBAR(); } while (0)
#define WAITL0() __builtin_amdgcn_s_waitcnt(0xC07F)
template <int N> DI void waitv() { asm volatile("s_waitcnt vmcnt(%0)" :: "i"(N) : "memory"); }
DI unsigned rfl(unsigned x) { return __builtin_amdgcn_readfirstlane(x); }
DI unsigned cvtpk(float lo, float hi) { f32x2 v = {lo, hi}; bf16x2 b = __builtin_convertvector(v, bf16x2); return __builtin_bit_cast(unsigned, b); }
DI void dma16(unsigned m0v, unsigned voff, __amdgpu_buffer_rsrc_t srd, unsigned soff) {
    asm volatile("s_mov_b32 m0, %0\n\ts_nop 0\n\tbuffer_load_dwordx4 %1, %2, %3 offen lds" :: "s"(m0v), "v"(voff), "s"(srd), "s"(soff) : "memory");
}
DI s16x4 trread(LAS const char* p) { return __builtin_amdgcn_ds_read_tr16_b64_v4i16((LAS s16x4*)p); }
DI bf16x8 ldsfrag(LAS const char* p) { return *(LAS const bf16x8*)p; }
DI float max3f(float a, float b, float c) { return __builtin_fmaxf(__builtin_fmaxf(a, b), c); }
DI bf16x8 pack8(float a, float b, float c, float d, float e, float f, float g, float h) { u32x4 v = {cvtpk(a, b), cvtpk(c, d), cvtpk(e, f), cvtpk(g, h)}; return __builtin_bit_cast(bf16x8, v); }

constexpr int LDS_A0 = 0, LDS_A1 = 32768, LDS_B = 81920, A_SLOT = 16384, B_SLOT = 24576;
constexpr int LDS_OWN = 0, LDS_PREV = 65536, LDS_WO0 = 131072;
constexpr int LDS_TOTAL = 163840;
constexpr size_t WS_FKV = 0, WS_EKV = 32768, WS_FO = 65536, WS_EO = 98304, WS_KV = 1 << 20, WS_O = 17 << 20, WS_END = 25 << 20, WS_ST = 32 << 20;
constexpr u64 TAGK = 0x9E3779B97F4A7C15ull;
constexpr int SPIN_CAP = 400000;
constexpr float QSCALE = 0.17677669529663687f * 1.4426950408889634f;

struct Args { const u16* x[2]; const u16* w[2]; const u16* wo[2]; u16* out; char* ws; };

#define STAMP(i) do {} while (0)

__global__ __launch_bounds__(512, 2) void fused_kernel(Args a) {
    __shared__ __attribute__((aligned(16))) char smem[LDS_TOTAL];
    LAS char* lds = (LAS char*)smem;
    const unsigned ldsb = (unsigned)(size_t)smem;
    const int tid = threadIdx.x, lane = tid & 63;
    const int wid = rfl(tid >> 6), half = wid >> 2, w4 = wid & 3;
    const int bid = blockIdx.x, g = bid & 7, j = bid >> 3;
    const int br = g >> 2, rb = ((g >> 1) & 1) * 4 + (j & 3), hp = (g & 1) * 8 + (j >> 2);
    const int me = (br * 8 + rb) * 16 + hp, grp = me & ~15;
    STAMP(0);
    u64 ew = 0;
    if (wid == 0) {
        const size_t eoff = lane < 16 ? WS_EO + (size_t)(grp + lane) * 128 + hp * 8 : lane < 32 ? WS_EO + (size_t)me * 128 + (lane - 16) * 8
                          : lane == 32 ? WS_EKV + (size_t)me * 128 : WS_EKV + (size_t)(rb > 0 ? me - 16 : me) * 128;
        ew = __hip_atomic_load((GAS u64*)(a.ws + eoff), __ATOMIC_RELAXED, __HIP_MEMORY_SCOPE_AGENT);
    }
    const __amdgpu_buffer_rsrc_t srdA = __builtin_amdgcn_make_buffer_rsrc((void*)(a.x[br] + (size_t)rb * 256 * 1024), (short)0, 256 * 2048, 0x00020000);
    const __amdgpu_buffer_rsrc_t srdB = __builtin_amdgcn_make_buffer_rsrc((void*)(a.w[br]), (short)0, 1024 * 6144, 0x00020000);
    const int wr = 2 * half + (w4 >> 1), wc = w4 & 1;
    const int r16 = lane & 15, g4 = lane >> 4, q4 = (lane & 15) >> 2, p4 = lane & 3;
    f32x4 acc[4][6];
    {
        const unsigned voffE = (lane >> 3) * 2048 + (((lane & 7) ^ (lane >> 4)) << 4);
        const unsigned voffO = (8 + (lane >> 3)) * 2048 + (((lane & 7) ^ (4 + (lane >> 4))) << 4);
        const unsigned voffB0 = (lane >> 3) * 6144 + (((lane & 7) ^ (((lane >> 4) & 1) << 1)) << 4);
        const unsigned voffB1 = (lane >> 3) * 6144 + (((lane & 7) ^ ((((lane >> 4) & 1) << 1) | 4)) << 4);
        const int gt0 = (w4 & 1) ? 7 - (w4 >> 1) : (w4 >> 1);
        const int gt1 = (w4 & 1) ? 5 - (w4 >> 1) : 2 + (w4 >> 1);
        const unsigned sA0 = gt0 * 32 * 2048, sA1 = gt1 * 32 * 2048;
        const unsigned sB = hp * 128 + (2 * w4) * 8 * 6144;
        unsigned aoff[2], boff[2];
#pragma unroll
        for (int s = 0; s < 2; ++s) aoff[s] = (half ? LDS_A1 : LDS_A0) + ((w4 >> 1) * 64 + r16) * 128 + (((4 * s + g4) ^ ((r16 >> 1) & 7)) << 4);
#pragma unroll
        for (int c = 0; c < 2; ++c) boff[c] = LDS_B + (8 * g4 + q4) * 128 + (((wc * 2 + c) ^ ((q4 >> 1) | ((g4 & 1) << 1))) << 5) + p4 * 8;
#pragma unroll
        for (int i = 0; i < 4; ++i)
#pragma unroll
            for (int s = 0; s < 6; ++s) acc[i][s] = (f32x4){0.f, 0.f, 0.f, 0.f};
        bf16x8 af[4][2], bfr[6][2];
        auto run = [&](auto HC) {
            constexpr int H = decltype(HC)::value;
            auto piece = [&](int kt, int s2, int s3i, int i) {
                const unsigned soB = sB + kt * (64 * 6144), dB = ldsb + LDS_B + s3i * B_SLOT + w4 * 2048;
                const unsigned soA0 = sA0 + kt * 128, dA0 = ldsb + LDS_A0 + s2 * A_SLOT + w4 * 4096;
                const unsigned soA1 = sA1 + kt * 128, dA1 = ldsb + LDS_A1 + s3i * A_SLOT + w4 * 4096;
                if (H == 0) {
                    if (i == 0) dma16(dA1 + 3 * 1024, voffO, srdA, soA1 + 32768);
                    else { const int k = i - 1; dma16(dB + (k >> 1) * 8192 + (k & 1) * 1024, (k & 1) ? voffB1 : voffB0, srdB, soB + (k >> 1) * 2048 + (k & 1) * 8 * 6144); }
                } else dma16((i < 4 ? dA0 : dA1) + (i & 3) * 1024, (i & 1) ? voffO : voffE, srdA, (i < 4 ? soA0 : soA1) + ((i >> 1) & 1) * 32768);
            };
#pragma unroll
            for (int i = 0; i < 7; ++i) piece(0, 0, 0, i);
#pragma unroll
            for (int i = 0; i < 7; ++i) piece(1, 1, 1, i);
            if (H == 0) waitv<7>(); else waitv<10>();
            BAR();
            if (H == 1) { waitv<7>(); BAR(); }
            STAMP(1);
            int s3 = 0;
            auto Lseg = [&](int t, bool issue) {
                const unsigned aslot = H ? s3 * A_SLOT : (t & 1) * A_SLOT;
                const unsigned bslot = s3 * B_SLOT;
                const int s3n = (s3 == 0) ? 2 : s3 - 1;
                auto rdA = [&](int i) { LAS const char* pa = lds + aoff[i >> 1] + aslot + (i & 1) * 4096; af[(i & 1) * 2][i >> 1] = ldsfrag(pa); af[(i & 1) * 2 + 1][i >> 1] = ldsfrag(pa + 2048); };
                auto rdB = [&](int i) { const int s = i / 6, seg = (i % 6) >> 1, c = i & 1; LAS const char* pb = lds + boff[c] + bslot;
                    s16x4 lo = trread(pb + seg * 8192 + s * 4096), hi = trread(pb + seg * 8192 + s * 4096 + 512);
                    bfr[seg * 2 + c][s] = __builtin_shufflevector(lo, hi, 0, 1, 2, 3, 4, 5, 6, 7); };
#pragma unroll
                for (int i = 0; i < 16; ++i) {
                    if (issue && (i & 1) == 0 && i / 2 < 7) piece(t + 2, t & 1, s3n, i / 2);
                    if (i < 4) rdA(i); else rdB(i - 4);
                }
                s3 = (s3 == 2) ? 0 : s3 + 1;
            };
            auto Mseg = [&]() {
                __builtin_amdgcn_s_setprio(1);
#pragma unroll
                for (int s = 0; s < 2; ++s)
#pragma unroll
                    for (int m = 0; m < 4; ++m)
#pragma unroll
                        for (int n = 0; n < 6; ++n) acc[m][n] = n < 4 ? MFMA16(bfr[n][s], af[m][s], acc[m][n]) : MFMA16(af[m][s], bfr[n][s], acc[m][n]);
                __builtin_amdgcn_s_setprio(0);
            };
            for (int t = 0; t < 14; ++t) {
                Lseg(t, true);
                if (H == 1) waitv<10>();
                WAITL0(); BAR();
                Mseg();
                waitv<7>();
                BAR();
            }
            Lseg(14, false); if (H == 1) waitv<3>(); WAITL0(); BAR(); Mseg(); waitv<0>(); BAR();
            Lseg(15, false); WAITL0(); BAR(); Mseg(); if (H == 0) BAR();
        };
        if (half == 0) run(std::integral_constant<int, 0>{}); else run(std::integral_constant<int, 1>{});
    }
    STAMP(2);
    asm volatile("" : "+v"(ew));
    const __amdgpu_buffer_rsrc_t srdW = __builtin_amdgcn_make_buffer_rsrc((void*)a.ws, (short)0, (int)WS_END, 0x00020000);
    const __amdgpu_buffer_rsrc_t srdWo = __builtin_amdgcn_make_buffer_rsrc((void*)(a.wo[br]), (short)0, 1024 * 2048, 0x00020000);
    const int ct = hp & 7, th = hp >> 3;
    const unsigned voffW0 = (lane >> 4) * 2048 + (((lane & 15) ^ ((lane >> 4) << 1)) << 4), voffW1 = (4 + (lane >> 4)) * 2048 + (((lane & 15) ^ ((4 + (lane >> 4)) << 1)) << 4);
    const unsigned sW = ct * 256 + (half * 512 + w4 * 8) * 2048;
    auto wo_step = [&](int s, int slot) { const unsigned d = ldsb + slot * 16384 + wid * 2048; dma16(d, voffW0, srdWo, sW + s * 65536); dma16(d + 1024, voffW1, srdWo, sW + s * 65536); };
    {
        const unsigned kpos = (g4 >> 1) * 1024 + (g4 & 1) * 512 + r16 * 16;
        const unsigned vpos = 2048 + ((g4 & 1) * 32 + (g4 >> 1) * 16 + r16) * 16;
        const unsigned gkv = (unsigned)WS_KV + me * 65536;
#pragma unroll
        for (int mi = 0; mi < 2; ++mi) {
            const int gt = mi ? 7 - wr : wr;
            const unsigned blk = (wc * 8 + gt) * 4096;
#pragma unroll
            for (int s = 0; s < 2; ++s) {
                const f32x4 k0 = acc[2 * mi + s][2], k1 = acc[2 * mi + s][3], v0 = acc[2 * mi + s][4], v1 = acc[2 * mi + s][5];
                const u32x4 kv = {cvtpk(k0[0], k0[1]), cvtpk(k0[2], k0[3]), cvtpk(k1[0], k1[1]), cvtpk(k1[2], k1[3])};
                const auto ra = __builtin_amdgcn_permlane32_swap(cvtpk(v0[0], v0[1]), cvtpk(v1[0], v1[1]), false, false);
                const auto rbb = __builtin_amdgcn_permlane32_swap(cvtpk(v0[2], v0[3]), cvtpk(v1[2], v1[3]), false, false);
                const u32x4 vv = {ra[0], rbb[0], ra[1], rbb[1]};
                if (rb < 7) {
                    __builtin_amdgcn_raw_buffer_store_b128(kv, srdW, gkv + blk + kpos + s * 256, 0, 16);
                    __builtin_amdgcn_raw_buffer_store_b128(vv, srdW, gkv + blk + vpos + s * 1024, 0, 16);
                }
                *(LAS u32x4*)(lds + LDS_OWN + blk + kpos + s * 256) = kv;
                *(LAS u32x4*)(lds + LDS_OWN + blk + vpos + s * 1024) = vv;
                const f32x4 q0 = acc[2 * mi + s][0] * QSCALE, q1 = acc[2 * mi + s][1] * QSCALE;
                const u32x4 qv = {cvtpk(q0[0], q0[1]), cvtpk(q0[2], q0[3]), cvtpk(q1[0], q1[1]), cvtpk(q1[2], q1[3])};
                *(LAS u32x4*)(lds + LDS_PREV + wid * 4096 + mi * 2048 + kpos + s * 256) = qv;
            }
        }
    }
    wo_step(0, 8); wo_step(1, 9);
    bf16x8 qf[2][2];
#pragma unroll
    for (int mi = 0; mi < 2; ++mi)
#pragma unroll
        for (int s = 0; s < 2; ++s) qf[mi][s] = ldsfrag(lds + LDS_PREV + wid * 4096 + mi * 2048 + s * 1024 + lane * 16);
    WAITL0(); BAR();
    STAMP(3);
    f32x16 oa, ob; float ma = -INFINITY, mb = -INFINITY; f32x2 la = {0.f, 0.f}, lb = {0.f, 0.f};
#pragma unroll
    for (int r = 0; r < 16; ++r) { oa[r] = 0.f; ob[r] = 0.f; }
    const f32x16 zero16 = oa;
    auto smax = [&](const f32x16& s, float m) {
#pragma unroll
        for (int r = 0; r < 16; r += 2) m = max3f(m, s[r], s[r + 1]);
        return m; };
    auto xmax = [&](float m) { const auto r = __builtin_amdgcn_permlane32_swap(__float_as_uint(m), __float_as_uint(m), false, false); return __builtin_fmaxf(__uint_as_float(r[0]), __uint_as_float(r[1])); };
    auto qk = [&](LAS const char* p, const bf16x8 (&q)[2], const f32x16& c) { f32x16 s = MFMA32(ldsfrag(p), q[0], c); return MFMA32(ldsfrag(p + 1024), q[1], s); };
    auto expv = [&](f32x16 s, LAS const char* p, f32x16& o, f32x2& l) {
#pragma unroll
        for (int r = 0; r < 16; ++r) s[r] = __builtin_amdgcn_exp2f(s[r]);
#pragma unroll
        for (int r = 0; r < 16; r += 2) l += (f32x2){s[r], s[r + 1]};
        const bf16x8 p0 = pack8(s[0], s[1], s[2], s[3], s[4], s[5], s[6], s[7]), p1 = pack8(s[8], s[9], s[10], s[11], s[12], s[13], s[14], s[15]);
        o = MFMA32(ldsfrag(p + 2048), p0, o); o = MFMA32(ldsfrag(p + 3072), p1, o); };
    auto splat = [&](float v) { f32x16 r;
#pragma unroll
        for (int i = 0; i < 16; ++i) r[i] = v;
        return r; };
    LAS const char* own = lds + LDS_OWN + wc * 32768 + lane * 16;
    LAS const char* prv = lds + LDS_PREV + wc * 32768 + lane * 16;
    {
        for (int i = 0; i < wr; ++i) ma = smax(qk(own + i * 4096, qf[0], zero16), ma);
        for (int i = 0; i < 7 - wr; ++i) mb = smax(qk(own + i * 4096, qf[1], zero16), mb);
        ma = xmax(ma); mb = xmax(mb);
        const f32x16 na = splat(-ma), nb = splat(-mb);
        for (int i = 0; i < wr; ++i) expv(qk(own + i * 4096, qf[0], na), own + i * 4096, oa, la);
        for (int i = 0; i < 7 - wr; ++i) expv(qk(own + i * 4096, qf[1], nb), own + i * 4096, ob, lb);
    }
    STAMP(4);
    waitv<0>(); BAR();
    const u64 tag = ew + TAGK;
    if (wid == 0) {
        if (lane == 32 && rb < 7) __hip_atomic_store((GAS u64*)(a.ws + WS_FKV + (size_t)me * 128), tag, __ATOMIC_RELAXED, __HIP_MEMORY_SCOPE_AGENT);
        if (rb > 0) {
            const unsigned wlo = __builtin_amdgcn_readlane((unsigned)tag, 33), whi = __builtin_amdgcn_readlane((unsigned)(tag >> 32), 33);
            GAS u64* f = (GAS u64*)(a.ws + WS_FKV + (size_t)(me - 16) * 128);
            for (int spin = 0; spin < SPIN_CAP; ++spin) {
                const u64 v = __hip_atomic_load(f, __ATOMIC_RELAXED, __HIP_MEMORY_SCOPE_AGENT);
                if (rfl((unsigned)v) == wlo && rfl((unsigned)(v >> 32)) == whi) break;
                __builtin_amdgcn_s_sleep(1);
            }
            if (lane == 33) __hip_atomic_store((GAS u64*)(a.ws + WS_EKV + (size_t)(me - 16) * 128), tag, __ATOMIC_RELAXED, __HIP_MEMORY_SCOPE_AGENT);
        }
    }
    if (rb > 0) {
        BAR();
        STAMP(5);
        u32x4 ld[8];
#pragma unroll
        for (int i = 0; i < 8; ++i) ld[i] = __builtin_amdgcn_raw_buffer_load_b128(srdW, (unsigned)WS_KV + (me - 16) * 65536 + wid * 8192 + i * 1024 + lane * 16, 0, 16);
#pragma unroll
        for (int i = 0; i < 8; ++i) *(LAS u32x4*)(lds + LDS_PREV + wid * 8192 + i * 1024 + lane * 16) = ld[i];
        WAITL0(); BAR();
    }
    STAMP(6);
    {
        const int r32 = lane & 31, hh = lane >> 5;
        auto diag = [&](int blk, const bf16x8 (&q)[2], const f32x16& c) {
            f32x16 s = qk(own + blk * 4096, q, c), t;
            if (rb > 0) t = qk(prv + blk * 4096, q, c); else t = splat(-INFINITY);
#pragma unroll
            for (int r = 0; r < 16; ++r) s[r] = ((r & 3) + 8 * (r >> 2) + 4 * hh <= r32) ? s[r] : t[r];
            return s; };
        float m2a = smax(diag(wr, qf[0], zero16), ma), m2b = smax(diag(7 - wr, qf[1], zero16), mb);
        if (rb > 0) {
            for (int i = wr + 1; i < 8; ++i) m2a = smax(qk(prv + i * 4096, qf[0], zero16), m2a);
            for (int i = 8 - wr; i < 8; ++i) m2b = smax(qk(prv + i * 4096, qf[1], zero16), m2b);
        }
        m2a = xmax(m2a); m2b = xmax(m2b);
        const float fa = __builtin_amdgcn_exp2f(ma - m2a), fb = __builtin_amdgcn_exp2f(mb - m2b);
        oa *= fa; ob *= fb; la *= fa; lb *= fb;
        const f32x16 na = splat(-m2a), nb = splat(-m2b);
        u32x4 mk0, mk1;
#pragma unroll
        for (int i = 0; i < 4; ++i) {
            const int k0 = 2 * i, k1 = 8 + 2 * i;
            mk0[i] = (((k0 & 3) + 8 * (k0 >> 2) + 4 * hh <= r32) ? 0xffffu : 0u) | ((((k0 + 1) & 3) + 8 * ((k0 + 1) >> 2) + 4 * hh <= r32) ? 0xffff0000u : 0u);
            mk1[i] = (((k1 & 3) + 8 * (k1 >> 2) + 4 * hh <= r32) ? 0xffffu : 0u) | ((((k1 + 1) & 3) + 8 * ((k1 + 1) >> 2) + 4 * hh <= r32) ? 0xffff0000u : 0u);
        }
        auto diagpv = [&](int blk, const bf16x8 (&q)[2], const f32x16& c, f32x16& o, f32x2& l) {
            f32x16 s = diag(blk, q, c);
#pragma unroll
            for (int r = 0; r < 16; ++r) s[r] = __builtin_amdgcn_exp2f(s[r]);
#pragma unroll
            for (int r = 0; r < 16; r += 2) l += (f32x2){s[r], s[r + 1]};
            const u32x4 p0 = __builtin_bit_cast(u32x4, pack8(s[0], s[1], s[2], s[3], s[4], s[5], s[6], s[7])), p1 = __builtin_bit_cast(u32x4, pack8(s[8], s[9], s[10], s[11], s[12], s[13], s[14], s[15]));
            o = MFMA32(ldsfrag(own + blk * 4096 + 2048), __builtin_bit_cast(bf16x8, p0 & mk0), o);
            o = MFMA32(ldsfrag(own + blk * 4096 + 3072), __builtin_bit_cast(bf16x8, p1 & mk1), o);
            if (rb > 0) {
                o = MFMA32(ldsfrag(prv + blk * 4096 + 2048), __builtin_bit_cast(bf16x8, p0 & ~mk0), o);
                o = MFMA32(ldsfrag(prv + blk * 4096 + 3072), __builtin_bit_cast(bf16x8, p1 & ~mk1), o);
            } };
        diagpv(wr, qf[0], na, oa, la); diagpv(7 - wr, qf[1], nb, ob, lb);
        if (rb > 0) {
            for (int i = wr + 1; i < 8; ++i) expv(qk(prv + i * 4096, qf[0], na), prv + i * 4096, oa, la);
            for (int i = 8 - wr; i < 8; ++i) expv(qk(prv + i * 4096, qf[1], nb), prv + i * 4096, ob, lb);
        }
        auto fin = [&](const f32x16& o, f32x2 l, int gt) {
            float ls = l[0] + l[1];
            const auto r = __builtin_amdgcn_permlane32_swap(__float_as_uint(ls), __float_as_uint(ls), false, false);
            const float inv = __builtin_amdgcn_rcpf(__uint_as_float(r[0]) + __uint_as_float(r[1]));
            const unsigned base = (unsigned)WS_O + (((br * 128 + rb * 16 + gt * 2 + (r32 >> 4)) * 32) + hp * 2 + wc) * 1024 + hh * 256 + (r32 & 15) * 16;
#pragma unroll
            for (int e = 0; e < 2; ++e) {
                const u32x4 v = {cvtpk(o[8 * e] * inv, o[8 * e + 1] * inv), cvtpk(o[8 * e + 2] * inv, o[8 * e + 3] * inv), cvtpk(o[8 * e + 4] * inv, o[8 * e + 5] * inv), cvtpk(o[8 * e + 6] * inv, o[8 * e + 7] * inv)};
                __builtin_amdgcn_raw_buffer_store_b128(v, srdW, base + e * 512, 0, 16);
            } };
        fin(oa, la, wr); fin(ob, lb, 7 - wr);
    }
    STAMP(7);
    WAITL0(); BAR();
#pragma unroll
    for (int s = 2; s < 10; ++s) wo_step(s, s - 2);
    waitv<16>(); BAR();
    if (wid == 0) {
        if (lane < 16) __hip_atomic_store((GAS u64*)(a.ws + WS_FO + (size_t)(grp + lane) * 128 + hp * 8), tag, __ATOMIC_RELAXED, __HIP_MEMORY_SCOPE_AGENT);
        GAS u64* f = (GAS u64*)(a.ws + WS_FO + (size_t)me * 128 + (lane & 15) * 8);
        const bool mine = lane >= 16 && lane < 32;
        for (int spin = 0; spin < SPIN_CAP; ++spin) {
            const u64 v = __hip_atomic_load(f, __ATOMIC_RELAXED, __HIP_MEMORY_SCOPE_AGENT);
            if (__all(!mine || v == tag)) break;
            __builtin_amdgcn_s_sleep(1);
        }
        if (mine) __hip_atomic_store((GAS u64*)(a.ws + WS_EO + (size_t)me * 128 + (lane - 16) * 8), tag, __ATOMIC_RELAXED, __HIP_MEMORY_SCOPE_AGENT);
    }
    waitv<0>(); BAR();
    STAMP(8);
    {
        bf16x8 of[16][2];
        const unsigned obase = (unsigned)WS_O + ((br * 128 + rb * 16 + th * 8 + w4 * 2) * 32 + half * 16) * 1024 + lane * 16;
#pragma unroll
        for (int s = 0; s < 16; ++s)
#pragma unroll
            for (int t = 0; t < 2; ++t) of[s][t] = __builtin_bit_cast(bf16x8, __builtin_amdgcn_raw_buffer_load_b128(srdW, obase + t * 32768 + s * 1024, 0, 16));
        f32x4 c2[2][8];
#pragma unroll
        for (int t = 0; t < 2; ++t)
#pragma unroll
            for (int n = 0; n < 8; ++n) c2[t][n] = (f32x4){0.f, 0.f, 0.f, 0.f};
        unsigned woff[8];
        { const int row = 16 * (g4 >> 1) + 4 * (g4 & 1) + q4;
#pragma unroll
          for (int n = 0; n < 8; ++n) woff[n] = half * 8192 + row * 256 + ((n ^ (row & 7)) << 5) + p4 * 8; }
        auto step = [&](int s) {
            const int slot = (s + 8) % 10;
#pragma unroll
            for (int n = 0; n < 8; ++n) {
                LAS const char* p = lds + woff[n] + slot * 16384;
                const s16x4 lo = trread(p), hi = trread(p + 2048);
                const bf16x8 wf = __builtin_shufflevector(lo, hi, 0, 1, 2, 3, 4, 5, 6, 7);
                c2[0][n] = MFMA16(wf, of[s][0], c2[0][n]); c2[1][n] = MFMA16(wf, of[s][1], c2[1][n]);
            } };
#pragma unroll
        for (int s = 0; s < 6; ++s) step(s);
        WAITL0(); BAR();
#pragma unroll
        for (int s = 10; s < 16; ++s) wo_step(s, (s + 8) % 10);
#pragma unroll
        for (int s = 6; s < 10; ++s) step(s);
        waitv<0>(); BAR();
#pragma unroll
        for (int s = 10; s < 16; ++s) step(s);
        STAMP(9);
        WAITL0(); BAR();
#pragma unroll
        for (int t = 0; t < 2; ++t)
#pragma unroll
            for (int n = 0; n < 8; ++n) *(LAS f32x4*)(lds + half * 65536 + (w4 * 32 + t * 16 + r16) * 512 + (((n * 4 + g4) ^ (r16 & 7)) << 4)) = c2[t][n];
        WAITL0(); BAR();
        GAS u16* outp = (GAS u16*)a.out + (size_t)(rb * 256 + th * 128) * 2048 + br * 1024 + ct * 128;
#pragma unroll
        for (int i = 0; i < 4; ++i) {
            const int row = wid * 16 + i * 4 + g4;
            LAS const char* p = lds + row * 512;
            const f32x4 x0 = *(LAS const f32x4*)(p + (((2 * r16) ^ (row & 7)) << 4)) + *(LAS const f32x4*)(p + 65536 + (((2 * r16) ^ (row & 7)) << 4));
            const f32x4 x1 = *(LAS const f32x4*)(p + (((2 * r16 + 1) ^ (row & 7)) << 4)) + *(LAS const f32x4*)(p + 65536 + (((2 * r16 + 1) ^ (row & 7)) << 4));
            const u32x4 v = {cvtpk(x0[0], x0[1]), cvtpk(x0[2], x0[3]), cvtpk(x1[0], x1[1]), cvtpk(x1[2], x1[3])};
            *(GAS u32x4*)(outp + (size_t)row * 2048 + r16 * 8) = v;
        }
    }
    STAMP(10);
}

extern "C" void kernel_launch(void* const* d_in, const int* in_sizes, int n_in, void* d_out, int out_size, void* d_ws, size_t ws_size, hipStream_t stream) {
    Args a;
    a.x[0] = (const u16*)d_in[0]; a.w[0] = (const u16*)d_in[1]; a.wo[0] = (const u16*)d_in[2];
    a.x[1] = (const u16*)d_in[3]; a.w[1] = (const u16*)d_in[4]; a.wo[1] = (const u16*)d_in[5];
    a.out = (u16*)d_out; a.ws = (char*)d_ws;
    fused_kernel<<<256, 512, 0, stream>>>(a);
}
```

```cpp
#include <hip/hip_runtime.h>
#include <hip/hip_bf16.h>
#include <type_traits>
typedef short bf16x8 __attribute__((ext_vector_type(8)));
typedef short s16x4 __attribute__((ext_vector_type(4)));
typedef float f32x16 __attribute__((ext_vector_type(16)));
typedef float f32x4 __attribute__((ext_vector_type(4)));
typedef float f32x2 __attribute__((ext_vector_type(2)));
typedef __bf16 bf16x2 __attribute__((ext_vector_type(2)));
typedef unsigned u32x4 __attribute__((ext_vector_type(4)));
typedef unsigned short u16;
typedef unsigned long long u64;
#define LAS __attribute__((address_space(3)))
#define GAS __attribute__((address_space(1)))
#define DI __device__ __forceinline__
#define MFMA16(a, b, c) __builtin_amdgcn_mfma_f32_16x16x32_bf16((a), (b), (c), 0, 0, 0)
#define MFMA32(a, b, c) __builtin_amdgcn_mfma_f32_32x32x16_bf16((a), (b), (c), 0, 0, 0)
#define SBAR() __builtin_amdgcn_sched_barrier(0)
#define BAR() do { SBAR(); asm volatile("s_barrier" ::: "memory"); SBAR(); } while (0)
#define PIN(x) asm volatile("" : "+v"(x))
#define WAITL0() __builtin_amdgcn_s_waitcnt(0xC07F)
template <int N> DI void waitv() { asm volatile("s_waitcnt vmcnt(%0)" :: "i"(N) : "memory"); }
DI unsigned rfl(unsigned x) { return __builtin_amdgcn_readfirstlane(x); }
DI int lane_id() { int l; asm volatile("v_mbcnt_lo_u32_b32 %0, -1, 0\n\tv_mbcnt_hi_u32_b32 %0, -1, %0" : "=v"(l)); return l; }
DI unsigned cvtpk(float lo, float hi) { f32x2 v = {lo, hi}; bf16x2 b = __builtin_convertvector(v, bf16x2); return __builtin_bit_cast(unsigned, b); }
DI void dma16(unsigned m0v, unsigned voff, __amdgpu_buffer_rsrc_t srd, unsigned soff) {
    asm volatile("s_mov_b32 m0, %0\n\ts_nop 0\n\tbuffer_load_dwordx4 %1, %2, %3 offen lds" :: "s"(m0v), "v"(voff), "s"(srd), "s"(soff) : "memory");
}
DI s16x4 trread(LAS const char* p) { return __builtin_amdgcn_ds_read_tr16_b64_v4i16((LAS s16x4*)p); }
DI bf16x8 ldsfrag(LAS const char* p) { return *(LAS const bf16x8*)p; }
DI float max3f(float a, float b, float c) { return __builtin_fmaxf(__builtin_fmaxf(a, b), c); }
DI float fadd_s(float a, float b) { return a + b; }
DI float fsub_s(float a, float b) { return a - b; }
DI float fmul_s(float a, float b) { return a * b; }
DI bf16x8 pack8(float a, float b, float c, float d, float e, float f, float g, float h) { u32x4 v = {cvtpk(a, b), cvtpk(c, d), cvtpk(e, f), cvtpk(g, h)}; return __builtin_bit_cast(bf16x8, v); }

constexpr int LDS_A0 = 0, LDS_A1 = 32768, LDS_B = 81920, A_SLOT = 16384, B_SLOT = 24576;
constexpr int LDS_TOTAL = 163840;
constexpr size_t WS_FKV = 0, WS_EKV = 32768, WS_FO = 65536, WS_EO = 98304, WS_KV = 1 << 20, WS_O = 17 << 20, WS_END = 25 << 20, WS_ST = 32 << 20;
constexpr u64 TAGK = 0x9E3779B97F4A7C15ull;
constexpr int SPIN_CAP = 400000;
constexpr float QSCALE = 0.17677669529663687f * 1.4426950408889634f;

#define PUBJ 3
#define POLJ 1
#define OW 5
#define LSAFE 1.152921504606847e18f
static_assert(OW >= 3 && OW <= 7, "the vmcnt(4) in phase 3 counts on o loads in steps 8 and 9");
struct Args { const u16* x[2]; const u16* w[2]; const u16* wo[2]; u16* out; char* ws; };

#define STAMP(i) do {} while (0)

__global__ __launch_bounds__(512, 2) void fused_kernel(Args a) {
    __shared__ __attribute__((aligned(16))) char smem[LDS_TOTAL];
    LAS char* lds = (LAS char*)smem;
    const unsigned ldsb = (unsigned)(size_t)smem;
    const int tid = threadIdx.x, lane = tid & 63;
    const int wid = rfl(tid >> 6), half = wid >> 2, w4 = wid & 3;
    const int bid = blockIdx.x, g = bid & 7, j = bid >> 3;
    const int br = g >> 2, rb = ((g >> 1) & 1) * 4 + (j & 3), hp = (g & 1) * 8 + (j >> 2);
    const int me = (br * 8 + rb) * 16 + hp, grp = me & ~15;
    STAMP(0);
    u64 ew = __hip_atomic_load((GAS u64*)(a.ws + WS_EKV + (size_t)(lane == 0 || rb == 0 ? me : me - 16) * 128), __ATOMIC_RELAXED, __HIP_MEMORY_SCOPE_AGENT);
    const __amdgpu_buffer_rsrc_t srdA = __builtin_amdgcn_make_buffer_rsrc((void*)(a.x[br] + (size_t)rb * 256 * 1024), (short)0, 256 * 2048, 0x00020000);
    const __amdgpu_buffer_rsrc_t srdB = __builtin_amdgcn_make_buffer_rsrc((void*)(a.w[br]), (short)0, 1024 * 6144, 0x00020000);
    const int wr = 2 * half + (w4 >> 1), wc = w4 & 1;
    const int r16 = lane & 15, g4 = lane >> 4, q4 = (lane & 15) >> 2, p4 = lane & 3;
    f32x4 acc[4][6];
    {
        const unsigned voffE = (lane >> 3) * 2048 + (((lane & 7) ^ (lane >> 4)) << 4);
        const unsigned voffO = (8 + (lane >> 3)) * 2048 + (((lane & 7) ^ (4 + (lane >> 4))) << 4);
        const unsigned voffB0 = (lane >> 3) * 6144 + (((lane & 7) ^ (((lane >> 4) & 1) << 1)) << 4);
        const unsigned voffB1 = (lane >> 3) * 6144 + (((lane & 7) ^ ((((lane >> 4) & 1) << 1) | 4)) << 4);
        const int gt0 = (w4 & 1) ? 7 - (w4 >> 1) : (w4 >> 1);
        const int gt1 = (w4 & 1) ? 5 - (w4 >> 1) : 2 + (w4 >> 1);
        const unsigned sA0 = gt0 * 32 * 2048, sA1 = gt1 * 32 * 2048;
        const unsigned sB = hp * 128 + (2 * w4) * 8 * 6144;
        unsigned aoff[2], boff[2];
#pragma unroll
        for (int s = 0; s < 2; ++s) aoff[s] = (half ? LDS_A1 : LDS_A0) + ((w4 >> 1) * 64 + r16) * 128 + (((4 * s + g4) ^ ((r16 >> 1) & 7)) << 4);
#pragma unroll
        for (int c = 0; c < 2; ++c) boff[c] = LDS_B + (8 * g4 + q4) * 128 + (((wc * 2 + c) ^ ((q4 >> 1) | ((g4 & 1) << 1))) << 5) + p4 * 8;
#pragma unroll
        for (int i = 0; i < 4; ++i)
#pragma unroll
            for (int s = 0; s < 6; ++s) acc[i][s] = (f32x4){0.f, 0.f, 0.f, 0.f};
        bf16x8 af[4][2], bfr[6][2];
        auto run = [&](auto HC) {
            constexpr int H = decltype(HC)::value;
            auto piece = [&](int kt, int s2, int s3i, int i) {
                const unsigned soB = sB + kt * (64 * 6144), dB = ldsb + LDS_B + s3i * B_SLOT + w4 * 2048;
                const unsigned soA0 = sA0 + kt * 128, dA0 = ldsb + LDS_A0 + s2 * A_SLOT + w4 * 4096;
                const unsigned soA1 = sA1 + kt * 128, dA1 = ldsb + LDS_A1 + s3i * A_SLOT + w4 * 4096;
                if (H == 0) {
                    if (i == 0) dma16(dA1 + 3 * 1024, voffO, srdA, soA1 + 32768);
                    else { const int k = i - 1; dma16(dB + (k >> 1) * 8192 + (k & 1) * 1024, (k & 1) ? voffB1 : voffB0, srdB, soB + (k >> 1) * 2048 + (k & 1) * 8 * 6144); }
                } else dma16((i < 4 ? dA0 : dA1) + (i & 3) * 1024, (i & 1) ? voffO : voffE, srdA, (i < 4 ? soA0 : soA1) + ((i >> 1) & 1) * 32768);
            };
#pragma unroll
            for (int i = 0; i < 7; ++i) piece(0, 0, 0, i);
#pragma unroll
            for (int i = 0; i < 7; ++i) piece(1, 1, 1, i);
#pragma unroll
            for (int i = 0; i < 4; ++i) asm volatile("" : "+v"(acc[i][0]), "+v"(acc[i][1]), "+v"(acc[i][2]), "+v"(acc[i][3]), "+v"(acc[i][4]), "+v"(acc[i][5]));
            if (H == 0) waitv<7>(); else waitv<10>();
            BAR();
            if (H == 1) { waitv<7>(); BAR(); }
            STAMP(1);
            int s3 = 0;
            auto Lseg = [&](int t, bool issue) {
                const unsigned aslot = H ? s3 * A_SLOT : (t & 1) * A_SLOT;
                const unsigned bslot = s3 * B_SLOT;
                const int s3n = (s3 == 0) ? 2 : s3 - 1;
                auto rdA = [&](int i) { LAS const char* pa = lds + aoff[i >> 1] + aslot + (i & 1) * 4096; af[(i & 1) * 2][i >> 1] = ldsfrag(pa); af[(i & 1) * 2 + 1][i >> 1] = ldsfrag(pa + 2048); };
                auto rdB = [&](int i) { const int s = i / 6, seg = (i % 6) >> 1, c = i & 1; LAS const char* pb = lds + boff[c] + bslot;
                    s16x4 lo = trread(pb + seg * 8192 + s * 4096), hi = trread(pb + seg * 8192 + s * 4096 + 512);
                    bfr[seg * 2 + c][s] = __builtin_shufflevector(lo, hi, 0, 1, 2, 3, 4, 5, 6, 7); };
#pragma unroll
                for (int i = 0; i < 16; ++i) {
                    if (issue && (i & 1) == 0 && i / 2 < 7) piece(t + 2, t & 1, s3n, i / 2);
                    if (i < 4) rdA(i); else rdB(i - 4);
                }
                s3 = (s3 == 2) ? 0 : s3 + 1;
            };
            auto Mseg = [&]() {
                __builtin_amdgcn_s_setprio(1);
#pragma unroll
                for (int s = 0; s < 2; ++s)
#pragma unroll
                    for (int m = 0; m < 4; ++m)
#pragma unroll
                        for (int n = 0; n < 6; ++n) acc[m][n] = n < 4 ? MFMA16(bfr[n][s], af[m][s], acc[m][n]) : MFMA16(af[m][s], bfr[n][s], acc[m][n]);
                __builtin_amdgcn_s_setprio(0);
            };
            for (int t = 0; t < 14; ++t) {
                Lseg(t, true);
                if (H == 1) waitv<10>();
                WAITL0(); BAR();
                Mseg();
                waitv<7>();
                BAR();
            }
            Lseg(14, false); if (H == 1) waitv<3>(); WAITL0(); BAR(); Mseg(); waitv<0>(); BAR();
            Lseg(15, false); WAITL0(); BAR(); Mseg(); if (H == 0) BAR();
        };
        if (half == 0) run(std::integral_constant<int, 0>{}); else run(std::integral_constant<int, 1>{});
    }
    STAMP(2);
    const u64 tagp = (((u64)__builtin_amdgcn_readlane((unsigned)(ew >> 32), 0) << 32) | __builtin_amdgcn_readlane((unsigned)ew, 0)) + TAGK;
    const u64 tagc = (((u64)__builtin_amdgcn_readlane((unsigned)(ew >> 32), 1) << 32) | __builtin_amdgcn_readlane((unsigned)ew, 1)) + TAGK;
    const __amdgpu_buffer_rsrc_t srdW = __builtin_amdgcn_make_buffer_rsrc((void*)a.ws, (short)0, (int)WS_END, 0x00020000);
    const __amdgpu_buffer_rsrc_t srdWo = __builtin_amdgcn_make_buffer_rsrc((void*)(a.wo[br]), (short)0, 1024 * 2048, 0x00020000);
    const int ct = hp & 7, th = hp >> 3;
    const unsigned sW = ct * 256 + (half * 512 + w4 * 8) * 2048;
    auto wo_steps = [&](int s0, int s1) {
        const int ln = lane_id();
        const unsigned voffW0 = (ln >> 4) * 2048 + (((ln & 15) ^ ((ln >> 4) << 1)) << 4), voffW1 = (4 + (ln >> 4)) * 2048 + (((ln & 15) ^ ((4 + (ln >> 4)) << 1)) << 4);
#pragma unroll
        for (int s = s0; s < s1; ++s) { const unsigned d = ldsb + ((s + 8) % 10) * 16384 + wid * 2048; dma16(d, voffW0, srdWo, sW + s * 65536); dma16(d + 1024, voffW1, srdWo, sW + s * 65536); } };
    bf16x8 qf[2][2];
    {
        const unsigned kpos = (g4 >> 1) * 1024 + (g4 & 1) * 512 + r16 * 16;
        const unsigned vpos = 2048 + ((g4 & 1) * 32 + (g4 >> 1) * 16 + r16) * 16;
        const unsigned gkv = (unsigned)WS_KV + me * 65536 + wc * 32768;
        u32x4 kv[2][2], vv[2][2];
#pragma unroll
        for (int mi = 0; mi < 2; ++mi)
#pragma unroll
            for (int s = 0; s < 2; ++s) {
                const f32x4 k0 = acc[2 * mi + s][2], k1 = acc[2 * mi + s][3], v0 = acc[2 * mi + s][4], v1 = acc[2 * mi + s][5];
                kv[mi][s] = (u32x4){cvtpk(k0[0], k0[1]), cvtpk(k0[2], k0[3]), cvtpk(k1[0], k1[1]), cvtpk(k1[2], k1[3])};
                const auto ra = __builtin_amdgcn_permlane32_swap(cvtpk(v0[0], v0[1]), cvtpk(v1[0], v1[1]), false, false);
                const auto rbb = __builtin_amdgcn_permlane32_swap(cvtpk(v0[2], v0[3]), cvtpk(v1[2], v1[3]), false, false);
                vv[mi][s] = (u32x4){ra[0], rbb[0], ra[1], rbb[1]};
                const unsigned blk = wc * 65536 + 32768 + (mi ? 7 - wr : wr) * 4096;
                *(LAS u32x4*)(lds + blk + kpos + s * 256) = kv[mi][s];
                *(LAS u32x4*)(lds + blk + vpos + s * 1024) = vv[mi][s];
            }
        if (rb < 7) {
#pragma unroll
            for (int mi = 0; mi < 2; ++mi)
#pragma unroll
                for (int s = 0; s < 2; ++s) { const unsigned blk = (mi ? 7 - wr : wr) * 4096;
                    __builtin_amdgcn_raw_buffer_store_b128(kv[mi][s], srdW, gkv + blk + kpos + s * 256, 0, 16);
                    __builtin_amdgcn_raw_buffer_store_b128(vv[mi][s], srdW, gkv + blk + vpos + s * 1024, 0, 16); }
        }
#pragma unroll
        for (int mi = 0; mi < 2; ++mi) {
            u32x4 x[2], y0, y1;
#pragma unroll
            for (int s = 0; s < 2; ++s) { const f32x4 q0 = acc[2 * mi + s][0] * QSCALE, q1 = acc[2 * mi + s][1] * QSCALE;
                x[s] = (u32x4){cvtpk(q0[0], q0[1]), cvtpk(q0[2], q0[3]), cvtpk(q1[0], q1[1]), cvtpk(q1[2], q1[3])}; }
#pragma unroll
            for (int i = 0; i < 4; ++i) { const auto u = __builtin_amdgcn_permlane16_swap(x[0][i], x[1][i], false, false);
                const auto v = __builtin_amdgcn_permlane32_swap(u[0], u[1], false, false); y0[i] = v[0]; y1[i] = v[1]; }
            qf[mi][0] = __builtin_bit_cast(bf16x8, y0); qf[mi][1] = __builtin_bit_cast(bf16x8, y1);
        }
    }
    f32x16 o[2], nm[2]; f32x4 l[2];
    const int r32 = lane & 31, hh = lane >> 5;
    LAS const char* kvb = lds + wc * 65536 + lane * 16;
    bf16x8 ones;
    { const short v = ((lane ^ (lane >> 4)) & 1) ? (short)0 : (short)0x3f80;
#pragma unroll
      for (int i = 0; i < 8; ++i) ones[i] = v; }
    auto jmax = [&](const f32x16& s, float m) {
#pragma unroll
        for (int r = 0; r < 16; r += 2) m = max3f(m, s[r], s[r + 1]);
        return m; };
    auto xmax = [&](float m) { const auto r = __builtin_amdgcn_permlane32_swap(__float_as_uint(m), __float_as_uint(m), false, false); return __builtin_fmaxf(__uint_as_float(r[0]), __uint_as_float(r[1])); };
    auto expo = [&](const f32x16& s, bf16x8& p0, bf16x8& p1) {
        float t[16];
#pragma unroll
        for (int r = 0; r < 16; ++r) t[r] = __builtin_amdgcn_exp2f(s[r]);
        p0 = pack8(t[0], t[1], t[2], t[3], t[4], t[5], t[6], t[7]); p1 = pack8(t[8], t[9], t[10], t[11], t[12], t[13], t[14], t[15]); };
    auto trimask = [&](bool lower) { f32x16 c;
#pragma unroll
        for (int r = 0; r < 16; ++r) c[r] = (((r & 3) + 8 * (r >> 2) + 4 * hh <= r32) == lower) ? 0.f : -INFINITY;
        return c; };
    {
        const f32x16 mk = trimask(true);
        f32x16 zero16, s[2];
#pragma unroll
        for (int r = 0; r < 16; ++r) zero16[r] = 0.f;
#pragma unroll
        for (int d = 0; d < 2; ++d) {
            LAS const char* p = kvb + (8 + (d ? 7 - wr : wr)) * 4096;
            s[d] = MFMA32(ldsfrag(p), qf[d][0], mk); s[d] = MFMA32(ldsfrag(p + 1024), qf[d][1], s[d]);
        }
        wo_steps(0, 2);
        const float m0 = xmax(jmax(s[0], -INFINITY)), m1 = xmax(jmax(s[1], -INFINITY));
#pragma unroll
        for (int d = 0; d < 2; ++d) {
            LAS const char* p = kvb + (8 + (d ? 7 - wr : wr)) * 4096;
            const float m = d ? m1 : m0;
#pragma unroll
            for (int r = 0; r < 16; ++r) { nm[d][r] = -m; s[d][r] = fsub_s(s[d][r], m); }
            bf16x8 p0, p1;
            expo(s[d], p0, p1);
            o[d] = MFMA32(ldsfrag(p + 2048), p0, zero16); l[d] = MFMA16(ones, p0, ((f32x4){0.f, 0.f, 0.f, 0.f}));
            o[d] = MFMA32(ldsfrag(p + 3072), p1, o[d]); l[d] = MFMA16(ones, p1, l[d]);
        }
    }
    waitv<0>(); WAITL0(); BAR();
    if (wid == 0 && lane == 0 && rb < 7) __hip_atomic_store((GAS u64*)(a.ws + WS_FKV + (size_t)me * 128), tagp, __ATOMIC_RELAXED, __HIP_MEMORY_SCOPE_AGENT);
    STAMP(3);
    auto attention = [&](auto WC_) {
        constexpr int WR = decltype(WC_)::value;
        u32x4 pld[8];
        auto jobs = [&](auto PART) {
            constexpr int P2 = decltype(PART)::value;
            constexpr int NJ = P2 ? 9 : 7;
            auto TOF = [](int j) { return P2 ? (j < 7 ? (j < 7 - WR ? 0 : 1) : j - 7) : (j < WR ? 0 : 1); };
            auto BOF = [](int j) { return P2 ? (j < 7 ? (j < 7 - WR ? WR + 1 + j : j + 1) : (j == 7 ? WR : 7 - WR)) : 8 + (j < WR ? j : j - WR); };
            f32x16 S[2], cm; bf16x8 kf[2][2], vf[2][2]; u32x4 p0, p1;
            auto rdk = [&](int j) { kf[j & 1][0] = ldsfrag(kvb + BOF(j) * 4096); kf[j & 1][1] = ldsfrag(kvb + BOF(j) * 4096 + 1024); };
            auto cof = [&](int j) -> const f32x16& { return (P2 && j >= 7) ? cm : nm[TOF(j)]; };
            auto mkc = [&](int j) { const f32x16 mi = trimask(false);
#pragma unroll
                for (int r = 0; r < 16; ++r) cm[r] = fadd_s(mi[r], nm[TOF(j)][r]); };
            rdk(0); rdk(1); vf[0][0] = ldsfrag(kvb + BOF(0) * 4096 + 2048);
            S[0] = MFMA32(kf[0][0], qf[TOF(0)][0], cof(0)); S[0] = MFMA32(kf[0][1], qf[TOF(0)][1], S[0]);
            SBAR();
#pragma unroll
            for (int j = 0; j < NJ; ++j) {
                const int T = TOF(j), TP = j > 0 ? TOF(j - 1) : 0, TN = j + 1 < NJ ? TOF(j + 1) : 0;
                f32x16& s = S[j & 1];
#define EX(i) s[i] = __builtin_amdgcn_exp2f(s[i])
                if (P2 && j + 1 >= 7 && j + 1 < NJ) { mkc(j + 1); PIN(cm); }
                if (j + 1 < NJ) S[(j + 1) & 1] = MFMA32(kf[(j + 1) & 1][0], qf[TN][0], cof(j + 1));
                SBAR();
                vf[j & 1][1] = ldsfrag(kvb + BOF(j) * 4096 + 3072);
                EX(0); EX(1); EX(2); PIN(s); SBAR();
                if (j > 0) o[TP] = MFMA32(vf[(j - 1) & 1][1], __builtin_bit_cast(bf16x8, p1), o[TP]);
                SBAR();
                EX(3); EX(4); EX(5); PIN(s); SBAR();
                if (j > 0) l[TP] = MFMA16(ones, __builtin_bit_cast(bf16x8, p1), l[TP]);
                SBAR();
                EX(6); EX(7);
                p0 = (u32x4){cvtpk(s[0], s[1]), cvtpk(s[2], s[3]), cvtpk(s[4], s[5]), cvtpk(s[6], s[7])}; PIN(p0); SBAR();
                if (j + 1 < NJ) S[(j + 1) & 1] = MFMA32(kf[(j + 1) & 1][1], qf[TN][1], S[(j + 1) & 1]);
                SBAR();
                if (j + 1 < NJ) vf[(j + 1) & 1][0] = ldsfrag(kvb + BOF(j + 1) * 4096 + 2048);
                EX(8); EX(9); EX(10); PIN(s); SBAR();
                o[T] = MFMA32(vf[j & 1][0], __builtin_bit_cast(bf16x8, p0), o[T]);
                SBAR();
                if (j + 2 < NJ) rdk(j + 2);
                EX(11); EX(12); EX(13); PIN(s); SBAR();
                l[T] = MFMA16(ones, __builtin_bit_cast(bf16x8, p0), l[T]);
                SBAR();
                EX(14); EX(15);
                p1 = (u32x4){cvtpk(s[8], s[9]), cvtpk(s[10], s[11]), cvtpk(s[12], s[13]), cvtpk(s[14], s[15])}; PIN(p1); SBAR();
#undef EX
                if (j == NJ - 1) { o[T] = MFMA32(vf[j & 1][1], __builtin_bit_cast(bf16x8, p1), o[T]); l[T] = MFMA16(ones, __builtin_bit_cast(bf16x8, p1), l[T]); }
                if (!P2 && j == POLJ && rb > 0) {
                    if (WR == 0 && wid == 0) {
                        GAS u64* f = (GAS u64*)(a.ws + WS_FKV + (size_t)(me - 16) * 128);
#pragma nounroll
                        for (int spin = 0; spin < SPIN_CAP; ++spin) {
                            const u64 v = __hip_atomic_load(f, __ATOMIC_RELAXED, __HIP_MEMORY_SCOPE_AGENT);
                            if (rfl((unsigned)v) == (unsigned)tagc && rfl((unsigned)(v >> 32)) == (unsigned)(tagc >> 32)) break;
                            __builtin_amdgcn_s_sleep(1);
                        }
                        if (lane == 0) __hip_atomic_store((GAS u64*)(a.ws + WS_EKV + (size_t)(me - 16) * 128), tagc, __ATOMIC_RELAXED, __HIP_MEMORY_SCOPE_AGENT);
                    }
                    BAR();
                    STAMP(5);
#pragma unroll
                    for (int k = 0; k < 8; ++k) pld[k] = __builtin_amdgcn_raw_buffer_load_b128(srdW, (unsigned)WS_KV + (me - 16) * 65536 + wid * 8192 + k * 1024 + lane * 16, 0, 16);
                    SBAR();
                }
            }
        };
        jobs(std::integral_constant<int, 0>{});
        STAMP(4);
        if (rb > 0) {
#pragma unroll
            for (int i = 0; i < 8; ++i) *(LAS u32x4*)(lds + half * 65536 + w4 * 8192 + i * 1024 + lane * 16) = pld[i];
            WAITL0(); BAR();
            STAMP(6);
            jobs(std::integral_constant<int, 1>{});
        }
    };
#define ATT(W) attention(std::integral_constant<int, W>{})
    if (wr == 0) ATT(0); else if (wr == 1) ATT(1); else if (wr == 2) ATT(2); else ATT(3);
#pragma unroll
    for (int d = 0; d < 2; ++d) {
        const int gt = d ? 7 - wr : wr;
        float lt = (lane & 16) ? l[d][1] : l[d][0];
        if (__builtin_expect(__any(!(lt <= LSAFE)), 0)) {
            const f32x16 mlo = trimask(true), mhi = trimask(false);
            const int b0 = rb > 0 ? gt : 8, b1 = 8 + gt;
            auto sc = [&](int b, float ref) { f32x16 c;
#pragma unroll
                for (int r = 0; r < 16; ++r) c[r] = (b == b1 ? mlo[r] : b == gt ? mhi[r] : 0.f) - ref;
                c = MFMA32(ldsfrag(kvb + b * 4096), qf[d][0], c); return MFMA32(ldsfrag(kvb + b * 4096 + 1024), qf[d][1], c); };
            float m = -INFINITY;
#pragma nounroll
            for (int b = b0; b <= b1; ++b) m = jmax(sc(b, 0.f), m);
            m = xmax(m);
            f32x4 ls = {0.f, 0.f, 0.f, 0.f};
#pragma unroll
            for (int r = 0; r < 16; ++r) o[d][r] = 0.f;
#pragma nounroll
            for (int b = b0; b <= b1; ++b) { bf16x8 p0, p1; expo(sc(b, m), p0, p1);
                o[d] = MFMA32(ldsfrag(kvb + b * 4096 + 2048), p0, o[d]); o[d] = MFMA32(ldsfrag(kvb + b * 4096 + 3072), p1, o[d]);
                ls = MFMA16(ones, p0, ls); ls = MFMA16(ones, p1, ls); }
            lt = (lane & 16) ? ls[1] : ls[0];
        }
        const float inv = __builtin_amdgcn_rcpf(lt);
        const unsigned base = (unsigned)WS_O + (((br * 128 + rb * 16 + gt * 2 + (r32 >> 4)) * 32) + hp * 2 + wc) * 1024 + hh * 256 + (r32 & 15) * 16;
#pragma unroll
        for (int e = 0; e < 2; ++e) {
            const f32x16& x = o[d];
            const u32x4 v = {cvtpk(fmul_s(x[8 * e], inv), fmul_s(x[8 * e + 1], inv)), cvtpk(fmul_s(x[8 * e + 2], inv), fmul_s(x[8 * e + 3], inv)), cvtpk(fmul_s(x[8 * e + 4], inv), fmul_s(x[8 * e + 5], inv)), cvtpk(fmul_s(x[8 * e + 6], inv), fmul_s(x[8 * e + 7], inv))};
            __builtin_amdgcn_raw_buffer_store_b128(v, srdW, base + e * 512, 0, 16);
        }
    }
    STAMP(7);
    {
        const int lane = lane_id(), r16 = lane & 15, g4 = lane >> 4, q4 = (lane & 15) >> 2, p4 = lane & 3;
        GAS char* ep = (GAS char*)a.ws + WS_EO + (lane < 16 ? (size_t)(grp + lane) * 128 + hp * 8 : (size_t)me * 128 + (lane & 15) * 8);
        u64 eo;
        asm volatile("global_load_dwordx2 %0, %1, off sc1" : "=v"(eo) : "v"(ep) : "memory");
        unsigned woff[8];
        { const int row = 16 * (g4 >> 1) + 4 * (g4 & 1) + q4;
#pragma unroll
          for (int n = 0; n < 8; ++n) woff[n] = half * 8192 + row * 256 + ((n ^ (row & 7)) << 5) + p4 * 8; }
        bf16x8 wf[2][8];
        auto wread = [&](int s, int n) { LAS const char* p = lds + woff[n] + ((s + 8) % 10) * 16384;
            const s16x4 lo = trread(p), hi = trread(p + 2048); wf[s & 1][n] = __builtin_shufflevector(lo, hi, 0, 1, 2, 3, 4, 5, 6, 7); };
        WAITL0(); BAR();
        wo_steps(2, 10);
#pragma unroll
        for (int n = 0; n < 8; ++n) { wread(0, n); wread(1, n); }
        waitv<16>(); PIN(eo); WAITL0(); BAR();
        if (wid == 0) {
            const u64 tag = eo + TAGK;
            if (lane < 16) __hip_atomic_store((GAS u64*)(ep - WS_EO + WS_FO), tag, __ATOMIC_RELAXED, __HIP_MEMORY_SCOPE_AGENT);
        }
        wo_steps(10, 12);
        if (wid == 0) {
            const u64 tag = eo + TAGK;
            GAS u64* f = (GAS u64*)(a.ws + WS_FO + (size_t)me * 128 + (lane & 15) * 8);
            const bool mine = lane >= 16 && lane < 32;
#pragma nounroll
            for (int spin = 0; spin < SPIN_CAP; ++spin) {
                const u64 v = __hip_atomic_load(f, __ATOMIC_RELAXED, __HIP_MEMORY_SCOPE_AGENT);
                if (__all(!mine || v == tag)) break;
                __builtin_amdgcn_s_sleep(1);
            }
            if (mine) __hip_atomic_store((GAS u64*)ep, tag, __ATOMIC_RELAXED, __HIP_MEMORY_SCOPE_AGENT);
        }
        waitv<0>(); BAR();
        STAMP(8);
        bf16x8 of[OW][2];
        const unsigned obase = (unsigned)WS_O + ((br * 128 + rb * 16 + th * 8 + w4 * 2) * 32 + half * 16) * 1024 + lane * 16;
        auto oload = [&](int s, int t) { of[s % OW][t] = __builtin_bit_cast(bf16x8, __builtin_amdgcn_raw_buffer_load_b128(srdW, obase + t * 32768 + s * 1024, 0, 16)); };
#pragma unroll
        for (int s = 0; s < OW - 1; ++s) { oload(s, 0); oload(s, 1); SBAR(); }
        f32x4 c2[2][8];
#pragma unroll
        for (int t = 0; t < 2; ++t)
#pragma unroll
            for (int n = 0; n < 8; ++n) c2[t][n] = (f32x4){0.f, 0.f, 0.f, 0.f};
        const unsigned voffW0 = (lane >> 4) * 2048 + (((lane & 15) ^ ((lane >> 4) << 1)) << 4), voffW1 = (4 + (lane >> 4)) * 2048 + (((lane & 15) ^ ((4 + (lane >> 4)) << 1)) << 4);
        auto step = [&](int s) {
#pragma unroll
            for (int n = 0; n < 8; ++n) {
                c2[0][n] = MFMA16(wf[s & 1][n], of[s % OW][0], c2[0][n]); c2[1][n] = MFMA16(wf[s & 1][n], of[s % OW][1], c2[1][n]);
                SBAR();
                if (s + 2 < 16) wread(s + 2, n);
                if (n == 1 && s + OW - 1 < 16) oload(s + OW - 1, 0);
                if (n == 5 && s + OW - 1 < 16) oload(s + OW - 1, 1);
                if (s >= 4 && s < 8 && (n == 3 || n == 7)) dma16(ldsb + ((s + 16) % 10) * 16384 + wid * 2048 + (n == 7) * 1024, n == 7 ? voffW1 : voffW0, srdWo, sW + (s + 8) * 65536);
                SBAR();
            } };
#pragma unroll
        for (int s = 0; s < 4; ++s) { step(s); if (s == 0) STAMP(11); }
        STAMP(12);
        WAITL0(); BAR();
#pragma unroll
        for (int s = 4; s < 10; ++s) step(s);
        STAMP(13);
        waitv<4>(); BAR();
        STAMP(14);
#pragma unroll
        for (int s = 10; s < 16; ++s) step(s);
        STAMP(9);
        BAR();
#pragma unroll
        for (int t = 0; t < 2; ++t)
#pragma unroll
            for (int n = 0; n < 4; ++n) *(LAS f32x4*)(lds + wid * 8192 + (t * 4 + n) * 1024 + lane * 16) = half ? c2[t][n] : c2[t][4 + n];
        WAITL0(); BAR();
        STAMP(15);
        GAS u16* outp = (GAS u16*)a.out + (size_t)(rb * 256 + th * 128 + w4 * 32 + r16) * 2048 + br * 1024 + ct * 128 + half * 64 + (g4 & 1) * 16 + (g4 >> 1) * 8;
#pragma unroll
        for (int t = 0; t < 2; ++t)
#pragma unroll
            for (int n = 0; n < 4; n += 2) {
                const f32x4 x = (half ? c2[t][4 + n] : c2[t][n]) + *(LAS const f32x4*)(lds + (wid ^ 4) * 8192 + (t * 4 + n) * 1024 + lane * 16);
                const f32x4 y = (half ? c2[t][5 + n] : c2[t][n + 1]) + *(LAS const f32x4*)(lds + (wid ^ 4) * 8192 + (t * 4 + n + 1) * 1024 + lane * 16);
                const auto u0 = __builtin_amdgcn_permlane16_swap(cvtpk(x[0], x[1]), cvtpk(y[0], y[1]), false, false);
                const auto u1 = __builtin_amdgcn_permlane16_swap(cvtpk(x[2], x[3]), cvtpk(y[2], y[3]), false, false);
                *(GAS u32x4*)(outp + (size_t)t * 16 * 2048 + n * 16) = (u32x4){u0[0], u1[0], u0[1], u1[1]};
            }
    }
    STAMP(10);
}

extern "C" void kernel_launch(void* const* d_in, const int* in_sizes, int n_in, void* d_out, int out_size, void* d_ws, size_t ws_size, hipStream_t stream) {
    Args a;
    a.x[0] = (const u16*)d_in[0]; a.w[0] = (const u16*)d_in[1]; a.wo[0] = (const u16*)d_in[2];
    a.x[1] = (const u16*)d_in[3]; a.w[1] = (const u16*)d_in[4]; a.wo[1] = (const u16*)d_in[5];
    a.out = (u16*)d_out; a.ws = (char*)d_ws;
    fused_kernel<<<256, 512, 0, stream>>>(a);
}
```

```cpp
#define MA 0
#define MB 0
#include <hip/hip_runtime.h>
#include <hip/hip_bf16.h>
#include <type_traits>
typedef short bf16x8 __attribute__((ext_vector_type(8)));
typedef short s16x4 __attribute__((ext_vector_type(4)));
typedef float f32x16 __attribute__((ext_vector_type(16)));
typedef float f32x4 __attribute__((ext_vector_type(4)));
typedef float f32x2 __attribute__((ext_vector_type(2)));
typedef __bf16 bf16x2 __attribute__((ext_vector_type(2)));
typedef unsigned u32x4 __attribute__((ext_vector_type(4)));
typedef unsigned short u16;
typedef unsigned long long u64;
#define LAS __attribute__((address_space(3)))
#define GAS __attribute__((address_space(1)))
#define DI __device__ __forceinline__
#define MFMA16(a, b, c) __builtin_amdgcn_mfma_f32_16x16x32_bf16((a), (b), (c), 0, 0, 0)
#define MFMA32(a, b, c) __builtin_amdgcn_mfma_f32_32x32x16_bf16((a), (b), (c), 0, 0, 0)
#define SBAR() __builtin_amdgcn_sched_barrier(0)
#define BAR() do { SBAR(); asm volatile("s_barrier" ::: "memory"); SBAR(); } while (0)
#define PIN(x) asm volatile("" : "+v"(x))
#define WAITL0() __builtin_amdgcn_s_waitcnt(0xC07F)
template <int N> DI void waitv() { asm volatile("s_waitcnt vmcnt(%0)" :: "i"(N) : "memory"); }
DI unsigned rfl(unsigned x) { return __builtin_amdgcn_readfirstlane(x); }
DI int lane_id() { int l; asm volatile("v_mbcnt_lo_u32_b32 %0, -1, 0\n\tv_mbcnt_hi_u32_b32 %0, -1, %0" : "=v"(l)); return l; }
DI unsigned cvtpk(float lo, float hi) { f32x2 v = {lo, hi}; bf16x2 b = __builtin_convertvector(v, bf16x2); return __builtin_bit_cast(unsigned, b); }
DI void dma16(unsigned m0v, unsigned voff, __amdgpu_buffer_rsrc_t srd, unsigned soff) {
    asm volatile("s_mov_b32 m0, %0\n\ts_nop 0\n\tbuffer_load_dwordx4 %1, %2, %3 offen lds" :: "s"(m0v), "v"(voff), "s"(srd), "s"(soff) : "memory");
}
DI s16x4 trread(LAS const char* p) { return __builtin_amdgcn_ds_read_tr16_b64_v4i16((LAS s16x4*)p); }
DI bf16x8 ldsfrag(LAS const char* p) { return *(LAS const bf16x8*)p; }
DI float max3f(float a, float b, float c) { return __builtin_fmaxf(__builtin_fmaxf(a, b), c); }
DI float fadd_s(float a, float b) { return a + b; }
DI float fsub_s(float a, float b) { return a - b; }
DI float fmul_s(float a, float b) { return a * b; }
DI bf16x8 pack8(float a, float b, float c, float d, float e, float f, float g, float h) { u32x4 v = {cvtpk(a, b), cvtpk(c, d), cvtpk(e, f), cvtpk(g, h)}; return __builtin_bit_cast(bf16x8, v); }

constexpr int LDS_A0 = 0, LDS_A1 = 32768, LDS_B = 81920, A_SLOT = 16384, B_SLOT = 24576;
constexpr int LDS_TOTAL = 163840;
constexpr size_t WS_FKV = 0, WS_EKV = 32768, WS_FO = 65536, WS_EO = 98304, WS_KV = 1 << 20, WS_O = 17 << 20, WS_END = 25 << 20, WS_ST = 32 << 20;
constexpr u64 TAGK = 0x9E3779B97F4A7C15ull;
constexpr int SPIN_CAP = 400000;
constexpr float QSCALE = 0.17677669529663687f * 1.4426950408889634f;

#define PUBJ 3
#define POLJ 1
#define OW 5
#define PA0 1
#define WPRE 4
#define LSAFE 1.152921504606847e18f
struct Args { const u16* x[2]; const u16* w[2]; const u16* wo[2]; u16* out; char* ws; };

#define STAMP(i) do { if ((i) == MA) mta = __builtin_amdgcn_s_memrealtime(); if ((i) == MB) mtb = __builtin_amdgcn_s_memrealtime(); } while (0)
__global__ __launch_bounds__(512, 2) void fused_kernel(Args a) {
    __shared__ __attribute__((aligned(16))) char smem[LDS_TOTAL];
    LAS char* lds = (LAS char*)smem;
    const unsigned ldsb = (unsigned)(size_t)smem;
    const int tid = threadIdx.x, lane = tid & 63;
    const int wid = rfl(tid >> 6), half = wid >> 2, w4 = wid & 3;
    const int bid = blockIdx.x, g = bid & 7, j = bid >> 3;
    const int br = g >> 2, rb = ((g >> 1) & 1) * 4 + (j & 3), hp = (g & 1) * 8 + (j >> 2);
    const int me = (br * 8 + rb) * 16 + hp, grp = me & ~15;
    u64 mta = 0, mtb = 0;
    STAMP(0);
    u64 ew = __hip_atomic_load((GAS u64*)(a.ws + WS_EKV + (size_t)(lane == 0 || rb == 0 ? me : me - 16) * 128), __ATOMIC_RELAXED, __HIP_MEMORY_SCOPE_AGENT);
    const __amdgpu_buffer_rsrc_t srdA = __builtin_amdgcn_make_buffer_rsrc((void*)(a.x[br] + (size_t)rb * 256 * 1024), (short)0, 256 * 2048, 0x00020000);
    const __amdgpu_buffer_rsrc_t srdB = __builtin_amdgcn_make_buffer_rsrc((void*)(a.w[br]), (short)0, 1024 * 6144, 0x00020000);
    const int wr = 2 * half + (w4 >> 1), wc = w4 & 1;
    const int r16 = lane & 15, g4 = lane >> 4, q4 = (lane & 15) >> 2, p4 = lane & 3;
    f32x4 acc[4][6];
    {
        const unsigned voffE = (lane >> 3) * 2048 + (((lane & 7) ^ (lane >> 4)) << 4);
        const unsigned voffO = (8 + (lane >> 3)) * 2048 + (((lane & 7) ^ (4 + (lane >> 4))) << 4);
        const unsigned voffB0 = (lane >> 3) * 6144 + (((lane & 7) ^ (((lane >> 4) & 1) << 1)) << 4);
        const unsigned voffB1 = (lane >> 3) * 6144 + (((lane & 7) ^ ((((lane >> 4) & 1) << 1) | 4)) << 4);
        const int gt0 = (w4 & 1) ? 7 - (w4 >> 1) : (w4 >> 1);
        const int gt1 = (w4 & 1) ? 5 - (w4 >> 1) : 2 + (w4 >> 1);
        const unsigned sA0 = gt0 * 32 * 2048, sA1 = gt1 * 32 * 2048;
        const unsigned sB = hp * 128 + (2 * w4) * 8 * 6144;
        unsigned aoff[2], boff[2];
#pragma unroll
        for (int s = 0; s < 2; ++s) aoff[s] = (half ? LDS_A1 : LDS_A0) + ((w4 >> 1) * 64 + r16) * 128 + (((4 * s + g4) ^ ((r16 >> 1) & 7)) << 4);
#pragma unroll
        for (int c = 0; c < 2; ++c) boff[c] = LDS_B + (8 * g4 + q4) * 128 + (((wc * 2 + c) ^ ((q4 >> 1) | ((g4 & 1) << 1))) << 5) + p4 * 8;
#pragma unroll
        for (int i = 0; i < 4; ++i)
#pragma unroll
            for (int s = 0; s < 6; ++s) acc[i][s] = (f32x4){0.f, 0.f, 0.f, 0.f};
        bf16x8 af[4][2], bfr[6][2];
        auto run = [&](auto HC) {
            constexpr int H = decltype(HC)::value;
            auto piece = [&](int kt, int s2, int s3i, int i) {
                const unsigned soB = sB + kt * (64 * 6144), dB = ldsb + LDS_B + s3i * B_SLOT + w4 * 2048;
                const unsigned soA0 = sA0 + kt * 128, dA0 = ldsb + LDS_A0 + s2 * A_SLOT + w4 * 4096;
                const unsigned soA1 = sA1 + kt * 128, dA1 = ldsb + LDS_A1 + s3i * A_SLOT + w4 * 4096;
                if (H == 0) {
                    if (i == 0) dma16(dA1 + 3 * 1024, voffO, srdA, soA1 + 32768);
                    else { const int k = i - 1; dma16(dB + (k >> 1) * 8192 + (k & 1) * 1024, (k & 1) ? voffB1 : voffB0, srdB, soB + (k >> 1) * 2048 + (k & 1) * 8 * 6144); }
                } else dma16((i < 4 ? dA0 : dA1) + (i & 3) * 1024, (i & 1) ? voffO : voffE, srdA, (i < 4 ? soA0 : soA1) + ((i >> 1) & 1) * 32768);
            };
#pragma unroll
            for (int i = 0; i < 7; ++i) piece(0, 0, 0, i);
#pragma unroll
            for (int i = 0; i < 7; ++i) piece(1, 1, 1, i);
#pragma unroll
            for (int i = 0; i < 4; ++i) asm volatile("" : "+v"(acc[i][0]), "+v"(acc[i][1]), "+v"(acc[i][2]), "+v"(acc[i][3]), "+v"(acc[i][4]), "+v"(acc[i][5]));
            if (H == 0) waitv<7>(); else waitv<10>();
            BAR();
            if (H == 1) { waitv<7>(); BAR(); }
            STAMP(1);
            int s3 = 0;
            auto Lseg = [&](int t, bool issue) {
                const unsigned aslot = H ? s3 * A_SLOT : (t & 1) * A_SLOT;
                const unsigned bslot = s3 * B_SLOT;
                const int s3n = (s3 == 0) ? 2 : s3 - 1;
                auto rdA = [&](int i) { LAS const char* pa = lds + aoff[i >> 1] + aslot + (i & 1) * 4096; af[(i & 1) * 2][i >> 1] = ldsfrag(pa); af[(i & 1) * 2 + 1][i >> 1] = ldsfrag(pa + 2048); };
                auto rdB = [&](int i) { const int s = i / 6, seg = (i % 6) >> 1, c = i & 1; LAS const char* pb = lds + boff[c] + bslot;
                    s16x4 lo = trread(pb + seg * 8192 + s * 4096), hi = trread(pb + seg * 8192 + s * 4096 + 512);
                    bfr[seg * 2 + c][s] = __builtin_shufflevector(lo, hi, 0, 1, 2, 3, 4, 5, 6, 7); };
#pragma unroll
                for (int i = 0; i < 16; ++i) {
                    if (issue && (i & 1) == 0 && i / 2 < 7) piece(t + 2, t & 1, s3n, i / 2);
                    if (i < 4) rdA(i); else rdB(i - 4);
                }
                s3 = (s3 == 2) ? 0 : s3 + 1;
            };
            auto Mseg = [&]() {
                __builtin_amdgcn_s_setprio(1);
#pragma unroll
                for (int s = 0; s < 2; ++s)
#pragma unroll
                    for (int m = 0; m < 4; ++m)
#pragma unroll
                        for (int n = 0; n < 6; ++n) acc[m][n] = n < 4 ? MFMA16(bfr[n][s], af[m][s], acc[m][n]) : MFMA16(af[m][s], bfr[n][s], acc[m][n]);
                __builtin_amdgcn_s_setprio(0);
            };
            for (int t = 0; t < 14; ++t) {
                Lseg(t, true);
                if (H == 1) waitv<10>();
                WAITL0(); BAR();
                Mseg();
                waitv<7>();
                BAR();
            }
            Lseg(14, false); if (H == 1) waitv<3>(); WAITL0(); BAR(); Mseg(); waitv<0>(); BAR();
            Lseg(15, false); WAITL0(); BAR(); Mseg(); if (H == 0) BAR();
        };
        if (half == 0) run(std::integral_constant<int, 0>{}); else run(std::integral_constant<int, 1>{});
    }
    STAMP(2);
    const u64 tagp = (((u64)__builtin_amdgcn_readlane((unsigned)(ew >> 32), 0) << 32) | __builtin_amdgcn_readlane((unsigned)ew, 0)) + TAGK;
    const u64 tagc = (((u64)__builtin_amdgcn_readlane((unsigned)(ew >> 32), 1) << 32) | __builtin_amdgcn_readlane((unsigned)ew, 1)) + TAGK;
    const __amdgpu_buffer_rsrc_t srdW = __builtin_amdgcn_make_buffer_rsrc((void*)a.ws, (short)0, (int)WS_END, 0x00020000);
    const __amdgpu_buffer_rsrc_t srdWo = __builtin_amdgcn_make_buffer_rsrc((void*)(a.wo[br]), (short)0, 1024 * 2048, 0x00020000);
    const int ct = hp & 7, th = hp >> 3;
    const unsigned sW = ct * 256 + (half * 512 + w4 * 8) * 2048;
    auto wo_steps = [&](int s0, int s1) {
        const int ln = lane_id();
        const unsigned voffW0 = (ln >> 4) * 2048 + (((ln & 15) ^ ((ln >> 4) << 1)) << 4), voffW1 = (4 + (ln >> 4)) * 2048 + (((ln & 15) ^ ((4 + (ln >> 4)) << 1)) << 4);
#pragma unroll
        for (int s = s0; s < s1; ++s) { const unsigned d = ldsb + ((s + 8) % 10) * 16384 + wid * 2048; dma16(d, voffW0, srdWo, sW + s * 65536); dma16(d + 1024, voffW1, srdWo, sW + s * 65536); } };
    bf16x8 qf[2][2];
    {
        const unsigned kpos = (g4 >> 1) * 1024 + (g4 & 1) * 512 + r16 * 16;
        const unsigned vpos = 2048 + ((g4 & 1) * 32 + (g4 >> 1) * 16 + r16) * 16;
        const unsigned gkv = (unsigned)WS_KV + me * 65536 + wc * 32768;
        u32x4 kv[2][2], vv[2][2];
#pragma unroll
        for (int mi = 0; mi < 2; ++mi)
#pragma unroll
            for (int s = 0; s < 2; ++s) {
                const f32x4 k0 = acc[2 * mi + s][2], k1 = acc[2 * mi + s][3], v0 = acc[2 * mi + s][4], v1 = acc[2 * mi + s][5];
                kv[mi][s] = (u32x4){cvtpk(k0[0], k0[1]), cvtpk(k0[2], k0[3]), cvtpk(k1[0], k1[1]), cvtpk(k1[2], k1[3])};
                const auto ra = __builtin_amdgcn_permlane32_swap(cvtpk(v0[0], v0[1]), cvtpk(v1[0], v1[1]), false, false);
                const auto rbb = __builtin_amdgcn_permlane32_swap(cvtpk(v0[2], v0[3]), cvtpk(v1[2], v1[3]), false, false);
                vv[mi][s] = (u32x4){ra[0], rbb[0], ra[1], rbb[1]};
                const unsigned blk = wc * 65536 + 32768 + (mi ? 7 - wr : wr) * 4096;
                *(LAS u32x4*)(lds + blk + kpos + s * 256) = kv[mi][s];
                *(LAS u32x4*)(lds + blk + vpos + s * 1024) = vv[mi][s];
            }
        if (rb < 7) {
#pragma unroll
            for (int mi = 0; mi < 2; ++mi)
#pragma unroll
                for (int s = 0; s < 2; ++s) { const unsigned blk = (mi ? 7 - wr : wr) * 4096;
                    __builtin_amdgcn_raw_buffer_store_b128(kv[mi][s], srdW, gkv + blk + kpos + s * 256, 0, 16);
                    __builtin_amdgcn_raw_buffer_store_b128(vv[mi][s], srdW, gkv + blk + vpos + s * 1024, 0, 16); }
        }
#pragma unroll
        for (int mi = 0; mi < 2; ++mi) {
            u32x4 x[2], y0, y1;
#pragma unroll
            for (int s = 0; s < 2; ++s) { const f32x4 q0 = acc[2 * mi + s][0] * QSCALE, q1 = acc[2 * mi + s][1] * QSCALE;
                x[s] = (u32x4){cvtpk(q0[0], q0[1]), cvtpk(q0[2], q0[3]), cvtpk(q1[0], q1[1]), cvtpk(q1[2], q1[3])}; }
#pragma unroll
            for (int i = 0; i < 4; ++i) { const auto u = __builtin_amdgcn_permlane16_swap(x[0][i], x[1][i], false, false);
                const auto v = __builtin_amdgcn_permlane32_swap(u[0], u[1], false, false); y0[i] = v[0]; y1[i] = v[1]; }
            qf[mi][0] = __builtin_bit_cast(bf16x8, y0); qf[mi][1] = __builtin_bit_cast(bf16x8, y1);
        }
    }
    f32x16 o[2], nm[2]; f32x4 l[2];
    const int r32 = lane & 31, hh = lane >> 5;
    LAS const char* kvb = lds + wc * 65536 + lane * 16;
    bf16x8 ones;
    { const short v = ((lane ^ (lane >> 4)) & 1) ? (short)0 : (short)0x3f80;
#pragma unroll
      for (int i = 0; i < 8; ++i) ones[i] = v; }
    auto jmax = [&](const f32x16& s, float m) {
#pragma unroll
        for (int r = 0; r < 16; r += 2) m = max3f(m, s[r], s[r + 1]);
        return m; };
    auto xmax = [&](float m) { const auto r = __builtin_amdgcn_permlane32_swap(__float_as_uint(m), __float_as_uint(m), false, false); return __builtin_fmaxf(__uint_as_float(r[0]), __uint_as_float(r[1])); };
    auto expo = [&](const f32x16& s, bf16x8& p0, bf16x8& p1) {
        float t[16];
#pragma unroll
        for (int r = 0; r < 16; ++r) t[r] = __builtin_amdgcn_exp2f(s[r]);
        p0 = pack8(t[0], t[1], t[2], t[3], t[4], t[5], t[6], t[7]); p1 = pack8(t[8], t[9], t[10], t[11], t[12], t[13], t[14], t[15]); };
    auto trimask = [&](bool lower) { f32x16 c;
#pragma unroll
        for (int r = 0; r < 16; ++r) c[r] = (((r & 3) + 8 * (r >> 2) + 4 * hh <= r32) == lower) ? 0.f : -INFINITY;
        return c; };
    {
        const f32x16 mk = trimask(true);
        f32x16 zero16, s[2];
#pragma unroll
        for (int r = 0; r < 16; ++r) zero16[r] = 0.f;
#pragma unroll
        for (int d = 0; d < 2; ++d) {
            LAS const char* p = kvb + (8 + (d ? 7 - wr : wr)) * 4096;
            s[d] = MFMA32(ldsfrag(p), qf[d][0], mk); s[d] = MFMA32(ldsfrag(p + 1024), qf[d][1], s[d]);
        }
        wo_steps(0, 2);
        const float m0 = xmax(jmax(s[0], -INFINITY)), m1 = xmax(jmax(s[1], -INFINITY));
#pragma unroll
        for (int d = 0; d < 2; ++d) {
            LAS const char* p = kvb + (8 + (d ? 7 - wr : wr)) * 4096;
            const float m = d ? m1 : m0;
#pragma unroll
            for (int r = 0; r < 16; ++r) { nm[d][r] = -m; s[d][r] = fsub_s(s[d][r], m); }
            bf16x8 p0, p1;
            expo(s[d], p0, p1);
            o[d] = MFMA32(ldsfrag(p + 2048), p0, zero16); l[d] = MFMA16(ones, p0, ((f32x4){0.f, 0.f, 0.f, 0.f}));
            o[d] = MFMA32(ldsfrag(p + 3072), p1, o[d]); l[d] = MFMA16(ones, p1, l[d]);
        }
    }
    waitv<4>(); WAITL0(); BAR();
    if (wid == 0 && lane == 0 && rb < 7) __hip_atomic_store((GAS u64*)(a.ws + WS_FKV + (size_t)me * 128), tagp, __ATOMIC_RELAXED, __HIP_MEMORY_SCOPE_AGENT);
    STAMP(3);
    auto attention = [&](auto WC_) {
        constexpr int WR = decltype(WC_)::value;
        u32x4 pld[8];
        auto jobs = [&](auto PART) {
            constexpr int P2 = decltype(PART)::value;
            constexpr int NJ = P2 ? 9 : 7;
            auto TOF = [](int j) { return P2 ? (j < 7 ? (j < 7 - WR ? 0 : 1) : j - 7) : (j < WR ? 0 : 1); };
            auto BOF = [](int j) { return P2 ? (j < 7 ? (j < 7 - WR ? WR + 1 + j : j + 1) : (j == 7 ? WR : 7 - WR)) : 8 + (j < WR ? j : j - WR); };
            f32x16 S[2], cm; bf16x8 kf[2][2], vf[2][2]; u32x4 p0, p1;
            auto rdk = [&](int j) { kf[j & 1][0] = ldsfrag(kvb + BOF(j) * 4096); kf[j & 1][1] = ldsfrag(kvb + BOF(j) * 4096 + 1024); };
            auto cof = [&](int j) -> const f32x16& { return (P2 && j >= 7) ? cm : nm[TOF(j)]; };
            auto mkc = [&](int j) { const f32x16 mi = trimask(false);
#pragma unroll
                for (int r = 0; r < 16; ++r) cm[r] = fadd_s(mi[r], nm[TOF(j)][r]); };
            rdk(0); rdk(1); vf[0][0] = ldsfrag(kvb + BOF(0) * 4096 + 2048);
            S[0] = MFMA32(kf[0][0], qf[TOF(0)][0], cof(0)); S[0] = MFMA32(kf[0][1], qf[TOF(0)][1], S[0]);
            SBAR();
#pragma unroll
            for (int j = 0; j < NJ; ++j) {
                const int T = TOF(j), TP = j > 0 ? TOF(j - 1) : 0, TN = j + 1 < NJ ? TOF(j + 1) : 0;
                f32x16& s = S[j & 1];
#define EX(i) s[i] = __builtin_amdgcn_exp2f(s[i])
                if (P2 && j + 1 >= 7 && j + 1 < NJ) { mkc(j + 1); PIN(cm); }
                if (j + 1 < NJ) S[(j + 1) & 1] = MFMA32(kf[(j + 1) & 1][0], qf[TN][0], cof(j + 1));
                SBAR();
                vf[j & 1][1] = ldsfrag(kvb + BOF(j) * 4096 + 3072);
                EX(0); EX(1); EX(2); PIN(s); SBAR();
                if (j > 0) o[TP] = MFMA32(vf[(j - 1) & 1][1], __builtin_bit_cast(bf16x8, p1), o[TP]);
                SBAR();
                EX(3); EX(4); EX(5); PIN(s); SBAR();
                if (j > 0) l[TP] = MFMA16(ones, __builtin_bit_cast(bf16x8, p1), l[TP]);
                SBAR();
                EX(6); EX(7);
                p0 = (u32x4){cvtpk(s[0], s[1]), cvtpk(s[2], s[3]), cvtpk(s[4], s[5]), cvtpk(s[6], s[7])}; PIN(p0); SBAR();
                if (j + 1 < NJ) S[(j + 1) & 1] = MFMA32(kf[(j + 1) & 1][1], qf[TN][1], S[(j + 1) & 1]);
                SBAR();
                if (j + 1 < NJ) vf[(j + 1) & 1][0] = ldsfrag(kvb + BOF(j + 1) * 4096 + 2048);
                EX(8); EX(9); EX(10); PIN(s); SBAR();
                o[T] = MFMA32(vf[j & 1][0], __builtin_bit_cast(bf16x8, p0), o[T]);
                SBAR();
                if (j + 2 < NJ) rdk(j + 2);
                EX(11); EX(12); EX(13); PIN(s); SBAR();
                l[T] = MFMA16(ones, __builtin_bit_cast(bf16x8, p0), l[T]);
                SBAR();
                EX(14); EX(15);
                p1 = (u32x4){cvtpk(s[8], s[9]), cvtpk(s[10], s[11]), cvtpk(s[12], s[13]), cvtpk(s[14], s[15])}; PIN(p1); SBAR();
#undef EX
                if (j == NJ - 1) { o[T] = MFMA32(vf[j & 1][1], __builtin_bit_cast(bf16x8, p1), o[T]); l[T] = MFMA16(ones, __builtin_bit_cast(bf16x8, p1), l[T]); }
                if (!P2 && j == POLJ && rb > 0) {
                    if (WR == 0 && wid == 0) {
                        GAS u64* f = (GAS u64*)(a.ws + WS_FKV + (size_t)(me - 16) * 128);
#pragma nounroll
                        for (int spin = 0; spin < SPIN_CAP; ++spin) {
                            const u64 v = __hip_atomic_load(f, __ATOMIC_RELAXED, __HIP_MEMORY_SCOPE_AGENT);
                            if (rfl((unsigned)v) == (unsigned)tagc && rfl((unsigned)(v >> 32)) == (unsigned)(tagc >> 32)) break;
                            __builtin_amdgcn_s_sleep(1);
                        }
                        if (lane == 0) __hip_atomic_store((GAS u64*)(a.ws + WS_EKV + (size_t)(me - 16) * 128), tagc, __ATOMIC_RELAXED, __HIP_MEMORY_SCOPE_AGENT);
                    }
                    BAR();
                    STAMP(5);
#pragma unroll
                    for (int k = 0; k < 8; ++k) pld[k] = __builtin_amdgcn_raw_buffer_load_b128(srdW, (unsigned)WS_KV + (me - 16) * 65536 + wid * 8192 + k * 1024 + lane * 16, 0, 16);
                    SBAR();
                }
            }
        };
        jobs(std::integral_constant<int, 0>{});
        STAMP(4);
        if (rb > 0) {
#pragma unroll
            for (int i = 0; i < 8; ++i) *(LAS u32x4*)(lds + half * 65536 + w4 * 8192 + i * 1024 + lane * 16) = pld[i];
            WAITL0(); BAR();
            STAMP(6);
            jobs(std::integral_constant<int, 1>{});
        }
    };
#define ATT(W) attention(std::integral_constant<int, W>{})
    if (wr == 0) ATT(0); else if (wr == 1) ATT(1); else if (wr == 2) ATT(2); else ATT(3);
#pragma unroll
    for (int d = 0; d < 2; ++d) {
        const int gt = d ? 7 - wr : wr;
        float lt = (lane & 16) ? l[d][1] : l[d][0];
        if (__builtin_expect(__any(!(lt <= LSAFE)), 0)) {
            const f32x16 mlo = trimask(true), mhi = trimask(false);
            const int b0 = rb > 0 ? gt : 8, b1 = 8 + gt;
            auto sc = [&](int b, float ref) { f32x16 c;
#pragma unroll
                for (int r = 0; r < 16; ++r) c[r] = (b == b1 ? mlo[r] : b == gt ? mhi[r] : 0.f) - ref;
                c = MFMA32(ldsfrag(kvb + b * 4096), qf[d][0], c); return MFMA32(ldsfrag(kvb + b * 4096 + 1024), qf[d][1], c); };
            float m = -INFINITY;
#pragma nounroll
            for (int b = b0; b <= b1; ++b) m = jmax(sc(b, 0.f), m);
            m = xmax(m);
            f32x4 ls = {0.f, 0.f, 0.f, 0.f};
#pragma unroll
            for (int r = 0; r < 16; ++r) o[d][r] = 0.f;
#pragma nounroll
            for (int b = b0; b <= b1; ++b) { bf16x8 p0, p1; expo(sc(b, m), p0, p1);
                o[d] = MFMA32(ldsfrag(kvb + b * 4096 + 2048), p0, o[d]); o[d] = MFMA32(ldsfrag(kvb + b * 4096 + 3072), p1, o[d]);
                ls = MFMA16(ones, p0, ls); ls = MFMA16(ones, p1, ls); }
            lt = (lane & 16) ? ls[1] : ls[0];
        }
        const float inv = __builtin_amdgcn_rcpf(lt);
        const unsigned base = (unsigned)WS_O + (((br * 128 + rb * 16 + gt * 2 + (r32 >> 4)) * 32) + hp * 2 + wc) * 1024 + hh * 256 + (r32 & 15) * 16;
#pragma unroll
        for (int e = 0; e < 2; ++e) {
            const f32x16& x = o[d];
            const u32x4 v = {cvtpk(fmul_s(x[8 * e], inv), fmul_s(x[8 * e + 1], inv)), cvtpk(fmul_s(x[8 * e + 2], inv), fmul_s(x[8 * e + 3], inv)), cvtpk(fmul_s(x[8 * e + 4], inv), fmul_s(x[8 * e + 5], inv)), cvtpk(fmul_s(x[8 * e + 6], inv), fmul_s(x[8 * e + 7], inv))};
            __builtin_amdgcn_raw_buffer_store_b128(v, srdW, base + e * 512, 0, 16);
        }
    }
    STAMP(7);
    auto phase3 = [&](auto HC) {
        constexpr int H = decltype(HC)::value;
        const int lane = lane_id(), r16 = lane & 15, g4 = lane >> 4, q4 = (lane & 15) >> 2, p4 = lane & 3;
        GAS char* ep = (GAS char*)a.ws + WS_EO + (lane < 16 ? (size_t)(grp + lane) * 128 + hp * 8 : (size_t)me * 128 + (lane & 15) * 8);
        u64 eo;
        asm volatile("global_load_dwordx2 %0, %1, off sc1" : "=v"(eo) : "v"(ep) : "memory");
        unsigned woff[8];
        { const int row = 16 * (g4 >> 1) + 4 * (g4 & 1) + q4;
#pragma unroll
          for (int n = 0; n < 8; ++n) woff[n] = 131072 + H * 8192 + row * 256 + ((n ^ (row & 7)) << 5) + p4 * 8; }
        bf16x8 wf[2][8];
        auto wread = [&](int s, int n) {
            if (s == 2 || s == 12) { woff[n] -= 131072; PIN(woff[n]); } else if (s == 6 || s == 10) { woff[n] += 65536; PIN(woff[n]); }
            LAS const char* p = lds + woff[n] + (((s + 8) % 10) & 3) * 16384;
            const s16x4 lo = trread(p), hi = trread(p + 2048); wf[s & 1][n] = __builtin_shufflevector(lo, hi, 0, 1, 2, 3, 4, 5, 6, 7); };
        waitv<5>(); WAITL0(); BAR();
        wo_steps(2, 2 + WPRE);
#pragma unroll
        for (int n = 0; n < 8; ++n) { wread(0, n); wread(1, n); }
        waitv<2 * WPRE>(); PIN(eo); WAITL0(); BAR();
        if (H == 0 && wid == 0) {
            const u64 tag = eo + TAGK;
            if (lane < 16) __hip_atomic_store((GAS u64*)(ep - WS_EO + WS_FO), tag, __ATOMIC_RELAXED, __HIP_MEMORY_SCOPE_AGENT);
        }
        wo_steps(2 + WPRE, 12);
        if (H == 0 && wid == 0) {
            const u64 tag = eo + TAGK;
            GAS u64* f = (GAS u64*)(a.ws + WS_FO + (size_t)me * 128 + (lane & 15) * 8);
            const bool mine = lane >= 16 && lane < 32;
#pragma nounroll
            for (int spin = 0; spin < SPIN_CAP; ++spin) {
                const u64 v = __hip_atomic_load(f, __ATOMIC_RELAXED, __HIP_MEMORY_SCOPE_AGENT);
                if (__all(!mine || v == tag)) break;
                __builtin_amdgcn_s_sleep(1);
            }
            if (mine) __hip_atomic_store((GAS u64*)ep, tag, __ATOMIC_RELAXED, __HIP_MEMORY_SCOPE_AGENT);
        }
        waitv<0>(); BAR();
        STAMP(8);
        bf16x8 of[OW][2];
        const unsigned obase = (unsigned)WS_O + ((br * 128 + rb * 16 + th * 8 + w4 * 2) * 32 + H * 16) * 1024 + lane * 16;
        auto oload = [&](int s, int t) { of[s % OW][t] = __builtin_bit_cast(bf16x8, __builtin_amdgcn_raw_buffer_load_b128(srdW, obase + t * 32768 + s * 1024, 0, 16)); };
#pragma unroll
        for (int s = 0; s < OW - 1; ++s) { oload(s, 0); oload(s, 1); SBAR(); }
        f32x4 c2[2][8];
#pragma unroll
        for (int t = 0; t < 2; ++t)
#pragma unroll
            for (int n = 0; n < 8; ++n) c2[t][n] = (f32x4){0.f, 0.f, 0.f, 0.f};
        const unsigned voffW0 = (lane >> 4) * 2048 + (((lane & 15) ^ ((lane >> 4) << 1)) << 4), voffW1 = (4 + (lane >> 4)) * 2048 + (((lane & 15) ^ ((4 + (lane >> 4)) << 1)) << 4);
        {
            auto step = [&](int s) {
#pragma unroll
                for (int n = 0; n < 8; ++n) {
                    c2[0][n] = MFMA16(wf[s & 1][n], of[s % OW][0], c2[0][n]); c2[1][n] = MFMA16(wf[s & 1][n], of[s % OW][1], c2[1][n]);
                    SBAR();
                    if (s + 2 < 16) wread(s + 2, n);
                    if (n == PA0 + 2 * H && s + OW - 1 < 16) oload(s + OW - 1, 0);
                    if (n == PA0 + 4 + 2 * H && s + OW - 1 < 16) oload(s + OW - 1, 1);
                    if (s >= 4 && s < 8 && (n == PA0 + 2 - 2 * H || n == PA0 + 6 - 2 * H)) dma16(ldsb + ((s + 16) % 10) * 16384 + wid * 2048 + (n >= 4) * 1024, n >= 4 ? voffW1 : voffW0, srdWo, sW + (s + 8) * 65536);
                    SBAR();
                } };
#pragma unroll
            for (int s = 0; s < 4; ++s) { step(s); if (s == 0) STAMP(11); }
            STAMP(12);
            WAITL0(); BAR();
#pragma unroll
            for (int s = 4; s < 10; ++s) step(s);
            STAMP(13);
            waitv<4>(); BAR();
            STAMP(14);
#pragma unroll
            for (int s = 10; s < 16; ++s) step(s);
            STAMP(9);
            BAR();
#pragma unroll
            for (int t = 0; t < 2; ++t)
#pragma unroll
                for (int n = 0; n < 4; ++n) *(LAS f32x4*)(lds + wid * 8192 + (t * 4 + n) * 1024 + lane * 16) = c2[t][4 - 4 * H + n];
            WAITL0(); BAR();
            STAMP(15);
            GAS u16* outp = (GAS u16*)a.out + (size_t)(rb * 256 + th * 128 + w4 * 32 + r16) * 2048 + br * 1024 + ct * 128 + H * 64 + (g4 & 1) * 16 + (g4 >> 1) * 8;
#pragma unroll
            for (int t = 0; t < 2; ++t)
#pragma unroll
                for (int n = 0; n < 4; n += 2) {
                    const f32x4 x = c2[t][4 * H + n] + *(LAS const f32x4*)(lds + (wid ^ 4) * 8192 + (t * 4 + n) * 1024 + lane * 16);
                    const f32x4 y = c2[t][4 * H + n + 1] + *(LAS const f32x4*)(lds + (wid ^ 4) * 8192 + (t * 4 + n + 1) * 1024 + lane * 16);
                    const auto u0 = __builtin_amdgcn_permlane16_swap(cvtpk(x[0], x[1]), cvtpk(y[0], y[1]), false, false);
                    const auto u1 = __builtin_amdgcn_permlane16_swap(cvtpk(x[2], x[3]), cvtpk(y[2], y[3]), false, false);
                    *(GAS u32x4*)(outp + (size_t)t * 16 * 2048 + n * 16) = (u32x4){u0[0], u1[0], u0[1], u1[1]};
                }
        }
    };
    if (half == 0) phase3(std::integral_constant<int, 0>{}); else phase3(std::integral_constant<int, 1>{});
    STAMP(10);
    { const u64 d = mtb - mta, t0 = __builtin_amdgcn_s_memrealtime(); if (d < 10000) while (__builtin_amdgcn_s_memrealtime() - t0 < 4 * d) __builtin_amdgcn_s_sleep(2); }
}

extern "C" void kernel_launch(void* const* d_in, const int* in_sizes, int n_in, void* d_out, int out_size, void* d_ws, size_t ws_size, hipStream_t stream) {
    Args a;
    a.x[0] = (const u16*)d_in[0]; a.w[0] = (const u16*)d_in[1]; a.wo[0] = (const u16*)d_in[2];
    a.x[1] = (const u16*)d_in[3]; a.w[1] = (const u16*)d_in[4]; a.wo[1] = (const u16*)d_in[5];
    a.out = (u16*)d_out; a.ws = (char*)d_ws;
    fused_kernel<<<256, 512, 0, stream>>>(a);
}
```

```cpp
#include <hip/hip_runtime.h>
#include <hip/hip_bf16.h>
#include <type_traits>
typedef short bf16x8 __attribute__((ext_vector_type(8)));
typedef short s16x4 __attribute__((ext_vector_type(4)));
typedef float f32x16 __attribute__((ext_vector_type(16)));
typedef float f32x4 __attribute__((ext_vector_type(4)));
typedef float f32x2 __attribute__((ext_vector_type(2)));
typedef __bf16 bf16x2 __attribute__((ext_vector_type(2)));
typedef unsigned u32x4 __attribute__((ext_vector_type(4)));
typedef unsigned short u16;
typedef unsigned long long u64;
#define LAS __attribute__((address_space(3)))
#define GAS __attribute__((address_space(1)))
#define DI __device__ __forceinline__
#define MFMA16(a, b, c) __builtin_amdgcn_mfma_f32_16x16x32_bf16((a), (b), (c), 0, 0, 0)
#define MFMA32(a, b, c) __builtin_amdgcn_mfma_f32_32x32x16_bf16((a), (b), (c), 0, 0, 0)
#define SBAR() __builtin_amdgcn_sched_barrier(0)
#define BAR() do { SBAR(); asm volatile("s_barrier" ::: "memory"); SBAR(); } while (0)
#define PIN(x) asm volatile("" : "+v"(x))
#define WAITL0() __builtin_amdgcn_s_waitcnt(0xC07F)
template <int N> DI void waitv() { asm volatile("s_waitcnt vmcnt(%0)" :: "i"(N) : "memory"); }
DI unsigned rfl(unsigned x) { return __builtin_amdgcn_readfirstlane(x); }
DI int lane_id() { int l; asm volatile("v_mbcnt_lo_u32_b32 %0, -1, 0\n\tv_mbcnt_hi_u32_b32 %0, -1, %0" : "=v"(l)); return l; }
DI unsigned cvtpk(float lo, float hi) { f32x2 v = {lo, hi}; bf16x2 b = __builtin_convertvector(v, bf16x2); return __builtin_bit_cast(unsigned, b); }
DI void dma16(unsigned m0v, unsigned voff, __amdgpu_buffer_rsrc_t srd, unsigned soff) {
    asm volatile("s_mov_b32 m0, %0\n\ts_nop 0\n\tbuffer_load_dwordx4 %1, %2, %3 offen lds" :: "s"(m0v), "v"(voff), "s"(srd), "s"(soff) : "memory");
}
DI s16x4 trread(LAS const char* p) { return __builtin_amdgcn_ds_read_tr16_b64_v4i16((LAS s16x4*)p); }
DI bf16x8 ldsfrag(LAS const char* p) { return *(LAS const bf16x8*)p; }
DI float max3f(float a, float b, float c) { return __builtin_fmaxf(__builtin_fmaxf(a, b), c); }
DI float fadd_s(float a, float b) { return a + b; }
DI float fsub_s(float a, float b) { return a - b; }
DI float fmul_s(float a, float b) { return a * b; }
DI bf16x8 pack8(float a, float b, float c, float d, float e, float f, float g, float h) { u32x4 v = {cvtpk(a, b), cvtpk(c, d), cvtpk(e, f), cvtpk(g, h)}; return __builtin_bit_cast(bf16x8, v); }

constexpr int LDS_A0 = 0, LDS_A1 = 32768, LDS_B = 81920, A_SLOT = 16384, B_SLOT = 24576;
constexpr int LDS_TOTAL = 163840;
constexpr size_t WS_FKV = 0, WS_EKV = 32768, WS_FO = 65536, WS_EO = 98304, WS_KV = 1 << 20, WS_O = 17 << 20, WS_END = 25 << 20, WS_ST = 32 << 20;
constexpr u64 TAGK = 0x9E3779B97F4A7C15ull;
constexpr int SPIN_CAP = 400000;
constexpr float QSCALE = 0.17677669529663687f * 1.4426950408889634f;

#define PUBJ 3
#define POLJ 1
#define OW 5
#define PA0 1
#define WPRE 4
#define LSAFE 1.152921504606847e18f
struct Args { const u16* x[2]; const u16* w[2]; const u16* wo[2]; u16* out; char* ws; };

#define STAMP(i) do {} while (0)

__global__ __launch_bounds__(512, 2) void fused_kernel(Args a) {
    __shared__ __attribute__((aligned(16))) char smem[LDS_TOTAL];
    LAS char* lds = (LAS char*)smem;
    const unsigned ldsb = (unsigned)(size_t)smem;
    const int tid = threadIdx.x, lane = tid & 63;
    const int wid = rfl(tid >> 6), half = wid >> 2, w4 = wid & 3;
    const int bid = blockIdx.x, g = bid & 7, j = bid >> 3;
    const int br = g >> 2, rb = ((g >> 1) & 1) * 4 + (j & 3), hp = (g & 1) * 8 + (j >> 2);
    const int me = (br * 8 + rb) * 16 + hp, grp = me & ~15;
    STAMP(0);
    u64 ew = __hip_atomic_load((GAS u64*)(a.ws + WS_EKV + (size_t)(lane == 0 || rb == 0 ? me : me - 16) * 128), __ATOMIC_RELAXED, __HIP_MEMORY_SCOPE_AGENT);
    const __amdgpu_buffer_rsrc_t srdA = __builtin_amdgcn_make_buffer_rsrc((void*)(a.x[br] + (size_t)rb * 256 * 1024), (short)0, 256 * 2048, 0x00020000);
    const __amdgpu_buffer_rsrc_t srdB = __builtin_amdgcn_make_buffer_rsrc((void*)(a.w[br]), (short)0, 1024 * 6144, 0x00020000);
    const int wr = 2 * half + (w4 >> 1), wc = w4 & 1;
    const int r16 = lane & 15, g4 = lane >> 4, q4 = (lane & 15) >> 2, p4 = lane & 3;
    f32x4 acc[4][6];
    {
        const unsigned voffE = (lane >> 3) * 2048 + (((lane & 7) ^ (lane >> 4)) << 4);
        const unsigned voffO = (8 + (lane >> 3)) * 2048 + (((lane & 7) ^ (4 + (lane >> 4))) << 4);
        const unsigned voffB0 = (lane >> 3) * 6144 + (((lane & 7) ^ (((lane >> 4) & 1) << 1)) << 4);
        const unsigned voffB1 = (lane >> 3) * 6144 + (((lane & 7) ^ ((((lane >> 4) & 1) << 1) | 4)) << 4);
        const int gt0 = (w4 & 1) ? 7 - (w4 >> 1) : (w4 >> 1);
        const int gt1 = (w4 & 1) ? 5 - (w4 >> 1) : 2 + (w4 >> 1);
        const unsigned sA0 = gt0 * 32 * 2048, sA1 = gt1 * 32 * 2048;
        const unsigned sB = hp * 128 + (2 * w4) * 8 * 6144;
        unsigned aoff[2], boff[2];
#pragma unroll
        for (int s = 0; s < 2; ++s) aoff[s] = (half ? LDS_A1 : LDS_A0) + ((w4 >> 1) * 64 + r16) * 128 + (((4 * s + g4) ^ ((r16 >> 1) & 7)) << 4);
#pragma unroll
        for (int c = 0; c < 2; ++c) boff[c] = LDS_B + (8 * g4 + q4) * 128 + (((wc * 2 + c) ^ ((q4 >> 1) | ((g4 & 1) << 1))) << 5) + p4 * 8;
#pragma unroll
        for (int i = 0; i < 4; ++i)
#pragma unroll
            for (int s = 0; s < 6; ++s) acc[i][s] = (f32x4){0.f, 0.f, 0.f, 0.f};
        bf16x8 af[4][2], bfr[6][2];
        auto run = [&](auto HC) {
            constexpr int H = decltype(HC)::value;
            auto piece = [&](int kt, int s2, int s3i, int i) {
                const unsigned soB = sB + kt * (64 * 6144), dB = ldsb + LDS_B + s3i * B_SLOT + w4 * 2048;
                const unsigned soA0 = sA0 + kt * 128, dA0 = ldsb + LDS_A0 + s2 * A_SLOT + w4 * 4096;
                const unsigned soA1 = sA1 + kt * 128, dA1 = ldsb + LDS_A1 + s3i * A_SLOT + w4 * 4096;
                if (H == 0) {
                    if (i == 0) dma16(dA1 + 3 * 1024, voffO, srdA, soA1 + 32768);
                    else { const int k = i - 1; dma16(dB + (k >> 1) * 8192 + (k & 1) * 1024, (k & 1) ? voffB1 : voffB0, srdB, soB + (k >> 1) * 2048 + (k & 1) * 8 * 6144); }
                } else dma16((i < 4 ? dA0 : dA1) + (i & 3) * 1024, (i & 1) ? voffO : voffE, srdA, (i < 4 ? soA0 : soA1) + ((i >> 1) & 1) * 32768);
            };
#pragma unroll
            for (int i = 0; i < 7; ++i) piece(0, 0, 0, i);
#pragma unroll
            for (int i = 0; i < 7; ++i) piece(1, 1, 1, i);
#pragma unroll
            for (int i = 0; i < 4; ++i) asm volatile("" : "+v"(acc[i][0]), "+v"(acc[i][1]), "+v"(acc[i][2]), "+v"(acc[i][3]), "+v"(acc[i][4]), "+v"(acc[i][5]));
            if (H == 0) waitv<7>(); else waitv<10>();
            BAR();
            if (H == 1) { waitv<7>(); BAR(); }
            STAMP(1);
            int s3 = 0;
            auto Lseg = [&](int t, bool issue) {
                const unsigned aslot = H ? s3 * A_SLOT : (t & 1) * A_SLOT;
                const unsigned bslot = s3 * B_SLOT;
                const int s3n = (s3 == 0) ? 2 : s3 - 1;
                auto rdA = [&](int i) { LAS const char* pa = lds + aoff[i >> 1] + aslot + (i & 1) * 4096; af[(i & 1) * 2][i >> 1] = ldsfrag(pa); af[(i & 1) * 2 + 1][i >> 1] = ldsfrag(pa + 2048); };
                auto rdB = [&](int i) { const int s = i / 6, seg = (i % 6) >> 1, c = i & 1; LAS const char* pb = lds + boff[c] + bslot;
                    s16x4 lo = trread(pb + seg * 8192 + s * 4096), hi = trread(pb + seg * 8192 + s * 4096 + 512);
                    bfr[seg * 2 + c][s] = __builtin_shufflevector(lo, hi, 0, 1, 2, 3, 4, 5, 6, 7); };
#pragma unroll
                for (int i = 0; i < 16; ++i) {
                    if (issue && (i & 1) == 0 && i / 2 < 7) piece(t + 2, t & 1, s3n, i / 2);
                    if (i < 4) rdA(i); else rdB(i - 4);
                }
                s3 = (s3 == 2) ? 0 : s3 + 1;
            };
            auto Mseg = [&]() {
                __builtin_amdgcn_s_setprio(1);
#pragma unroll
                for (int s = 0; s < 2; ++s)
#pragma unroll
                    for (int m = 0; m < 4; ++m)
#pragma unroll
                        for (int n = 0; n < 6; ++n) acc[m][n] = n < 4 ? MFMA16(bfr[n][s], af[m][s], acc[m][n]) : MFMA16(af[m][s], bfr[n][s], acc[m][n]);
                __builtin_amdgcn_s_setprio(0);
            };
            for (int t = 0; t < 14; ++t) {
                Lseg(t, true);
                if (H == 1) waitv<10>();
                WAITL0(); BAR();
                Mseg();
                waitv<7>();
                BAR();
            }
            Lseg(14, false); if (H == 1) waitv<3>(); WAITL0(); BAR(); Mseg(); waitv<0>(); BAR();
            Lseg(15, false); WAITL0(); BAR(); Mseg(); if (H == 0) BAR();
        };
        if (half == 0) run(std::integral_constant<int, 0>{}); else run(std::integral_constant<int, 1>{});
    }
    STAMP(2);
    const u64 tagp = (((u64)__builtin_amdgcn_readlane((unsigned)(ew >> 32), 0) << 32) | __builtin_amdgcn_readlane((unsigned)ew, 0)) + TAGK;
    const u64 tagc = (((u64)__builtin_amdgcn_readlane((unsigned)(ew >> 32), 1) << 32) | __builtin_amdgcn_readlane((unsigned)ew, 1)) + TAGK;
    const __amdgpu_buffer_rsrc_t srdW = __builtin_amdgcn_make_buffer_rsrc((void*)a.ws, (short)0, (int)WS_END, 0x00020000);
    const __amdgpu_buffer_rsrc_t srdWo = __builtin_amdgcn_make_buffer_rsrc((void*)(a.wo[br]), (short)0, 1024 * 2048, 0x00020000);
    const int ct = hp & 7, th = hp >> 3;
    const unsigned sW = ct * 256 + (half * 512 + w4 * 8) * 2048;
    auto wo_steps = [&](int s0, int s1) {
        const int ln = lane_id();
        const unsigned voffW0 = (ln >> 4) * 2048 + (((ln & 15) ^ ((ln >> 4) << 1)) << 4), voffW1 = (4 + (ln >> 4)) * 2048 + (((ln & 15) ^ ((4 + (ln >> 4)) << 1)) << 4);
#pragma unroll
        for (int s = s0; s < s1; ++s) { const unsigned d = ldsb + ((s + 8) % 10) * 16384 + wid * 2048; dma16(d, voffW0, srdWo, sW + s * 65536); dma16(d + 1024, voffW1, srdWo, sW + s * 65536); } };
    bf16x8 qf[2][2];
    {
        const unsigned kpos = (g4 >> 1) * 1024 + (g4 & 1) * 512 + r16 * 16;
        const unsigned vpos = 2048 + ((g4 & 1) * 32 + (g4 >> 1) * 16 + r16) * 16;
        const unsigned gkv = (unsigned)WS_KV + me * 65536 + wc * 32768;
        u32x4 kv[2][2], vv[2][2];
#pragma unroll
        for (int mi = 0; mi < 2; ++mi)
#pragma unroll
            for (int s = 0; s < 2; ++s) {
                const f32x4 k0 = acc[2 * mi + s][2], k1 = acc[2 * mi + s][3], v0 = acc[2 * mi + s][4], v1 = acc[2 * mi + s][5];
                kv[mi][s] = (u32x4){cvtpk(k0[0], k0[1]), cvtpk(k0[2], k0[3]), cvtpk(k1[0], k1[1]), cvtpk(k1[2], k1[3])};
                const auto ra = __builtin_amdgcn_permlane32_swap(cvtpk(v0[0], v0[1]), cvtpk(v1[0], v1[1]), false, false);
                const auto rbb = __builtin_amdgcn_permlane32_swap(cvtpk(v0[2], v0[3]), cvtpk(v1[2], v1[3]), false, false);
                vv[mi][s] = (u32x4){ra[0], rbb[0], ra[1], rbb[1]};
                const unsigned blk = wc * 65536 + 32768 + (mi ? 7 - wr : wr) * 4096;
                *(LAS u32x4*)(lds + blk + kpos + s * 256) = kv[mi][s];
                *(LAS u32x4*)(lds + blk + vpos + s * 1024) = vv[mi][s];
            }
        if (rb < 7) {
#pragma unroll
            for (int mi = 0; mi < 2; ++mi)
#pragma unroll
                for (int s = 0; s < 2; ++s) { const unsigned blk = (mi ? 7 - wr : wr) * 4096;
                    __builtin_amdgcn_raw_buffer_store_b128(kv[mi][s], srdW, gkv + blk + kpos + s * 256, 0, 16);
                    __builtin_amdgcn_raw_buffer_store_b128(vv[mi][s], srdW, gkv + blk + vpos + s * 1024, 0, 16); }
        }
#pragma unroll
        for (int mi = 0; mi < 2; ++mi) {
            u32x4 x[2], y0, y1;
#pragma unroll
            for (int s = 0; s < 2; ++s) { const f32x4 q0 = acc[2 * mi + s][0] * QSCALE, q1 = acc[2 * mi + s][1] * QSCALE;
                x[s] = (u32x4){cvtpk(q0[0], q0[1]), cvtpk(q0[2], q0[3]), cvtpk(q1[0], q1[1]), cvtpk(q1[2], q1[3])}; }
#pragma unroll
            for (int i = 0; i < 4; ++i) { const auto u = __builtin_amdgcn_permlane16_swap(x[0][i], x[1][i], false, false);
                const auto v = __builtin_amdgcn_permlane32_swap(u[0], u[1], false, false); y0[i] = v[0]; y1[i] = v[1]; }
            qf[mi][0] = __builtin_bit_cast(bf16x8, y0); qf[mi][1] = __builtin_bit_cast(bf16x8, y1);
        }
    }
    f32x16 o[2], nm[2]; f32x4 l[2];
    const int r32 = lane & 31, hh = lane >> 5;
    LAS const char* kvb = lds + wc * 65536 + lane * 16;
    bf16x8 ones;
    { const short v = ((lane ^ (lane >> 4)) & 1) ? (short)0 : (short)0x3f80;
#pragma unroll
      for (int i = 0; i < 8; ++i) ones[i] = v; }
    auto jmax = [&](const f32x16& s, float m) {
#pragma unroll
        for (int r = 0; r < 16; r += 2) m = max3f(m, s[r], s[r + 1]);
        return m; };
    auto xmax = [&](float m) { const auto r = __builtin_amdgcn_permlane32_swap(__float_as_uint(m), __float_as_uint(m), false, false); return __builtin_fmaxf(__uint_as_float(r[0]), __uint_as_float(r[1])); };
    auto expo = [&](const f32x16& s, bf16x8& p0, bf16x8& p1) {
        float t[16];
#pragma unroll
        for (int r = 0; r < 16; ++r) t[r] = __builtin_amdgcn_exp2f(s[r]);
        p0 = pack8(t[0], t[1], t[2], t[3], t[4], t[5], t[6], t[7]); p1 = pack8(t[8], t[9], t[10], t[11], t[12], t[13], t[14], t[15]); };
    auto trimask = [&](bool lower) { f32x16 c;
#pragma unroll
        for (int r = 0; r < 16; ++r) c[r] = (((r & 3) + 8 * (r >> 2) + 4 * hh <= r32) == lower) ? 0.f : -INFINITY;
        return c; };
    {
        const f32x16 mk = trimask(true);
        f32x16 zero16, s[2];
#pragma unroll
        for (int r = 0; r < 16; ++r) zero16[r] = 0.f;
#pragma unroll
        for (int d = 0; d < 2; ++d) {
            LAS const char* p = kvb + (8 + (d ? 7 - wr : wr)) * 4096;
            s[d] = MFMA32(ldsfrag(p), qf[d][0], mk); s[d] = MFMA32(ldsfrag(p + 1024), qf[d][1], s[d]);
        }
        wo_steps(0, 2);
        const float m0 = xmax(jmax(s[0], -INFINITY)), m1 = xmax(jmax(s[1], -INFINITY));
#pragma unroll
        for (int d = 0; d < 2; ++d) {
            LAS const char* p = kvb + (8 + (d ? 7 - wr : wr)) * 4096;
            const float m = d ? m1 : m0;
#pragma unroll
            for (int r = 0; r < 16; ++r) { nm[d][r] = -m; s[d][r] = fsub_s(s[d][r], m); }
            bf16x8 p0, p1;
            expo(s[d], p0, p1);
            o[d] = MFMA32(ldsfrag(p + 2048), p0, zero16); l[d] = MFMA16(ones, p0, ((f32x4){0.f, 0.f, 0.f, 0.f}));
            o[d] = MFMA32(ldsfrag(p + 3072), p1, o[d]); l[d] = MFMA16(ones, p1, l[d]);
        }
    }
    waitv<4>(); WAITL0(); BAR();
    if (wid == 0 && lane == 0 && rb < 7) __hip_atomic_store((GAS u64*)(a.ws + WS_FKV + (size_t)me * 128), tagp, __ATOMIC_RELAXED, __HIP_MEMORY_SCOPE_AGENT);
    STAMP(3);
    auto attention = [&](auto WC_) {
        constexpr int WR = decltype(WC_)::value;
        u32x4 pld[8];
        auto jobs = [&](auto PART) {
            constexpr int P2 = decltype(PART)::value;
            constexpr int NJ = P2 ? 9 : 7;
            auto TOF = [](int j) { return P2 ? (j < 7 ? (j < 7 - WR ? 0 : 1) : j - 7) : (j < WR ? 0 : 1); };
            auto BOF = [](int j) { return P2 ? (j < 7 ? (j < 7 - WR ? WR + 1 + j : j + 1) : (j == 7 ? WR : 7 - WR)) : 8 + (j < WR ? j : j - WR); };
            f32x16 S[2], cm; bf16x8 kf[2][2], vf[2][2]; u32x4 p0, p1;
            auto rdk = [&](int j) { kf[j & 1][0] = ldsfrag(kvb + BOF(j) * 4096); kf[j & 1][1] = ldsfrag(kvb + BOF(j) * 4096 + 1024); };
            auto cof = [&](int j) -> const f32x16& { return (P2 && j >= 7) ? cm : nm[TOF(j)]; };
            auto mkc = [&](int j) { const f32x16 mi = trimask(false);
#pragma unroll
                for (int r = 0; r < 16; ++r) cm[r] = fadd_s(mi[r], nm[TOF(j)][r]); };
            rdk(0); rdk(1); vf[0][0] = ldsfrag(kvb + BOF(0) * 4096 + 2048);
            S[0] = MFMA32(kf[0][0], qf[TOF(0)][0], cof(0)); S[0] = MFMA32(kf[0][1], qf[TOF(0)][1], S[0]);
            SBAR();
#pragma unroll
            for (int j = 0; j < NJ; ++j) {
                const int T = TOF(j), TP = j > 0 ? TOF(j - 1) : 0, TN = j + 1 < NJ ? TOF(j + 1) : 0;
                f32x16& s = S[j & 1];
#define EX(i) s[i] = __builtin_amdgcn_exp2f(s[i])
                if (P2 && j + 1 >= 7 && j + 1 < NJ) { mkc(j + 1); PIN(cm); }
                if (j + 1 < NJ) S[(j + 1) & 1] = MFMA32(kf[(j + 1) & 1][0], qf[TN][0], cof(j + 1));
                SBAR();
                vf[j & 1][1] = ldsfrag(kvb + BOF(j) * 4096 + 3072);
                EX(0); EX(1); EX(2); PIN(s); SBAR();
                if (j > 0) o[TP] = MFMA32(vf[(j - 1) & 1][1], __builtin_bit_cast(bf16x8, p1), o[TP]);
                SBAR();
                EX(3); EX(4); EX(5); PIN(s); SBAR();
                if (j > 0) l[TP] = MFMA16(ones, __builtin_bit_cast(bf16x8, p1), l[TP]);
                SBAR();
                EX(6); EX(7);
                p0 = (u32x4){cvtpk(s[0], s[1]), cvtpk(s[2], s[3]), cvtpk(s[4], s[5]), cvtpk(s[6], s[7])}; PIN(p0); SBAR();
                if (j + 1 < NJ) S[(j + 1) & 1] = MFMA32(kf[(j + 1) & 1][1], qf[TN][1], S[(j + 1) & 1]);
                SBAR();
                if (j + 1 < NJ) vf[(j + 1) & 1][0] = ldsfrag(kvb + BOF(j + 1) * 4096 + 2048);
                EX(8); EX(9); EX(10); PIN(s); SBAR();
                o[T] = MFMA32(vf[j & 1][0], __builtin_bit_cast(bf16x8, p0), o[T]);
                SBAR();
                if (j + 2 < NJ) rdk(j + 2);
                EX(11); EX(12); EX(13); PIN(s); SBAR();
                l[T] = MFMA16(ones, __builtin_bit_cast(bf16x8, p0), l[T]);
                SBAR();
                EX(14); EX(15);
                p1 = (u32x4){cvtpk(s[8], s[9]), cvtpk(s[10], s[11]), cvtpk(s[12], s[13]), cvtpk(s[14], s[15])}; PIN(p1); SBAR();
#undef EX
                if (j == NJ - 1) { o[T] = MFMA32(vf[j & 1][1], __builtin_bit_cast(bf16x8, p1), o[T]); l[T] = MFMA16(ones, __builtin_bit_cast(bf16x8, p1), l[T]); }
                if (!P2 && j == POLJ && rb > 0) {
                    if (WR == 0 && wid == 0) {
                        GAS u64* f = (GAS u64*)(a.ws + WS_FKV + (size_t)(me - 16) * 128);
#pragma nounroll
                        for (int spin = 0; spin < SPIN_CAP; ++spin) {
                            const u64 v = __hip_atomic_load(f, __ATOMIC_RELAXED, __HIP_MEMORY_SCOPE_AGENT);
                            if (rfl((unsigned)v) == (unsigned)tagc && rfl((unsigned)(v >> 32)) == (unsigned)(tagc >> 32)) break;
                            __builtin_amdgcn_s_sleep(1);
                        }
                        if (lane == 0) __hip_atomic_store((GAS u64*)(a.ws + WS_EKV + (size_t)(me - 16) * 128), tagc, __ATOMIC_RELAXED, __HIP_MEMORY_SCOPE_AGENT);
                    }
                    BAR();
                    STAMP(5);
#pragma unroll
                    for (int k = 0; k < 8; ++k) pld[k] = __builtin_amdgcn_raw_buffer_load_b128(srdW, (unsigned)WS_KV + (me - 16) * 65536 + wid * 8192 + k * 1024 + lane * 16, 0, 16);
                    SBAR();
                }
            }
        };
        jobs(std::integral_constant<int, 0>{});
        STAMP(4);
        if (rb > 0) {
#pragma unroll
            for (int i = 0; i < 8; ++i) *(LAS u32x4*)(lds + half * 65536 + w4 * 8192 + i * 1024 + lane * 16) = pld[i];
            WAITL0(); BAR();
            STAMP(6);
            jobs(std::integral_constant<int, 1>{});
        }
    };
#define ATT(W) attention(std::integral_constant<int, W>{})
    if (wr == 0) ATT(0); else if (wr == 1) ATT(1); else if (wr == 2) ATT(2); else ATT(3);
#pragma unroll
    for (int d = 0; d < 2; ++d) {
        const int gt = d ? 7 - wr : wr;
        float lt = (lane & 16) ? l[d][1] : l[d][0];
        if (__builtin_expect(__any(!(lt <= LSAFE)), 0)) {
            const f32x16 mlo = trimask(true), mhi = trimask(false);
            const int b0 = rb > 0 ? gt : 8, b1 = 8 + gt;
            auto sc = [&](int b, float ref) { f32x16 c;
#pragma unroll
                for (int r = 0; r < 16; ++r) c[r] = (b == b1 ? mlo[r] : b == gt ? mhi[r] : 0.f) - ref;
                c = MFMA32(ldsfrag(kvb + b * 4096), qf[d][0], c); return MFMA32(ldsfrag(kvb + b * 4096 + 1024), qf[d][1], c); };
            float m = -INFINITY;
#pragma nounroll
            for (int b = b0; b <= b1; ++b) m = jmax(sc(b, 0.f), m);
            m = xmax(m);
            f32x4 ls = {0.f, 0.f, 0.f, 0.f};
#pragma unroll
            for (int r = 0; r < 16; ++r) o[d][r] = 0.f;
#pragma nounroll
            for (int b = b0; b <= b1; ++b) { bf16x8 p0, p1; expo(sc(b, m), p0, p1);
                o[d] = MFMA32(ldsfrag(kvb + b * 4096 + 2048), p0, o[d]); o[d] = MFMA32(ldsfrag(kvb + b * 4096 + 3072), p1, o[d]);
                ls = MFMA16(ones, p0, ls); ls = MFMA16(ones, p1, ls); }
            lt = (lane & 16) ? ls[1] : ls[0];
        }
        const float inv = __builtin_amdgcn_rcpf(lt);
        const unsigned base = (unsigned)WS_O + (((br * 128 + rb * 16 + gt * 2 + (r32 >> 4)) * 32) + hp * 2 + wc) * 1024 + hh * 256 + (r32 & 15) * 16;
#pragma unroll
        for (int e = 0; e < 2; ++e) {
            const f32x16& x = o[d];
            const u32x4 v = {cvtpk(fmul_s(x[8 * e], inv), fmul_s(x[8 * e + 1], inv)), cvtpk(fmul_s(x[8 * e + 2], inv), fmul_s(x[8 * e + 3], inv)), cvtpk(fmul_s(x[8 * e + 4], inv), fmul_s(x[8 * e + 5], inv)), cvtpk(fmul_s(x[8 * e + 6], inv), fmul_s(x[8 * e + 7], inv))};
            __builtin_amdgcn_raw_buffer_store_b128(v, srdW, base + e * 512, 0, 16);
        }
    }
    STAMP(7);
    auto phase3 = [&](auto HC) {
        constexpr int H = decltype(HC)::value;
        const int lane = lane_id(), r16 = lane & 15, g4 = lane >> 4, q4 = (lane & 15) >> 2, p4 = lane & 3;
        GAS char* ep = (GAS char*)a.ws + WS_EO + (lane < 16 ? (size_t)(grp + lane) * 128 + hp * 8 : (size_t)me * 128 + (lane & 15) * 8);
        u64 eo;
        asm volatile("global_load_dwordx2 %0, %1, off sc1" : "=v"(eo) : "v"(ep) : "memory");
        unsigned woff[8];
        { const int row = 16 * (g4 >> 1) + 4 * (g4 & 1) + q4;
#pragma unroll
          for (int n = 0; n < 8; ++n) woff[n] = 131072 + H * 8192 + row * 256 + ((n ^ (row & 7)) << 5) + p4 * 8; }
        bf16x8 wf[2][8];
        auto wread = [&](int s, int n) {
            if (s == 2 || s == 12) { woff[n] -= 131072; PIN(woff[n]); } else if (s == 6 || s == 10) { woff[n] += 65536; PIN(woff[n]); }
            LAS const char* p = lds + woff[n] + (((s + 8) % 10) & 3) * 16384;
            const s16x4 lo = trread(p), hi = trread(p + 2048); wf[s & 1][n] = __builtin_shufflevector(lo, hi, 0, 1, 2, 3, 4, 5, 6, 7); };
        waitv<5>(); WAITL0(); BAR();
        wo_steps(2, 2 + WPRE);
#pragma unroll
        for (int n = 0; n < 8; ++n) { wread(0, n); wread(1, n); }
        waitv<2 * WPRE>(); PIN(eo); WAITL0(); BAR();
        if (H == 0 && wid == 0) {
            const u64 tag = eo + TAGK;
            if (lane < 16) __hip_atomic_store((GAS u64*)(ep - WS_EO + WS_FO), tag, __ATOMIC_RELAXED, __HIP_MEMORY_SCOPE_AGENT);
        }
        wo_steps(2 + WPRE, 12);
        if (H == 0 && wid == 0) {
            const u64 tag = eo + TAGK;
            GAS u64* f = (GAS u64*)(a.ws + WS_FO + (size_t)me * 128 + (lane & 15) * 8);
            const bool mine = lane >= 16 && lane < 32;
#pragma nounroll
            for (int spin = 0; spin < SPIN_CAP; ++spin) {
                const u64 v = __hip_atomic_load(f, __ATOMIC_RELAXED, __HIP_MEMORY_SCOPE_AGENT);
                if (__all(!mine || v == tag)) break;
                __builtin_amdgcn_s_sleep(1);
            }
            if (mine) __hip_atomic_store((GAS u64*)ep, tag, __ATOMIC_RELAXED, __HIP_MEMORY_SCOPE_AGENT);
        }
        waitv<0>(); BAR();
        STAMP(8);
        bf16x8 of[OW][2];
        const unsigned obase = (unsigned)WS_O + ((br * 128 + rb * 16 + th * 8 + w4 * 2) * 32 + H * 16) * 1024 + lane * 16;
        auto oload = [&](int s, int t) { of[s % OW][t] = __builtin_bit_cast(bf16x8, __builtin_amdgcn_raw_buffer_load_b128(srdW, obase + t * 32768 + s * 1024, 0, 16)); };
#pragma unroll
        for (int s = 0; s < OW - 1; ++s) { oload(s, 0); oload(s, 1); SBAR(); }
        f32x4 c2[2][8];
#pragma unroll
        for (int t = 0; t < 2; ++t)
#pragma unroll
            for (int n = 0; n < 8; ++n) c2[t][n] = (f32x4){0.f, 0.f, 0.f, 0.f};
        const unsigned voffW0 = (lane >> 4) * 2048 + (((lane & 15) ^ ((lane >> 4) << 1)) << 4), voffW1 = (4 + (lane >> 4)) * 2048 + (((lane & 15) ^ ((4 + (lane >> 4)) << 1)) << 4);
        {
            auto step = [&](int s) {
#pragma unroll
                for (int n = 0; n < 8; ++n) {
                    c2[0][n] = MFMA16(wf[s & 1][n], of[s % OW][0], c2[0][n]); c2[1][n] = MFMA16(wf[s & 1][n], of[s % OW][1], c2[1][n]);
                    SBAR();
                    if (s + 2 < 16) wread(s + 2, n);
                    if (n == PA0 + 2 * H && s + OW - 1 < 16) oload(s + OW - 1, 0);
                    if (n == PA0 + 4 + 2 * H && s + OW - 1 < 16) oload(s + OW - 1, 1);
                    if (s >= 4 && s < 8 && (n == PA0 + 2 - 2 * H || n == PA0 + 6 - 2 * H)) dma16(ldsb + ((s + 16) % 10) * 16384 + wid * 2048 + (n >= 4) * 1024, n >= 4 ? voffW1 : voffW0, srdWo, sW + (s + 8) * 65536);
                    SBAR();
                } };
#pragma unroll
            for (int s = 0; s < 4; ++s) { step(s); if (s == 0) STAMP(11); }
            STAMP(12);
            WAITL0(); BAR();
#pragma unroll
            for (int s = 4; s < 10; ++s) step(s);
            STAMP(13);
            waitv<4>(); BAR();
            STAMP(14);
#pragma unroll
            for (int s = 10; s < 16; ++s) step(s);
            STAMP(9);
            BAR();
#pragma unroll
            for (int t = 0; t < 2; ++t)
#pragma unroll
                for (int n = 0; n < 4; ++n) *(LAS f32x4*)(lds + wid * 8192 + (t * 4 + n) * 1024 + lane * 16) = c2[t][4 - 4 * H + n];
            WAITL0(); BAR();
            STAMP(15);
            GAS u16* outp = (GAS u16*)a.out + (size_t)(rb * 256 + th * 128 + w4 * 32 + r16) * 2048 + br * 1024 + ct * 128 + H * 64 + (g4 & 1) * 16 + (g4 >> 1) * 8;
#pragma unroll
            for (int t = 0; t < 2; ++t)
#pragma unroll
                for (int n = 0; n < 4; n += 2) {
                    const f32x4 x = c2[t][4 * H + n] + *(LAS const f32x4*)(lds + (wid ^ 4) * 8192 + (t * 4 + n) * 1024 + lane * 16);
                    const f32x4 y = c2[t][4 * H + n + 1] + *(LAS const f32x4*)(lds + (wid ^ 4) * 8192 + (t * 4 + n + 1) * 1024 + lane * 16);
                    const auto u0 = __builtin_amdgcn_permlane16_swap(cvtpk(x[0], x[1]), cvtpk(y[0], y[1]), false, false);
                    const auto u1 = __builtin_amdgcn_permlane16_swap(cvtpk(x[2], x[3]), cvtpk(y[2], y[3]), false, false);
                    { const u32x4 vv = {u0[0], u1[0], u0[1], u1[1]}; GAS u16* pp = outp + (size_t)t * 16 * 2048 + n * 16; asm volatile("global_store_dwordx4 %0, %1, off sc0 sc1\n\ts_nop 1" :: "v"(pp), "v"(vv) : "memory"); }
                }
        }
    };
    if (half == 0) phase3(std::integral_constant<int, 0>{}); else phase3(std::integral_constant<int, 1>{});
    STAMP(10);
}

extern "C" void kernel_launch(void* const* d_in, const int* in_sizes, int n_in, void* d_out, int out_size, void* d_ws, size_t ws_size, hipStream_t stream) {
    Args a;
    a.x[0] = (const u16*)d_in[0]; a.w[0] = (const u16*)d_in[1]; a.wo[0] = (const u16*)d_in[2];
    a.x[1] = (const u16*)d_in[3]; a.w[1] = (const u16*)d_in[4]; a.wo[1] = (const u16*)d_in[5];
    a.out = (u16*)d_out; a.ws = (char*)d_ws;
    fused_kernel<<<256, 512, 0, stream>>>(a);
}
```

```cpp
#define CODEPF 48
#include <hip/hip_runtime.h>
#include <hip/hip_bf16.h>
#include <type_traits>
typedef short bf16x8 __attribute__((ext_vector_type(8)));
typedef short s16x4 __attribute__((ext_vector_type(4)));
typedef float f32x16 __attribute__((ext_vector_type(16)));
typedef float f32x4 __attribute__((ext_vector_type(4)));
typedef float f32x2 __attribute__((ext_vector_type(2)));
typedef __bf16 bf16x2 __attribute__((ext_vector_type(2)));
typedef unsigned u32x4 __attribute__((ext_vector_type(4)));
typedef unsigned short u16;
typedef unsigned long long u64;
#define LAS __attribute__((address_space(3)))
#define GAS __attribute__((address_space(1)))
#define DI __device__ __forceinline__
#define MFMA16(a, b, c) __builtin_amdgcn_mfma_f32_16x16x32_bf16((a), (b), (c), 0, 0, 0)
#define MFMA32(a, b, c) __builtin_amdgcn_mfma_f32_32x32x16_bf16((a), (b), (c), 0, 0, 0)
#define SBAR() __builtin_amdgcn_sched_barrier(0)
#define BAR() do { SBAR(); asm volatile("s_barrier" ::: "memory"); SBAR(); } while (0)
#define PIN(x) asm volatile("" : "+v"(x))
#define WAITL0() __builtin_amdgcn_s_waitcnt(0xC07F)
template <int N> DI void waitv() { asm volatile("s_waitcnt vmcnt(%0)" :: "i"(N) : "memory"); }
DI unsigned rfl(unsigned x) { return __builtin_amdgcn_readfirstlane(x); }
DI int lane_id() { int l; asm volatile("v_mbcnt_lo_u32_b32 %0, -1, 0\n\tv_mbcnt_hi_u32_b32 %0, -1, %0" : "=v"(l)); return l; }
DI unsigned cvtpk(float lo, float hi) { f32x2 v = {lo, hi}; bf16x2 b = __builtin_convertvector(v, bf16x2); return __builtin_bit_cast(unsigned, b); }
DI void dma16(unsigned m0v, unsigned voff, __amdgpu_buffer_rsrc_t srd, unsigned soff) {
    asm volatile("s_mov_b32 m0, %0\n\ts_nop 0\n\tbuffer_load_dwordx4 %1, %2, %3 offen lds" :: "s"(m0v), "v"(voff), "s"(srd), "s"(soff) : "memory");
}
DI void dma4(unsigned m0v, unsigned voff, __amdgpu_buffer_rsrc_t srd, unsigned soff) {
    asm volatile("s_mov_b32 m0, %0\n\ts_nop 0\n\tbuffer_load_dword %1, %2, %3 offen lds" :: "s"(m0v), "v"(voff), "s"(srd), "s"(soff) : "memory");
}
DI s16x4 trread(LAS const char* p) { return __builtin_amdgcn_ds_read_tr16_b64_v4i16((LAS s16x4*)p); }
DI bf16x8 ldsfrag(LAS const char* p) { return *(LAS const bf16x8*)p; }
DI float max3f(float a, float b, float c) { return __builtin_fmaxf(__builtin_fmaxf(a, b), c); }
DI float fadd_s(float a, float b) { return a + b; }
DI float fsub_s(float a, float b) { return a - b; }
DI float fmul_s(float a, float b) { return a * b; }
DI bf16x8 pack8(float a, float b, float c, float d, float e, float f, float g, float h) { u32x4 v = {cvtpk(a, b), cvtpk(c, d), cvtpk(e, f), cvtpk(g, h)}; return __builtin_bit_cast(bf16x8, v); }

constexpr int LDS_A0 = 0, LDS_A1 = 32768, LDS_B = 81920, A_SLOT = 16384, B_SLOT = 24576;
constexpr int LDS_TOTAL = 163840;
constexpr size_t WS_FKV = 0, WS_EKV = 32768, WS_FO = 65536, WS_EO = 98304, WS_KV = 1 << 20, WS_O = 17 << 20, WS_END = 25 << 20, WS_ST = 32 << 20;
constexpr u64 TAGK = 0x9E3779B97F4A7C15ull;
constexpr int SPIN_CAP = 400000;
constexpr float QSCALE = 0.17677669529663687f * 1.4426950408889634f;

#define PUBJ 3
#define POLJ 1
#define OW 5
#define PA0 1
#define WPRE 4
#define LSAFE 1.152921504606847e18f
#define KROT 0
#define TOUCH 0
#define PD 5
constexpr int LDS_SCR = 155648;
struct Args { const u16* x[2]; const u16* w[2]; const u16* wo[2]; u16* out; char* ws; };

#define STAMP(i) do {} while (0)

__global__ __launch_bounds__(512, 2) void fused_kernel(Args a) {
    __shared__ __attribute__((aligned(16))) char smem[LDS_TOTAL];
    LAS char* lds = (LAS char*)smem;
    const unsigned ldsb = (unsigned)(size_t)smem;
    const int tid = threadIdx.x, lane = tid & 63;
    const int wid = rfl(tid >> 6), half = wid >> 2, w4 = wid & 3;
    const int bid = blockIdx.x, g = bid & 7, j = bid >> 3;
    const int br = g >> 2, rb = ((g >> 1) & 1) * 4 + (j & 3), hp = (g & 1) * 8 + (j >> 2);
    const int me = (br * 8 + rb) * 16 + hp, grp = me & ~15;
    STAMP(0);
    if (CODEPF && wid == 7) {
        u64 pc; asm volatile("s_getpc_b64 %0" : "=s"(pc));
        const __amdgpu_buffer_rsrc_t srdC = __builtin_amdgcn_make_buffer_rsrc((void*)pc, (short)0, CODEPF * 1024, 0x00020000);
        if (lane < 13) dma4(ldsb + LDS_SCR, (j * 13 + lane) * 128, srdC, 0);
    }
    u64 ew = __hip_atomic_load((GAS u64*)(a.ws + WS_EKV + (size_t)(lane == 0 || rb == 0 ? me : me - 16) * 128), __ATOMIC_RELAXED, __HIP_MEMORY_SCOPE_AGENT);
    const __amdgpu_buffer_rsrc_t srdA = __builtin_amdgcn_make_buffer_rsrc((void*)(a.x[br] + (size_t)rb * 256 * 1024), (short)0, 256 * 2048, 0x00020000);
    const __amdgpu_buffer_rsrc_t srdB = __builtin_amdgcn_make_buffer_rsrc((void*)(a.w[br]), (short)0, 1024 * 6144, 0x00020000);
    const int wr = 2 * half + (w4 >> 1), wc = w4 & 1;
    const int r16 = lane & 15, g4 = lane >> 4, q4 = (lane & 15) >> 2, p4 = lane & 3;
    f32x4 acc[4][6];
    {
        const unsigned voffE = (lane >> 3) * 2048 + (((lane & 7) ^ (lane >> 4)) << 4);
        const unsigned voffO = (8 + (lane >> 3)) * 2048 + (((lane & 7) ^ (4 + (lane >> 4))) << 4);
        const unsigned voffB0 = (lane >> 3) * 6144 + (((lane & 7) ^ (((lane >> 4) & 1) << 1)) << 4);
        const unsigned voffB1 = (lane >> 3) * 6144 + (((lane & 7) ^ ((((lane >> 4) & 1) << 1) | 4)) << 4);
        const int gt0 = (w4 & 1) ? 7 - (w4 >> 1) : (w4 >> 1);
        const int gt1 = (w4 & 1) ? 5 - (w4 >> 1) : 2 + (w4 >> 1);
        const unsigned sA0 = gt0 * 32 * 2048, sA1 = gt1 * 32 * 2048;
        const unsigned sB = hp * 128 + (2 * w4) * 8 * 6144;
        unsigned aoff[2], boff[2];
#pragma unroll
        for (int s = 0; s < 2; ++s) aoff[s] = (half ? LDS_A1 : LDS_A0) + ((w4 >> 1) * 64 + r16) * 128 + (((4 * s + g4) ^ ((r16 >> 1) & 7)) << 4);
#pragma unroll
        for (int c = 0; c < 2; ++c) boff[c] = LDS_B + (8 * g4 + q4) * 128 + (((wc * 2 + c) ^ ((q4 >> 1) | ((g4 & 1) << 1))) << 5) + p4 * 8;
#pragma unroll
        for (int i = 0; i < 4; ++i)
#pragma unroll
            for (int s = 0; s < 6; ++s) acc[i][s] = (f32x4){0.f, 0.f, 0.f, 0.f};
        bf16x8 af[4][2], bfr[6][2];
        const unsigned voffT = w4 < 2 ? (32 * (hp & 7) + 16 * w4 + lane) * 2048 : (16 * (rb & 3) + 8 * (w4 - 2) + lane / 3) * 6144 + (lane % 3) * 2048 + hp * 128;
        const __amdgpu_buffer_rsrc_t srdT = __builtin_amdgcn_make_buffer_rsrc(w4 < 2 ? (void*)(a.x[br] + (size_t)rb * 256 * 1024) : (void*)(a.w[br]), (short)0, w4 < 2 ? 256 * 2048 : 1024 * 6144, 0x00020000);
        const unsigned tstep = w4 < 2 ? 128 : 64 * 6144; const int tl = w4 < 2 ? 16 : 24;
        auto run = [&](auto HC) {
            constexpr int H = decltype(HC)::value;
            constexpr int TW = (TOUCH && H == 0) ? 1 : 0;
            auto touch = [&](int kt0) { const int kt = (kt0 + KROT * g) & 15; if (lane < tl) dma4(ldsb + LDS_SCR, voffT, srdT, kt * tstep); };
            auto piece = [&](int kt0, int s2, int s3i, int i) {
                const int kt = KROT ? (kt0 + KROT * g) & 15 : kt0;
                const unsigned soB = sB + kt * (64 * 6144), dB = ldsb + LDS_B + s3i * B_SLOT + w4 * 2048;
                const unsigned soA0 = sA0 + kt * 128, dA0 = ldsb + LDS_A0 + s2 * A_SLOT + w4 * 4096;
                const unsigned soA1 = sA1 + kt * 128, dA1 = ldsb + LDS_A1 + s3i * A_SLOT + w4 * 4096;
                if (H == 0) {
                    if (i == 0) dma16(dA1 + 3 * 1024, voffO, srdA, soA1 + 32768);
                    else { const int k = i - 1; dma16(dB + (k >> 1) * 8192 + (k & 1) * 1024, (k & 1) ? voffB1 : voffB0, srdB, soB + (k >> 1) * 2048 + (k & 1) * 8 * 6144); }
                } else dma16((i < 4 ? dA0 : dA1) + (i & 3) * 1024, (i & 1) ? voffO : voffE, srdA, (i < 4 ? soA0 : soA1) + ((i >> 1) & 1) * 32768);
            };
#pragma unroll
            for (int i = 0; i < 7; ++i) piece(0, 0, 0, i);
#pragma unroll
            for (int i = 0; i < 7; ++i) piece(1, 1, 1, i);
#pragma unroll
            for (int i = 0; i < 4; ++i) asm volatile("" : "+v"(acc[i][0]), "+v"(acc[i][1]), "+v"(acc[i][2]), "+v"(acc[i][3]), "+v"(acc[i][4]), "+v"(acc[i][5]));
            if (H == 0) waitv<7>(); else waitv<10>();
            BAR();
            if (H == 1) { waitv<7>(); BAR(); }
            STAMP(1);
            int s3 = 0;
            auto Lseg = [&](int t, bool issue) {
                const unsigned aslot = H ? s3 * A_SLOT : (t & 1) * A_SLOT;
                const unsigned bslot = s3 * B_SLOT;
                const int s3n = (s3 == 0) ? 2 : s3 - 1;
                auto rdA = [&](int i) { LAS const char* pa = lds + aoff[i >> 1] + aslot + (i & 1) * 4096; af[(i & 1) * 2][i >> 1] = ldsfrag(pa); af[(i & 1) * 2 + 1][i >> 1] = ldsfrag(pa + 2048); };
                auto rdB = [&](int i) { const int s = i / 6, seg = (i % 6) >> 1, c = i & 1; LAS const char* pb = lds + boff[c] + bslot;
                    s16x4 lo = trread(pb + seg * 8192 + s * 4096), hi = trread(pb + seg * 8192 + s * 4096 + 512);
                    bfr[seg * 2 + c][s] = __builtin_shufflevector(lo, hi, 0, 1, 2, 3, 4, 5, 6, 7); };
                if (TW && TOUCH == 2 && issue) touch(t + PD);
#pragma unroll
                for (int i = 0; i < 16; ++i) {
                    if (issue && (i & 1) == 0 && i / 2 < 7) piece(t + 2, t & 1, s3n, i / 2);
                    if (TW && TOUCH == 3 && issue && i == 13) touch(t + PD);
                    if (i < 4) rdA(i); else rdB(i - 4);
                }
                if (TW && TOUCH == 1 && issue) touch(t + PD);
                s3 = (s3 == 2) ? 0 : s3 + 1;
            };
            auto Mseg = [&]() {
                __builtin_amdgcn_s_setprio(1);
#pragma unroll
                for (int s = 0; s < 2; ++s)
#pragma unroll
                    for (int m = 0; m < 4; ++m)
#pragma unroll
                        for (int n = 0; n < 6; ++n) acc[m][n] = n < 4 ? MFMA16(bfr[n][s], af[m][s], acc[m][n]) : MFMA16(af[m][s], bfr[n][s], acc[m][n]);
                __builtin_amdgcn_s_setprio(0);
            };
            if (TW && TOUCH != 2) { Lseg(0, true); WAITL0(); BAR(); Mseg(); waitv<8>(); BAR(); }
            for (int t = (TW && TOUCH != 2) ? 1 : 0; t < 14; ++t) {
                Lseg(t, true);
                if (H == 1) waitv<10>();
                WAITL0(); BAR();
                Mseg();
                waitv<7 + (TW ? (TOUCH == 2 ? 1 : 2) : 0)>();
                BAR();
            }
            Lseg(14, false); if (H == 1) waitv<3>(); WAITL0(); BAR(); Mseg(); waitv<0>(); BAR();
            Lseg(15, false); WAITL0(); BAR(); Mseg(); if (H == 0) BAR();
        };
        if (half == 0) run(std::integral_constant<int, 0>{}); else run(std::integral_constant<int, 1>{});
    }
    STAMP(2);
    const u64 tagp = (((u64)__builtin_amdgcn_readlane((unsigned)(ew >> 32), 0) << 32) | __builtin_amdgcn_readlane((unsigned)ew, 0)) + TAGK;
    const u64 tagc = (((u64)__builtin_amdgcn_readlane((unsigned)(ew >> 32), 1) << 32) | __builtin_amdgcn_readlane((unsigned)ew, 1)) + TAGK;
    const __amdgpu_buffer_rsrc_t srdW = __builtin_amdgcn_make_buffer_rsrc((void*)a.ws, (short)0, (int)WS_END, 0x00020000);
    const __amdgpu_buffer_rsrc_t srdWo = __builtin_amdgcn_make_buffer_rsrc((void*)(a.wo[br]), (short)0, 1024 * 2048, 0x00020000);
    const int ct = hp & 7, th = hp >> 3;
    const unsigned sW = ct * 256 + (half * 512 + w4 * 8) * 2048;
    auto wo_steps = [&](int s0, int s1) {
        const int ln = lane_id();
        const unsigned voffW0 = (ln >> 4) * 2048 + (((ln & 15) ^ ((ln >> 4) << 1)) << 4), voffW1 = (4 + (ln >> 4)) * 2048 + (((ln & 15) ^ ((4 + (ln >> 4)) << 1)) << 4);
#pragma unroll
        for (int s = s0; s < s1; ++s) { const unsigned d = ldsb + ((s + 8) % 10) * 16384 + wid * 2048; dma16(d, voffW0, srdWo, sW + s * 65536); dma16(d + 1024, voffW1, srdWo, sW + s * 65536); } };
    bf16x8 qf[2][2];
    {
        const unsigned kpos = (g4 >> 1) * 1024 + (g4 & 1) * 512 + r16 * 16;
        const unsigned vpos = 2048 + ((g4 & 1) * 32 + (g4 >> 1) * 16 + r16) * 16;
        const unsigned gkv = (unsigned)WS_KV + me * 65536 + wc * 32768;
        u32x4 kv[2][2], vv[2][2];
#pragma unroll
        for (int mi = 0; mi < 2; ++mi)
#pragma unroll
            for (int s = 0; s < 2; ++s) {
                const f32x4 k0 = acc[2 * mi + s][2], k1 = acc[2 * mi + s][3], v0 = acc[2 * mi + s][4], v1 = acc[2 * mi + s][5];
                kv[mi][s] = (u32x4){cvtpk(k0[0], k0[1]), cvtpk(k0[2], k0[3]), cvtpk(k1[0], k1[1]), cvtpk(k1[2], k1[3])};
                const auto ra = __builtin_amdgcn_permlane32_swap(cvtpk(v0[0], v0[1]), cvtpk(v1[0], v1[1]), false, false);
                const auto rbb = __builtin_amdgcn_permlane32_swap(cvtpk(v0[2], v0[3]), cvtpk(v1[2], v1[3]), false, false);
                vv[mi][s] = (u32x4){ra[0], rbb[0], ra[1], rbb[1]};
                const unsigned blk = wc * 65536 + 32768 + (mi ? 7 - wr : wr) * 4096;
                *(LAS u32x4*)(lds + blk + kpos + s * 256) = kv[mi][s];
                *(LAS u32x4*)(lds + blk + vpos + s * 1024) = vv[mi][s];
            }
        if (rb < 7) {
#pragma unroll
            for (int mi = 0; mi < 2; ++mi)
#pragma unroll
                for (int s = 0; s < 2; ++s) { const unsigned blk = (mi ? 7 - wr : wr) * 4096;
                    __builtin_amdgcn_raw_buffer_store_b128(kv[mi][s], srdW, gkv + blk + kpos + s * 256, 0, 16);
                    __builtin_amdgcn_raw_buffer_store_b128(vv[mi][s], srdW, gkv + blk + vpos + s * 1024, 0, 16); }
        }
#pragma unroll
        for (int mi = 0; mi < 2; ++mi) {
            u32x4 x[2], y0, y1;
#pragma unroll
            for (int s = 0; s < 2; ++s) { const f32x4 q0 = acc[2 * mi + s][0] * QSCALE, q1 = acc[2 * mi + s][1] * QSCALE;
                x[s] = (u32x4){cvtpk(q0[0], q0[1]), cvtpk(q0[2], q0[3]), cvtpk(q1[0], q1[1]), cvtpk(q1[2], q1[3])}; }
#pragma unroll
            for (int i = 0; i < 4; ++i) { const auto u = __builtin_amdgcn_permlane16_swap(x[0][i], x[1][i], false, false);
                const auto v = __builtin_amdgcn_permlane32_swap(u[0], u[1], false, false); y0[i] = v[0]; y1[i] = v[1]; }
            qf[mi][0] = __builtin_bit_cast(bf16x8, y0); qf[mi][1] = __builtin_bit_cast(bf16x8, y1);
        }
    }
    f32x16 o[2], nm[2]; f32x4 l[2];
    const int r32 = lane & 31, hh = lane >> 5;
    LAS const char* kvb = lds + wc * 65536 + lane * 16;
    bf16x8 ones;
    { const short v = ((lane ^ (lane >> 4)) & 1) ? (short)0 : (short)0x3f80;
#pragma unroll
      for (int i = 0; i < 8; ++i) ones[i] = v; }
    auto jmax = [&](const f32x16& s, float m) {
#pragma unroll
        for (int r = 0; r < 16; r += 2) m = max3f(m, s[r], s[r + 1]);
        return m; };
    auto xmax = [&](float m) { const auto r = __builtin_amdgcn_permlane32_swap(__float_as_uint(m), __float_as_uint(m), false, false); return __builtin_fmaxf(__uint_as_float(r[0]), __uint_as_float(r[1])); };
    auto expo = [&](const f32x16& s, bf16x8& p0, bf16x8& p1) {
        float t[16];
#pragma unroll
        for (int r = 0; r < 16; ++r) t[r] = __builtin_amdgcn_exp2f(s[r]);
        p0 = pack8(t[0], t[1], t[2], t[3], t[4], t[5], t[6], t[7]); p1 = pack8(t[8], t[9], t[10], t[11], t[12], t[13], t[14], t[15]); };
    auto trimask = [&](bool lower) { f32x16 c;
#pragma unroll
        for (int r = 0; r < 16; ++r) c[r] = (((r & 3) + 8 * (r >> 2) + 4 * hh <= r32) == lower) ? 0.f : -INFINITY;
        return c; };
    {
        const f32x16 mk = trimask(true);
        f32x16 zero16, s[2];
#pragma unroll
        for (int r = 0; r < 16; ++r) zero16[r] = 0.f;
#pragma unroll
        for (int d = 0; d < 2; ++d) {
            LAS const char* p = kvb + (8 + (d ? 7 - wr : wr)) * 4096;
            s[d] = MFMA32(ldsfrag(p), qf[d][0], mk); s[d] = MFMA32(ldsfrag(p + 1024), qf[d][1], s[d]);
        }
        wo_steps(0, 2);
        const float m0 = xmax(jmax(s[0], -INFINITY)), m1 = xmax(jmax(s[1], -INFINITY));
#pragma unroll
        for (int d = 0; d < 2; ++d) {
            LAS const char* p = kvb + (8 + (d ? 7 - wr : wr)) * 4096;
            const float m = d ? m1 : m0;
#pragma unroll
            for (int r = 0; r < 16; ++r) { nm[d][r] = -m; s[d][r] = fsub_s(s[d][r], m); }
            bf16x8 p0, p1;
            expo(s[d], p0, p1);
            o[d] = MFMA32(ldsfrag(p + 2048), p0, zero16); l[d] = MFMA16(ones, p0, ((f32x4){0.f, 0.f, 0.f, 0.f}));
            o[d] = MFMA32(ldsfrag(p + 3072), p1, o[d]); l[d] = MFMA16(ones, p1, l[d]);
        }
    }
    waitv<4>(); WAITL0(); BAR();
    if (wid == 0 && lane == 0 && rb < 7) __hip_atomic_store((GAS u64*)(a.ws + WS_FKV + (size_t)me * 128), tagp, __ATOMIC_RELAXED, __HIP_MEMORY_SCOPE_AGENT);
    STAMP(3);
    auto attention = [&](auto WC_) {
        constexpr int WR = decltype(WC_)::value;
        u32x4 pld[8];
        auto jobs = [&](auto PART) {
            constexpr int P2 = decltype(PART)::value;
            constexpr int NJ = P2 ? 9 : 7;
            auto TOF = [](int j) { return P2 ? (j < 7 ? (j < 7 - WR ? 0 : 1) : j - 7) : (j < WR ? 0 : 1); };
            auto BOF = [](int j) { return P2 ? (j < 7 ? (j < 7 - WR ? WR + 1 + j : j + 1) : (j == 7 ? WR : 7 - WR)) : 8 + (j < WR ? j : j - WR); };
            f32x16 S[2], cm; bf16x8 kf[2][2], vf[2][2]; u32x4 p0, p1;
            auto rdk = [&](int j) { kf[j & 1][0] = ldsfrag(kvb + BOF(j) * 4096); kf[j & 1][1] = ldsfrag(kvb + BOF(j) * 4096 + 1024); };
            auto cof = [&](int j) -> const f32x16& { return (P2 && j >= 7) ? cm : nm[TOF(j)]; };
            auto mkc = [&](int j) { const f32x16 mi = trimask(false);
#pragma unroll
                for (int r = 0; r < 16; ++r) cm[r] = fadd_s(mi[r], nm[TOF(j)][r]); };
            rdk(0); rdk(1); vf[0][0] = ldsfrag(kvb + BOF(0) * 4096 + 2048);
            S[0] = MFMA32(kf[0][0], qf[TOF(0)][0], cof(0)); S[0] = MFMA32(kf[0][1], qf[TOF(0)][1], S[0]);
            SBAR();
#pragma unroll
            for (int j = 0; j < NJ; ++j) {
                const int T = TOF(j), TP = j > 0 ? TOF(j - 1) : 0, TN = j + 1 < NJ ? TOF(j + 1) : 0;
                f32x16& s = S[j & 1];
#define EX(i) s[i] = __builtin_amdgcn_exp2f(s[i])
                if (P2 && j + 1 >= 7 && j + 1 < NJ) { mkc(j + 1); PIN(cm); }
                if (j + 1 < NJ) S[(j + 1) & 1] = MFMA32(kf[(j + 1) & 1][0], qf[TN][0], cof(j + 1));
                SBAR();
                vf[j & 1][1] = ldsfrag(kvb + BOF(j) * 4096 + 3072);
                EX(0); EX(1); EX(2); PIN(s); SBAR();
                if (j > 0) o[TP] = MFMA32(vf[(j - 1) & 1][1], __builtin_bit_cast(bf16x8, p1), o[TP]);
                SBAR();
                EX(3); EX(4); EX(5); PIN(s); SBAR();
                if (j > 0) l[TP] = MFMA16(ones, __builtin_bit_cast(bf16x8, p1), l[TP]);
                SBAR();
                EX(6); EX(7);
                p0 = (u32x4){cvtpk(s[0], s[1]), cvtpk(s[2], s[3]), cvtpk(s[4], s[5]), cvtpk(s[6], s[7])}; PIN(p0); SBAR();
                if (j + 1 < NJ) S[(j + 1) & 1] = MFMA32(kf[(j + 1) & 1][1], qf[TN][1], S[(j + 1) & 1]);
                SBAR();
                if (j + 1 < NJ) vf[(j + 1) & 1][0] = ldsfrag(kvb + BOF(j + 1) * 4096 + 2048);
                EX(8); EX(9); EX(10); PIN(s); SBAR();
                o[T] = MFMA32(vf[j & 1][0], __builtin_bit_cast(bf16x8, p0), o[T]);
                SBAR();
                if (j + 2 < NJ) rdk(j + 2);
                EX(11); EX(12); EX(13); PIN(s); SBAR();
                l[T] = MFMA16(ones, __builtin_bit_cast(bf16x8, p0), l[T]);
                SBAR();
                EX(14); EX(15);
                p1 = (u32x4){cvtpk(s[8], s[9]), cvtpk(s[10], s[11]), cvtpk(s[12], s[13]), cvtpk(s[14], s[15])}; PIN(p1); SBAR();
#undef EX
                if (j == NJ - 1) { o[T] = MFMA32(vf[j & 1][1], __builtin_bit_cast(bf16x8, p1), o[T]); l[T] = MFMA16(ones, __builtin_bit_cast(bf16x8, p1), l[T]); }
                if (!P2 && j == POLJ && rb > 0) {
                    if (WR == 0 && wid == 0) {
                        GAS u64* f = (GAS u64*)(a.ws + WS_FKV + (size_t)(me - 16) * 128);
#pragma nounroll
                        for (int spin = 0; spin < SPIN_CAP; ++spin) {
                            const u64 v = __hip_atomic_load(f, __ATOMIC_RELAXED, __HIP_MEMORY_SCOPE_AGENT);
                            if (rfl((unsigned)v) == (unsigned)tagc && rfl((unsigned)(v >> 32)) == (unsigned)(tagc >> 32)) break;
                            __builtin_amdgcn_s_sleep(1);
                        }
                        if (lane == 0) __hip_atomic_store((GAS u64*)(a.ws + WS_EKV + (size_t)(me - 16) * 128), tagc, __ATOMIC_RELAXED, __HIP_MEMORY_SCOPE_AGENT);
                    }
                    BAR();
                    STAMP(5);
#pragma unroll
                    for (int k = 0; k < 8; ++k) pld[k] = __builtin_amdgcn_raw_buffer_load_b128(srdW, (unsigned)WS_KV + (me - 16) * 65536 + wid * 8192 + k * 1024 + lane * 16, 0, 16);
                    SBAR();
                }
            }
        };
        jobs(std::integral_constant<int, 0>{});
        STAMP(4);
        if (rb > 0) {
#pragma unroll
            for (int i = 0; i < 8; ++i) *(LAS u32x4*)(lds + half * 65536 + w4 * 8192 + i * 1024 + lane * 16) = pld[i];
            WAITL0(); BAR();
            STAMP(6);
            jobs(std::integral_constant<int, 1>{});
        }
    };
#define ATT(W) attention(std::integral_constant<int, W>{})
    if (wr == 0) ATT(0); else if (wr == 1) ATT(1); else if (wr == 2) ATT(2); else ATT(3);
#pragma unroll
    for (int d = 0; d < 2; ++d) {
        const int gt = d ? 7 - wr : wr;
        float lt = (lane & 16) ? l[d][1] : l[d][0];
        if (__builtin_expect(__any(!(lt <= LSAFE)), 0)) {
            const f32x16 mlo = trimask(true), mhi = trimask(false);
            const int b0 = rb > 0 ? gt : 8, b1 = 8 + gt;
            auto sc = [&](int b, float ref) { f32x16 c;
#pragma unroll
                for (int r = 0; r < 16; ++r) c[r] = (b == b1 ? mlo[r] : b == gt ? mhi[r] : 0.f) - ref;
                c = MFMA32(ldsfrag(kvb + b * 4096), qf[d][0], c); return MFMA32(ldsfrag(kvb + b * 4096 + 1024), qf[d][1], c); };
            float m = -INFINITY;
#pragma nounroll
            for (int b = b0; b <= b1; ++b) m = jmax(sc(b, 0.f), m);
            m = xmax(m);
            f32x4 ls = {0.f, 0.f, 0.f, 0.f};
#pragma unroll
            for (int r = 0; r < 16; ++r) o[d][r] = 0.f;
#pragma nounroll
            for (int b = b0; b <= b1; ++b) { bf16x8 p0, p1; expo(sc(b, m), p0, p1);
                o[d] = MFMA32(ldsfrag(kvb + b * 4096 + 2048), p0, o[d]); o[d] = MFMA32(ldsfrag(kvb + b * 4096 + 3072), p1, o[d]);
                ls = MFMA16(ones, p0, ls); ls = MFMA16(ones, p1, ls); }
            lt = (lane & 16) ? ls[1] : ls[0];
        }
        const float inv = __builtin_amdgcn_rcpf(lt);
        const unsigned base = (unsigned)WS_O + (((br * 128 + rb * 16 + gt * 2 + (r32 >> 4)) * 32) + hp * 2 + wc) * 1024 + hh * 256 + (r32 & 15) * 16;
#pragma unroll
        for (int e = 0; e < 2; ++e) {
            const f32x16& x = o[d];
            const u32x4 v = {cvtpk(fmul_s(x[8 * e], inv), fmul_s(x[8 * e + 1], inv)), cvtpk(fmul_s(x[8 * e + 2], inv), fmul_s(x[8 * e + 3], inv)), cvtpk(fmul_s(x[8 * e + 4], inv), fmul_s(x[8 * e + 5], inv)), cvtpk(fmul_s(x[8 * e + 6], inv), fmul_s(x[8 * e + 7], inv))};
            __builtin_amdgcn_raw_buffer_store_b128(v, srdW, base + e * 512, 0, 16);
        }
    }
    STAMP(7);
    auto phase3 = [&](auto HC) {
        constexpr int H = decltype(HC)::value;
        const int lane = lane_id(), r16 = lane & 15, g4 = lane >> 4, q4 = (lane & 15) >> 2, p4 = lane & 3;
        GAS char* ep = (GAS char*)a.ws + WS_EO + (lane < 16 ? (size_t)(grp + lane) * 128 + hp * 8 : (size_t)me * 128 + (lane & 15) * 8);
        u64 eo;
        asm volatile("global_load_dwordx2 %0, %1, off sc1" : "=v"(eo) : "v"(ep) : "memory");
        unsigned woff[8];
        { const int row = 16 * (g4 >> 1) + 4 * (g4 & 1) + q4;
#pragma unroll
          for (int n = 0; n < 8; ++n) woff[n] = 131072 + H * 8192 + row * 256 + ((n ^ (row & 7)) << 5) + p4 * 8; }
        bf16x8 wf[2][8];
        auto wread = [&](int s, int n) {
            if (s == 2 || s == 12) { woff[n] -= 131072; PIN(woff[n]); } else if (s == 6 || s == 10) { woff[n] += 65536; PIN(woff[n]); }
            LAS const char* p = lds + woff[n] + (((s + 8) % 10) & 3) * 16384;
            const s16x4 lo = trread(p), hi = trread(p + 2048); wf[s & 1][n] = __builtin_shufflevector(lo, hi, 0, 1, 2, 3, 4, 5, 6, 7); };
        waitv<5>(); WAITL0(); BAR();
        wo_steps(2, 2 + WPRE);
#pragma unroll
        for (int n = 0; n < 8; ++n) { wread(0, n); wread(1, n); }
        waitv<2 * WPRE>(); PIN(eo); WAITL0(); BAR();
        if (H == 0 && wid == 0) {
            const u64 tag = eo + TAGK;
            if (lane < 16) __hip_atomic_store((GAS u64*)(ep - WS_EO + WS_FO), tag, __ATOMIC_RELAXED, __HIP_MEMORY_SCOPE_AGENT);
        }
        wo_steps(2 + WPRE, 12);
        if (H == 0 && wid == 0) {
            const u64 tag = eo + TAGK;
            GAS u64* f = (GAS u64*)(a.ws + WS_FO + (size_t)me * 128 + (lane & 15) * 8);
            const bool mine = lane >= 16 && lane < 32;
#pragma nounroll
            for (int spin = 0; spin < SPIN_CAP; ++spin) {
                const u64 v = __hip_atomic_load(f, __ATOMIC_RELAXED, __HIP_MEMORY_SCOPE_AGENT);
                if (__all(!mine || v == tag)) break;
                __builtin_amdgcn_s_sleep(1);
            }
            if (mine) __hip_atomic_store((GAS u64*)ep, tag, __ATOMIC_RELAXED, __HIP_MEMORY_SCOPE_AGENT);
        }
        waitv<0>(); BAR();
        STAMP(8);
        bf16x8 of[OW][2];
        const unsigned obase = (unsigned)WS_O + ((br * 128 + rb * 16 + th * 8 + w4 * 2) * 32 + H * 16) * 1024 + lane * 16;
        auto oload = [&](int s, int t) { of[s % OW][t] = __builtin_bit_cast(bf16x8, __builtin_amdgcn_raw_buffer_load_b128(srdW, obase + t * 32768 + s * 1024, 0, 16)); };
#pragma unroll
        for (int s = 0; s < OW - 1; ++s) { oload(s, 0); oload(s, 1); SBAR(); }
        f32x4 c2[2][8];
#pragma unroll
        for (int t = 0; t < 2; ++t)
#pragma unroll
            for (int n = 0; n < 8; ++n) c2[t][n] = (f32x4){0.f, 0.f, 0.f, 0.f};
        const unsigned voffW0 = (lane >> 4) * 2048 + (((lane & 15) ^ ((lane >> 4) << 1)) << 4), voffW1 = (4 + (lane >> 4)) * 2048 + (((lane & 15) ^ ((4 + (lane >> 4)) << 1)) << 4);
        {
            auto step = [&](int s) {
#pragma unroll
                for (int n = 0; n < 8; ++n) {
                    c2[0][n] = MFMA16(wf[s & 1][n], of[s % OW][0], c2[0][n]); c2[1][n] = MFMA16(wf[s & 1][n], of[s % OW][1], c2[1][n]);
                    SBAR();
                    if (s + 2 < 16) wread(s + 2, n);
                    if (n == PA0 + 2 * H && s + OW - 1 < 16) oload(s + OW - 1, 0);
                    if (n == PA0 + 4 + 2 * H && s + OW - 1 < 16) oload(s + OW - 1, 1);
                    if (s >= 4 && s < 8 && (n == PA0 + 2 - 2 * H || n == PA0 + 6 - 2 * H)) dma16(ldsb + ((s + 16) % 10) * 16384 + wid * 2048 + (n >= 4) * 1024, n >= 4 ? voffW1 : voffW0, srdWo, sW + (s + 8) * 65536);
                    SBAR();
                } };
#pragma unroll
            for (int s = 0; s < 4; ++s) { step(s); if (s == 0) STAMP(11); }
            STAMP(12);
            WAITL0(); BAR();
#pragma unroll
            for (int s = 4; s < 10; ++s) step(s);
            STAMP(13);
            waitv<4>(); BAR();
            STAMP(14);
#pragma unroll
            for (int s = 10; s < 16; ++s) step(s);
            STAMP(9);
            BAR();
#pragma unroll
            for (int t = 0; t < 2; ++t)
#pragma unroll
                for (int n = 0; n < 4; ++n) *(LAS f32x4*)(lds + wid * 8192 + (t * 4 + n) * 1024 + lane * 16) = c2[t][4 - 4 * H + n];
            WAITL0(); BAR();
            STAMP(15);
            GAS u16* outp = (GAS u16*)a.out + (size_t)(rb * 256 + th * 128 + w4 * 32 + r16) * 2048 + br * 1024 + ct * 128 + H * 64 + (g4 & 1) * 16 + (g4 >> 1) * 8;
#pragma unroll
            for (int t = 0; t < 2; ++t)
#pragma unroll
                for (int n = 0; n < 4; n += 2) {
                    const f32x4 x = c2[t][4 * H + n] + *(LAS const f32x4*)(lds + (wid ^ 4) * 8192 + (t * 4 + n) * 1024 + lane * 16);
                    const f32x4 y = c2[t][4 * H + n + 1] + *(LAS const f32x4*)(lds + (wid ^ 4) * 8192 + (t * 4 + n + 1) * 1024 + lane * 16);
                    const auto u0 = __builtin_amdgcn_permlane16_swap(cvtpk(x[0], x[1]), cvtpk(y[0], y[1]), false, false);
                    const auto u1 = __builtin_amdgcn_permlane16_swap(cvtpk(x[2], x[3]), cvtpk(y[2], y[3]), false, false);
                    { const u32x4 vv = {u0[0], u1[0], u0[1], u1[1]}; GAS u16* pp = outp + (size_t)t * 16 * 2048 + n * 16; asm volatile("global_store_dwordx4 %0, %1, off sc0 sc1\n\ts_nop 1" :: "v"(pp), "v"(vv) : "memory"); }
                }
        }
    };
    if (half == 0) phase3(std::integral_constant<int, 0>{}); else phase3(std::integral_constant<int, 1>{});
    STAMP(10);
}

extern "C" void kernel_launch(void* const* d_in, const int* in_sizes, int n_in, void* d_out, int out_size, void* d_ws, size_t ws_size, hipStream_t stream) {
    Args a;
    a.x[0] = (const u16*)d_in[0]; a.w[0] = (const u16*)d_in[1]; a.wo[0] = (const u16*)d_in[2];
    a.x[1] = (const u16*)d_in[3]; a.w[1] = (const u16*)d_in[4]; a.wo[1] = (const u16*)d_in[5];
    a.out = (u16*)d_out; a.ws = (char*)d_ws;
    fused_kernel<<<256, 512, 0, stream>>>(a);
}
```

```cpp
#define MA 1
#define MB 2
#include <hip/hip_runtime.h>
#include <hip/hip_bf16.h>
#include <type_traits>
typedef short bf16x8 __attribute__((ext_vector_type(8)));
typedef short s16x4 __attribute__((ext_vector_type(4)));
typedef float f32x16 __attribute__((ext_vector_type(16)));
typedef float f32x4 __attribute__((ext_vector_type(4)));
typedef float f32x2 __attribute__((ext_vector_type(2)));
typedef __bf16 bf16x2 __attribute__((ext_vector_type(2)));
typedef unsigned u32x4 __attribute__((ext_vector_type(4)));
typedef unsigned short u16;
typedef unsigned long long u64;
#define LAS __attribute__((address_space(3)))
#define GAS __attribute__((address_space(1)))
#define DI __device__ __forceinline__
#define MFMA16(a, b, c) __builtin_amdgcn_mfma_f32_16x16x32_bf16((a), (b), (c), 0, 0, 0)
#define MFMA32(a, b, c) __builtin_amdgcn_mfma_f32_32x32x16_bf16((a), (b), (c), 0, 0, 0)
#define SBAR() __builtin_amdgcn_sched_barrier(0)
#define BAR() do { SBAR(); asm volatile("s_barrier" ::: "memory"); SBAR(); } while (0)
#define PIN(x) asm volatile("" : "+v"(x))
#define WAITL0() __builtin_amdgcn_s_waitcnt(0xC07F)
template <int N> DI void waitv() { asm volatile("s_waitcnt vmcnt(%0)" :: "i"(N) : "memory"); }
DI unsigned rfl(unsigned x) { return __builtin_amdgcn_readfirstlane(x); }
DI int lane_id() { int l; asm volatile("v_mbcnt_lo_u32_b32 %0, -1, 0\n\tv_mbcnt_hi_u32_b32 %0, -1, %0" : "=v"(l)); return l; }
DI unsigned cvtpk(float lo, float hi) { f32x2 v = {lo, hi}; bf16x2 b = __builtin_convertvector(v, bf16x2); return __builtin_bit_cast(unsigned, b); }
DI void dma16(unsigned m0v, unsigned voff, __amdgpu_buffer_rsrc_t srd, unsigned soff) {
    asm volatile("s_mov_b32 m0, %0\n\ts_nop 0\n\tbuffer_load_dwordx4 %1, %2, %3 offen lds" :: "s"(m0v), "v"(voff), "s"(srd), "s"(soff) : "memory");
}
DI s16x4 trread(LAS const char* p) { return __builtin_amdgcn_ds_read_tr16_b64_v4i16((LAS s16x4*)p); }
DI bf16x8 ldsfrag(LAS const char* p) { return *(LAS const bf16x8*)p; }
DI float max3f(float a, float b, float c) { return __builtin_fmaxf(__builtin_fmaxf(a, b), c); }
DI float fadd_s(float a, float b) { return a + b; }
DI float fsub_s(float a, float b) { return a - b; }
DI float fmul_s(float a, float b) { return a * b; }
DI bf16x8 pack8(float a, float b, float c, float d, float e, float f, float g, float h) { u32x4 v = {cvtpk(a, b), cvtpk(c, d), cvtpk(e, f), cvtpk(g, h)}; return __builtin_bit_cast(bf16x8, v); }

constexpr int LDS_A0 = 0, LDS_A1 = 32768, LDS_B = 81920, A_SLOT = 16384, B_SLOT = 24576;
constexpr int LDS_TOTAL = 163840;
constexpr size_t WS_FKV = 0, WS_EKV = 32768, WS_FO = 65536, WS_EO = 98304, WS_KV = 1 << 20, WS_O = 17 << 20, WS_END = 25 << 20, WS_ST = 32 << 20;
constexpr u64 TAGK = 0x9E3779B97F4A7C15ull;
constexpr int SPIN_CAP = 400000;
constexpr float QSCALE = 0.17677669529663687f * 1.4426950408889634f;

#define PUBJ 3
#define POLJ 1
#define OW 5
#define PA0 1
#define WPRE 4
#define LSAFE 1.152921504606847e18f
struct Args { const u16* x[2]; const u16* w[2]; const u16* wo[2]; u16* out; char* ws; };

#define STAMP(i) do { if ((i) == MA) mta = __builtin_amdgcn_s_memtime(); if ((i) == MB) mtb = __builtin_amdgcn_s_memtime(); } while (0)
__global__ __launch_bounds__(512, 2) void fused_kernel(Args a) {
    __shared__ __attribute__((aligned(16))) char smem[LDS_TOTAL];
    LAS char* lds = (LAS char*)smem;
    const unsigned ldsb = (unsigned)(size_t)smem;
    const int tid = threadIdx.x, lane = tid & 63;
    const int wid = rfl(tid >> 6), half = wid >> 2, w4 = wid & 3;
    const int bid = blockIdx.x, g = bid & 7, j = bid >> 3;
    const int br = g >> 2, rb = ((g >> 1) & 1) * 4 + (j & 3), hp = (g & 1) * 8 + (j >> 2);
    const int me = (br * 8 + rb) * 16 + hp, grp = me & ~15;
    u64 mta = 0, mtb = 0;
    STAMP(0);
    u64 ew = __hip_atomic_load((GAS u64*)(a.ws + WS_EKV + (size_t)(lane == 0 || rb == 0 ? me : me - 16) * 128), __ATOMIC_RELAXED, __HIP_MEMORY_SCOPE_AGENT);
    const __amdgpu_buffer_rsrc_t srdA = __builtin_amdgcn_make_buffer_rsrc((void*)(a.x[br] + (size_t)rb * 256 * 1024), (short)0, 256 * 2048, 0x00020000);
    const __amdgpu_buffer_rsrc_t srdB = __builtin_amdgcn_make_buffer_rsrc((void*)(a.w[br]), (short)0, 1024 * 6144, 0x00020000);
    const int wr = 2 * half + (w4 >> 1), wc = w4 & 1;
    const int r16 = lane & 15, g4 = lane >> 4, q4 = (lane & 15) >> 2, p4 = lane & 3;
    f32x4 acc[4][6];
    {
        const unsigned voffE = (lane >> 3) * 2048 + (((lane & 7) ^ (lane >> 4)) << 4);
        const unsigned voffO = (8 + (lane >> 3)) * 2048 + (((lane & 7) ^ (4 + (lane >> 4))) << 4);
        const unsigned voffB0 = (lane >> 3) * 6144 + (((lane & 7) ^ (((lane >> 4) & 1) << 1)) << 4);
        const unsigned voffB1 = (lane >> 3) * 6144 + (((lane & 7) ^ ((((lane >> 4) & 1) << 1) | 4)) << 4);
        const int gt0 = (w4 & 1) ? 7 - (w4 >> 1) : (w4 >> 1);
        const int gt1 = (w4 & 1) ? 5 - (w4 >> 1) : 2 + (w4 >> 1);
        const unsigned sA0 = gt0 * 32 * 2048, sA1 = gt1 * 32 * 2048;
        const unsigned sB = hp * 128 + (2 * w4) * 8 * 6144;
        unsigned aoff[2], boff[2];
#pragma unroll
        for (int s = 0; s < 2; ++s) aoff[s] = (half ? LDS_A1 : LDS_A0) + ((w4 >> 1) * 64 + r16) * 128 + (((4 * s + g4) ^ ((r16 >> 1) & 7)) << 4);
#pragma unroll
        for (int c = 0; c < 2; ++c) boff[c] = LDS_B + (8 * g4 + q4) * 128 + (((wc * 2 + c) ^ ((q4 >> 1) | ((g4 & 1) << 1))) << 5) + p4 * 8;
#pragma unroll
        for (int i = 0; i < 4; ++i)
#pragma unroll
            for (int s = 0; s < 6; ++s) acc[i][s] = (f32x4){0.f, 0.f, 0.f, 0.f};
        bf16x8 af[4][2], bfr[6][2];
        auto run = [&](auto HC) {
            constexpr int H = decltype(HC)::value;
            auto piece = [&](int kt, int s2, int s3i, int i) {
                const unsigned soB = sB + kt * (64 * 6144), dB = ldsb + LDS_B + s3i * B_SLOT + w4 * 2048;
                const unsigned soA0 = sA0 + kt * 128, dA0 = ldsb + LDS_A0 + s2 * A_SLOT + w4 * 4096;
                const unsigned soA1 = sA1 + kt * 128, dA1 = ldsb + LDS_A1 + s3i * A_SLOT + w4 * 4096;
                if (H == 0) {
                    if (i == 0) dma16(dA1 + 3 * 1024, voffO, srdA, soA1 + 32768);
                    else { const int k = i - 1; dma16(dB + (k >> 1) * 8192 + (k & 1) * 1024, (k & 1) ? voffB1 : voffB0, srdB, soB + (k >> 1) * 2048 + (k & 1) * 8 * 6144); }
                } else dma16((i < 4 ? dA0 : dA1) + (i & 3) * 1024, (i & 1) ? voffO : voffE, srdA, (i < 4 ? soA0 : soA1) + ((i >> 1) & 1) * 32768);
            };
#pragma unroll
            for (int i = 0; i < 7; ++i) piece(0, 0, 0, i);
#pragma unroll
            for (int i = 0; i < 7; ++i) piece(1, 1, 1, i);
#pragma unroll
            for (int i = 0; i < 4; ++i) asm volatile("" : "+v"(acc[i][0]), "+v"(acc[i][1]), "+v"(acc[i][2]), "+v"(acc[i][3]), "+v"(acc[i][4]), "+v"(acc[i][5]));
            if (H == 0) waitv<7>(); else waitv<10>();
            BAR();
            if (H == 1) { waitv<7>(); BAR(); }
            STAMP(1);
            int s3 = 0;
            auto Lseg = [&](int t, bool issue) {
                const unsigned aslot = H ? s3 * A_SLOT : (t & 1) * A_SLOT;
                const unsigned bslot = s3 * B_SLOT;
                const int s3n = (s3 == 0) ? 2 : s3 - 1;
                auto rdA = [&](int i) { LAS const char* pa = lds + aoff[i >> 1] + aslot + (i & 1) * 4096; af[(i & 1) * 2][i >> 1] = ldsfrag(pa); af[(i & 1) * 2 + 1][i >> 1] = ldsfrag(pa + 2048); };
                auto rdB = [&](int i) { const int s = i / 6, seg = (i % 6) >> 1, c = i & 1; LAS const char* pb = lds + boff[c] + bslot;
                    s16x4 lo = trread(pb + seg * 8192 + s * 4096), hi = trread(pb + seg * 8192 + s * 4096 + 512);
                    bfr[seg * 2 + c][s] = __builtin_shufflevector(lo, hi, 0, 1, 2, 3, 4, 5, 6, 7); };
#pragma unroll
                for (int i = 0; i < 16; ++i) {
                    if (issue && (i & 1) == 0 && i / 2 < 7) piece(t + 2, t & 1, s3n, i / 2);
                    if (i < 4) rdA(i); else rdB(i - 4);
                }
                s3 = (s3 == 2) ? 0 : s3 + 1;
            };
            auto Mseg = [&]() {
                __builtin_amdgcn_s_setprio(1);
#pragma unroll
                for (int s = 0; s < 2; ++s)
#pragma unroll
                    for (int m = 0; m < 4; ++m)
#pragma unroll
                        for (int n = 0; n < 6; ++n) acc[m][n] = n < 4 ? MFMA16(bfr[n][s], af[m][s], acc[m][n]) : MFMA16(af[m][s], bfr[n][s], acc[m][n]);
                __builtin_amdgcn_s_setprio(0);
            };
            for (int t = 0; t < 14; ++t) {
                Lseg(t, true);
                if (H == 1) waitv<10>();
                WAITL0(); BAR();
                Mseg();
                waitv<7>();
                BAR();
            }
            Lseg(14, false); if (H == 1) waitv<3>(); WAITL0(); BAR(); Mseg(); waitv<0>(); BAR();
            Lseg(15, false); WAITL0(); BAR(); Mseg(); if (H == 0) BAR();
        };
        if (half == 0) run(std::integral_constant<int, 0>{}); else run(std::integral_constant<int, 1>{});
    }
    STAMP(2);
    const u64 tagp = (((u64)__builtin_amdgcn_readlane((unsigned)(ew >> 32), 0) << 32) | __builtin_amdgcn_readlane((unsigned)ew, 0)) + TAGK;
    const u64 tagc = (((u64)__builtin_amdgcn_readlane((unsigned)(ew >> 32), 1) << 32) | __builtin_amdgcn_readlane((unsigned)ew, 1)) + TAGK;
    const __amdgpu_buffer_rsrc_t srdW = __builtin_amdgcn_make_buffer_rsrc((void*)a.ws, (short)0, (int)WS_END, 0x00020000);
    const __amdgpu_buffer_rsrc_t srdWo = __builtin_amdgcn_make_buffer_rsrc((void*)(a.wo[br]), (short)0, 1024 * 2048, 0x00020000);
    const int ct = hp & 7, th = hp >> 3;
    const unsigned sW = ct * 256 + (half * 512 + w4 * 8) * 2048;
    auto wo_steps = [&](int s0, int s1) {
        const int ln = lane_id();
        const unsigned voffW0 = (ln >> 4) * 2048 + (((ln & 15) ^ ((ln >> 4) << 1)) << 4), voffW1 = (4 + (ln >> 4)) * 2048 + (((ln & 15) ^ ((4 + (ln >> 4)) << 1)) << 4);
#pragma unroll
        for (int s = s0; s < s1; ++s) { const unsigned d = ldsb + ((s + 8) % 10) * 16384 + wid * 2048; dma16(d, voffW0, srdWo, sW + s * 65536); dma16(d + 1024, voffW1, srdWo, sW + s * 65536); } };
    bf16x8 qf[2][2];
    {
        const unsigned kpos = (g4 >> 1) * 1024 + (g4 & 1) * 512 + r16 * 16;
        const unsigned vpos = 2048 + ((g4 & 1) * 32 + (g4 >> 1) * 16 + r16) * 16;
        const unsigned gkv = (unsigned)WS_KV + me * 65536 + wc * 32768;
        u32x4 kv[2][2], vv[2][2];
#pragma unroll
        for (int mi = 0; mi < 2; ++mi)
#pragma unroll
            for (int s = 0; s < 2; ++s) {
                const f32x4 k0 = acc[2 * mi + s][2], k1 = acc[2 * mi + s][3], v0 = acc[2 * mi + s][4], v1 = acc[2 * mi + s][5];
                kv[mi][s] = (u32x4){cvtpk(k0[0], k0[1]), cvtpk(k0[2], k0[3]), cvtpk(k1[0], k1[1]), cvtpk(k1[2], k1[3])};
                const auto ra = __builtin_amdgcn_permlane32_swap(cvtpk(v0[0], v0[1]), cvtpk(v1[0], v1[1]), false, false);
                const auto rbb = __builtin_amdgcn_permlane32_swap(cvtpk(v0[2], v0[3]), cvtpk(v1[2], v1[3]), false, false);
                vv[mi][s] = (u32x4){ra[0], rbb[0], ra[1], rbb[1]};
                const unsigned blk = wc * 65536 + 32768 + (mi ? 7 - wr : wr) * 4096;
                *(LAS u32x4*)(lds + blk + kpos + s * 256) = kv[mi][s];
                *(LAS u32x4*)(lds + blk + vpos + s * 1024) = vv[mi][s];
            }
        if (rb < 7) {
#pragma unroll
            for (int mi = 0; mi < 2; ++mi)
#pragma unroll
                for (int s = 0; s < 2; ++s) { const unsigned blk = (mi ? 7 - wr : wr) * 4096;
                    __builtin_amdgcn_raw_buffer_store_b128(kv[mi][s], srdW, gkv + blk + kpos + s * 256, 0, 16);
                    __builtin_amdgcn_raw_buffer_store_b128(vv[mi][s], srdW, gkv + blk + vpos + s * 1024, 0, 16); }
        }
#pragma unroll
        for (int mi = 0; mi < 2; ++mi) {
            u32x4 x[2], y0, y1;
#pragma unroll
            for (int s = 0; s < 2; ++s) { const f32x4 q0 = acc[2 * mi + s][0] * QSCALE, q1 = acc[2 * mi + s][1] * QSCALE;
                x[s] = (u32x4){cvtpk(q0[0], q0[1]), cvtpk(q0[2], q0[3]), cvtpk(q1[0], q1[1]), cvtpk(q1[2], q1[3])}; }
#pragma unroll
            for (int i = 0; i < 4; ++i) { const auto u = __builtin_amdgcn_permlane16_swap(x[0][i], x[1][i], false, false);
                const auto v = __builtin_amdgcn_permlane32_swap(u[0], u[1], false, false); y0[i] = v[0]; y1[i] = v[1]; }
            qf[mi][0] = __builtin_bit_cast(bf16x8, y0); qf[mi][1] = __builtin_bit_cast(bf16x8, y1);
        }
    }
    f32x16 o[2], nm[2]; f32x4 l[2];
    const int r32 = lane & 31, hh = lane >> 5;
    LAS const char* kvb = lds + wc * 65536 + lane * 16;
    bf16x8 ones;
    { const short v = ((lane ^ (lane >> 4)) & 1) ? (short)0 : (short)0x3f80;
#pragma unroll
      for (int i = 0; i < 8; ++i) ones[i] = v; }
    auto jmax = [&](const f32x16& s, float m) {
#pragma unroll
        for (int r = 0; r < 16; r += 2) m = max3f(m, s[r], s[r + 1]);
        return m; };
    auto xmax = [&](float m) { const auto r = __builtin_amdgcn_permlane32_swap(__float_as_uint(m), __float_as_uint(m), false, false); return __builtin_fmaxf(__uint_as_float(r[0]), __uint_as_float(r[1])); };
    auto expo = [&](const f32x16& s, bf16x8& p0, bf16x8& p1) {
        float t[16];
#pragma unroll
        for (int r = 0; r < 16; ++r) t[r] = __builtin_amdgcn_exp2f(s[r]);
        p0 = pack8(t[0], t[1], t[2], t[3], t[4], t[5], t[6], t[7]); p1 = pack8(t[8], t[9], t[10], t[11], t[12], t[13], t[14], t[15]); };
    auto trimask = [&](bool lower) { f32x16 c;
#pragma unroll
        for (int r = 0; r < 16; ++r) c[r] = (((r & 3) + 8 * (r >> 2) + 4 * hh <= r32) == lower) ? 0.f : -INFINITY;
        return c; };
    {
        const f32x16 mk = trimask(true);
        f32x16 zero16, s[2];
#pragma unroll
        for (int r = 0; r < 16; ++r) zero16[r] = 0.f;
#pragma unroll
        for (int d = 0; d < 2; ++d) {
            LAS const char* p = kvb + (8 + (d ? 7 - wr : wr)) * 4096;
            s[d] = MFMA32(ldsfrag(p), qf[d][0], mk); s[d] = MFMA32(ldsfrag(p + 1024), qf[d][1], s[d]);
        }
        wo_steps(0, 2);
        const float m0 = xmax(jmax(s[0], -INFINITY)), m1 = xmax(jmax(s[1], -INFINITY));
#pragma unroll
        for (int d = 0; d < 2; ++d) {
            LAS const char* p = kvb + (8 + (d ? 7 - wr : wr)) * 4096;
            const float m = d ? m1 : m0;
#pragma unroll
            for (int r = 0; r < 16; ++r) { nm[d][r] = -m; s[d][r] = fsub_s(s[d][r], m); }
            bf16x8 p0, p1;
            expo(s[d], p0, p1);
            o[d] = MFMA32(ldsfrag(p + 2048), p0, zero16); l[d] = MFMA16(ones, p0, ((f32x4){0.f, 0.f, 0.f, 0.f}));
            o[d] = MFMA32(ldsfrag(p + 3072), p1, o[d]); l[d] = MFMA16(ones, p1, l[d]);
        }
    }
    waitv<4>(); WAITL0(); BAR();
    if (wid == 0 && lane == 0 && rb < 7) __hip_atomic_store((GAS u64*)(a.ws + WS_FKV + (size_t)me * 128), tagp, __ATOMIC_RELAXED, __HIP_MEMORY_SCOPE_AGENT);
    STAMP(3);
    auto attention = [&](auto WC_) {
        constexpr int WR = decltype(WC_)::value;
        u32x4 pld[8];
        auto jobs = [&](auto PART) {
            constexpr int P2 = decltype(PART)::value;
            constexpr int NJ = P2 ? 9 : 7;
            auto TOF = [](int j) { return P2 ? (j < 7 ? (j < 7 - WR ? 0 : 1) : j - 7) : (j < WR ? 0 : 1); };
            auto BOF = [](int j) { return P2 ? (j < 7 ? (j < 7 - WR ? WR + 1 + j : j + 1) : (j == 7 ? WR : 7 - WR)) : 8 + (j < WR ? j : j - WR); };
            f32x16 S[2], cm; bf16x8 kf[2][2], vf[2][2]; u32x4 p0, p1;
            auto rdk = [&](int j) { kf[j & 1][0] = ldsfrag(kvb + BOF(j) * 4096); kf[j & 1][1] = ldsfrag(kvb + BOF(j) * 4096 + 1024); };
            auto cof = [&](int j) -> const f32x16& { return (P2 && j >= 7) ? cm : nm[TOF(j)]; };
            auto mkc = [&](int j) { const f32x16 mi = trimask(false);
#pragma unroll
                for (int r = 0; r < 16; ++r) cm[r] = fadd_s(mi[r], nm[TOF(j)][r]); };
            rdk(0); rdk(1); vf[0][0] = ldsfrag(kvb + BOF(0) * 4096 + 2048);
            S[0] = MFMA32(kf[0][0], qf[TOF(0)][0], cof(0)); S[0] = MFMA32(kf[0][1], qf[TOF(0)][1], S[0]);
            SBAR();
#pragma unroll
            for (int j = 0; j < NJ; ++j) {
                const int T = TOF(j), TP = j > 0 ? TOF(j - 1) : 0, TN = j + 1 < NJ ? TOF(j + 1) : 0;
                f32x16& s = S[j & 1];
#define EX(i) s[i] = __builtin_amdgcn_exp2f(s[i])
                if (P2 && j + 1 >= 7 && j + 1 < NJ) { mkc(j + 1); PIN(cm); }
                if (j + 1 < NJ) S[(j + 1) & 1] = MFMA32(kf[(j + 1) & 1][0], qf[TN][0], cof(j + 1));
                SBAR();
                vf[j & 1][1] = ldsfrag(kvb + BOF(j) * 4096 + 3072);
                EX(0); EX(1); EX(2); PIN(s); SBAR();
                if (j > 0) o[TP] = MFMA32(vf[(j - 1) & 1][1], __builtin_bit_cast(bf16x8, p1), o[TP]);
                SBAR();
                EX(3); EX(4); EX(5); PIN(s); SBAR();
                if (j > 0) l[TP] = MFMA16(ones, __builtin_bit_cast(bf16x8, p1), l[TP]);
                SBAR();
                EX(6); EX(7);
                p0 = (u32x4){cvtpk(s[0], s[1]), cvtpk(s[2], s[3]), cvtpk(s[4], s[5]), cvtpk(s[6], s[7])}; PIN(p0); SBAR();
                if (j + 1 < NJ) S[(j + 1) & 1] = MFMA32(kf[(j + 1) & 1][1], qf[TN][1], S[(j + 1) & 1]);
                SBAR();
                if (j + 1 < NJ) vf[(j + 1) & 1][0] = ldsfrag(kvb + BOF(j + 1) * 4096 + 2048);
                EX(8); EX(9); EX(10); PIN(s); SBAR();
                o[T] = MFMA32(vf[j & 1][0], __builtin_bit_cast(bf16x8, p0), o[T]);
                SBAR();
                if (j + 2 < NJ) rdk(j + 2);
                EX(11); EX(12); EX(13); PIN(s); SBAR();
                l[T] = MFMA16(ones, __builtin_bit_cast(bf16x8, p0), l[T]);
                SBAR();
                EX(14); EX(15);
                p1 = (u32x4){cvtpk(s[8], s[9]), cvtpk(s[10], s[11]), cvtpk(s[12], s[13]), cvtpk(s[14], s[15])}; PIN(p1); SBAR();
#undef EX
                if (j == NJ - 1) { o[T] = MFMA32(vf[j & 1][1], __builtin_bit_cast(bf16x8, p1), o[T]); l[T] = MFMA16(ones, __builtin_bit_cast(bf16x8, p1), l[T]); }
                if (!P2 && j == POLJ && rb > 0) {
                    if (WR == 0 && wid == 0) {
                        GAS u64* f = (GAS u64*)(a.ws + WS_FKV + (size_t)(me - 16) * 128);
#pragma nounroll
                        for (int spin = 0; spin < SPIN_CAP; ++spin) {
                            const u64 v = __hip_atomic_load(f, __ATOMIC_RELAXED, __HIP_MEMORY_SCOPE_AGENT);
                            if (rfl((unsigned)v) == (unsigned)tagc && rfl((unsigned)(v >> 32)) == (unsigned)(tagc >> 32)) break;
                            __builtin_amdgcn_s_sleep(1);
                        }
                        if (lane == 0) __hip_atomic_store((GAS u64*)(a.ws + WS_EKV + (size_t)(me - 16) * 128), tagc, __ATOMIC_RELAXED, __HIP_MEMORY_SCOPE_AGENT);
                    }
                    BAR();
                    STAMP(5);
#pragma unroll
                    for (int k = 0; k < 8; ++k) pld[k] = __builtin_amdgcn_raw_buffer_load_b128(srdW, (unsigned)WS_KV + (me - 16) * 65536 + wid * 8192 + k * 1024 + lane * 16, 0, 16);
                    SBAR();
                }
            }
        };
        jobs(std::integral_constant<int, 0>{});
        STAMP(4);
        if (rb > 0) {
#pragma unroll
            for (int i = 0; i < 8; ++i) *(LAS u32x4*)(lds + half * 65536 + w4 * 8192 + i * 1024 + lane * 16) = pld[i];
            WAITL0(); BAR();
            STAMP(6);
            jobs(std::integral_constant<int, 1>{});
        }
    };
#define ATT(W) attention(std::integral_constant<int, W>{})
    if (wr == 0) ATT(0); else if (wr == 1) ATT(1); else if (wr == 2) ATT(2); else ATT(3);
#pragma unroll
    for (int d = 0; d < 2; ++d) {
        const int gt = d ? 7 - wr : wr;
        float lt = (lane & 16) ? l[d][1] : l[d][0];
        if (__builtin_expect(__any(!(lt <= LSAFE)), 0)) {
            const f32x16 mlo = trimask(true), mhi = trimask(false);
            const int b0 = rb > 0 ? gt : 8, b1 = 8 + gt;
            auto sc = [&](int b, float ref) { f32x16 c;
#pragma unroll
                for (int r = 0; r < 16; ++r) c[r] = (b == b1 ? mlo[r] : b == gt ? mhi[r] : 0.f) - ref;
                c = MFMA32(ldsfrag(kvb + b * 4096), qf[d][0], c); return MFMA32(ldsfrag(kvb + b * 4096 + 1024), qf[d][1], c); };
            float m = -INFINITY;
#pragma nounroll
            for (int b = b0; b <= b1; ++b) m = jmax(sc(b, 0.f), m);
            m = xmax(m);
            f32x4 ls = {0.f, 0.f, 0.f, 0.f};
#pragma unroll
            for (int r = 0; r < 16; ++r) o[d][r] = 0.f;
#pragma nounroll
            for (int b = b0; b <= b1; ++b) { bf16x8 p0, p1; expo(sc(b, m), p0, p1);
                o[d] = MFMA32(ldsfrag(kvb + b * 4096 + 2048), p0, o[d]); o[d] = MFMA32(ldsfrag(kvb + b * 4096 + 3072), p1, o[d]);
                ls = MFMA16(ones, p0, ls); ls = MFMA16(ones, p1, ls); }
            lt = (lane & 16) ? ls[1] : ls[0];
        }
        const float inv = __builtin_amdgcn_rcpf(lt);
        const unsigned base = (unsigned)WS_O + (((br * 128 + rb * 16 + gt * 2 + (r32 >> 4)) * 32) + hp * 2 + wc) * 1024 + hh * 256 + (r32 & 15) * 16;
#pragma unroll
        for (int e = 0; e < 2; ++e) {
            const f32x16& x = o[d];
            const u32x4 v = {cvtpk(fmul_s(x[8 * e], inv), fmul_s(x[8 * e + 1], inv)), cvtpk(fmul_s(x[8 * e + 2], inv), fmul_s(x[8 * e + 3], inv)), cvtpk(fmul_s(x[8 * e + 4], inv), fmul_s(x[8 * e + 5], inv)), cvtpk(fmul_s(x[8 * e + 6], inv), fmul_s(x[8 * e + 7], inv))};
            __builtin_amdgcn_raw_buffer_store_b128(v, srdW, base + e * 512, 0, 16);
        }
    }
    STAMP(7);
    auto phase3 = [&](auto HC) {
        constexpr int H = decltype(HC)::value;
        const int lane = lane_id(), r16 = lane & 15, g4 = lane >> 4, q4 = (lane & 15) >> 2, p4 = lane & 3;
        GAS char* ep = (GAS char*)a.ws + WS_EO + (lane < 16 ? (size_t)(grp + lane) * 128 + hp * 8 : (size_t)me * 128 + (lane & 15) * 8);
        u64 eo;
        asm volatile("global_load_dwordx2 %0, %1, off sc1" : "=v"(eo) : "v"(ep) : "memory");
        unsigned woff[8];
        { const int row = 16 * (g4 >> 1) + 4 * (g4 & 1) + q4;
#pragma unroll
          for (int n = 0; n < 8; ++n) woff[n] = 131072 + H * 8192 + row * 256 + ((n ^ (row & 7)) << 5) + p4 * 8; }
        bf16x8 wf[2][8];
        auto wread = [&](int s, int n) {
            if (s == 2 || s == 12) { woff[n] -= 131072; PIN(woff[n]); } else if (s == 6 || s == 10) { woff[n] += 65536; PIN(woff[n]); }
            LAS const char* p = lds + woff[n] + (((s + 8) % 10) & 3) * 16384;
            const s16x4 lo = trread(p), hi = trread(p + 2048); wf[s & 1][n] = __builtin_shufflevector(lo, hi, 0, 1, 2, 3, 4, 5, 6, 7); };
        waitv<5>(); WAITL0(); BAR();
        wo_steps(2, 2 + WPRE);
#pragma unroll
        for (int n = 0; n < 8; ++n) { wread(0, n); wread(1, n); }
        waitv<2 * WPRE>(); PIN(eo); WAITL0(); BAR();
        if (H == 0 && wid == 0) {
            const u64 tag = eo + TAGK;
            if (lane < 16) __hip_atomic_store((GAS u64*)(ep - WS_EO + WS_FO), tag, __ATOMIC_RELAXED, __HIP_MEMORY_SCOPE_AGENT);
        }
        wo_steps(2 + WPRE, 12);
        if (H == 0 && wid == 0) {
            const u64 tag = eo + TAGK;
            GAS u64* f = (GAS u64*)(a.ws + WS_FO + (size_t)me * 128 + (lane & 15) * 8);
            const bool mine = lane >= 16 && lane < 32;
#pragma nounroll
            for (int spin = 0; spin < SPIN_CAP; ++spin) {
                const u64 v = __hip_atomic_load(f, __ATOMIC_RELAXED, __HIP_MEMORY_SCOPE_AGENT);
                if (__all(!mine || v == tag)) break;
                __builtin_amdgcn_s_sleep(1);
            }
            if (mine) __hip_atomic_store((GAS u64*)ep, tag, __ATOMIC_RELAXED, __HIP_MEMORY_SCOPE_AGENT);
        }
        waitv<0>(); BAR();
        STAMP(8);
        bf16x8 of[OW][2];
        const unsigned obase = (unsigned)WS_O + ((br * 128 + rb * 16 + th * 8 + w4 * 2) * 32 + H * 16) * 1024 + lane * 16;
        auto oload = [&](int s, int t) { of[s % OW][t] = __builtin_bit_cast(bf16x8, __builtin_amdgcn_raw_buffer_load_b128(srdW, obase + t * 32768 + s * 1024, 0, 16)); };
#pragma unroll
        for (int s = 0; s < OW - 1; ++s) { oload(s, 0); oload(s, 1); SBAR(); }
        f32x4 c2[2][8];
#pragma unroll
        for (int t = 0; t < 2; ++t)
#pragma unroll
            for (int n = 0; n < 8; ++n) c2[t][n] = (f32x4){0.f, 0.f, 0.f, 0.f};
        const unsigned voffW0 = (lane >> 4) * 2048 + (((lane & 15) ^ ((lane >> 4) << 1)) << 4), voffW1 = (4 + (lane >> 4)) * 2048 + (((lane & 15) ^ ((4 + (lane >> 4)) << 1)) << 4);
        {
            auto step = [&](int s) {
#pragma unroll
                for (int n = 0; n < 8; ++n) {
                    c2[0][n] = MFMA16(wf[s & 1][n], of[s % OW][0], c2[0][n]); c2[1][n] = MFMA16(wf[s & 1][n], of[s % OW][1], c2[1][n]);
                    SBAR();
                    if (s + 2 < 16) wread(s + 2, n);
                    if (n == PA0 + 2 * H && s + OW - 1 < 16) oload(s + OW - 1, 0);
                    if (n == PA0 + 4 + 2 * H && s + OW - 1 < 16) oload(s + OW - 1, 1);
                    if (s >= 4 && s < 8 && (n == PA0 + 2 - 2 * H || n == PA0 + 6 - 2 * H)) dma16(ldsb + ((s + 16) % 10) * 16384 + wid * 2048 + (n >= 4) * 1024, n >= 4 ? voffW1 : voffW0, srdWo, sW + (s + 8) * 65536);
                    SBAR();
                } };
#pragma unroll
            for (int s = 0; s < 4; ++s) { step(s); if (s == 0) STAMP(11); }
            STAMP(12);
            WAITL0(); BAR();
#pragma unroll
            for (int s = 4; s < 10; ++s) step(s);
            STAMP(13);
            waitv<4>(); BAR();
            STAMP(14);
#pragma unroll
            for (int s = 10; s < 16; ++s) step(s);
            STAMP(9);
            BAR();
#pragma unroll
            for (int t = 0; t < 2; ++t)
#pragma unroll
                for (int n = 0; n < 4; ++n) *(LAS f32x4*)(lds + wid * 8192 + (t * 4 + n) * 1024 + lane * 16) = c2[t][4 - 4 * H + n];
            WAITL0(); BAR();
            STAMP(15);
            GAS u16* outp = (GAS u16*)a.out + (size_t)(rb * 256 + th * 128 + w4 * 32 + r16) * 2048 + br * 1024 + ct * 128 + H * 64 + (g4 & 1) * 16 + (g4 >> 1) * 8;
#pragma unroll
            for (int t = 0; t < 2; ++t)
#pragma unroll
                for (int n = 0; n < 4; n += 2) {
                    const f32x4 x = c2[t][4 * H + n] + *(LAS const f32x4*)(lds + (wid ^ 4) * 8192 + (t * 4 + n) * 1024 + lane * 16);
                    const f32x4 y = c2[t][4 * H + n + 1] + *(LAS const f32x4*)(lds + (wid ^ 4) * 8192 + (t * 4 + n + 1) * 1024 + lane * 16);
                    const auto u0 = __builtin_amdgcn_permlane16_swap(cvtpk(x[0], x[1]), cvtpk(y[0], y[1]), false, false);
                    const auto u1 = __builtin_amdgcn_permlane16_swap(cvtpk(x[2], x[3]), cvtpk(y[2], y[3]), false, false);
                    *(GAS u32x4*)(outp + (size_t)t * 16 * 2048 + n * 16) = (u32x4){u0[0], u1[0], u0[1], u1[1]};
                }
        }
    };
    if (half == 0) phase3(std::integral_constant<int, 0>{}); else phase3(std::integral_constant<int, 1>{});
    STAMP(10);
    { const u64 d = (mtb - mta) / 20, t0 = __builtin_amdgcn_s_memrealtime(); if (d < 10000 && (hp & 7) == 0) while (__builtin_amdgcn_s_memrealtime() - t0 < 4 * d) __builtin_amdgcn_s_sleep(2); }
}

extern "C" void kernel_launch(void* const* d_in, const int* in_sizes, int n_in, void* d_out, int out_size, void* d_ws, size_t ws_size, hipStream_t stream) {
    Args a;
    a.x[0] = (const u16*)d_in[0]; a.w[0] = (const u16*)d_in[1]; a.wo[0] = (const u16*)d_in[2];
    a.x[1] = (const u16*)d_in[3]; a.w[1] = (const u16*)d_in[4]; a.wo[1] = (const u16*)d_in[5];
    a.out = (u16*)d_out; a.ws = (char*)d_ws;
    fused_kernel<<<256, 512, 0, stream>>>(a);
}
```

```cpp
#define TOUCH 1
#define PD 5
#include <hip/hip_runtime.h>
#include <hip/hip_bf16.h>
#include <type_traits>
typedef short bf16x8 __attribute__((ext_vector_type(8)));
typedef short s16x4 __attribute__((ext_vector_type(4)));
typedef float f32x16 __attribute__((ext_vector_type(16)));
typedef float f32x4 __attribute__((ext_vector_type(4)));
typedef float f32x2 __attribute__((ext_vector_type(2)));
typedef __bf16 bf16x2 __attribute__((ext_vector_type(2)));
typedef unsigned u32x4 __attribute__((ext_vector_type(4)));
typedef double f64x8 __attribute__((ext_vector_type(8)));
typedef double f64x2 __attribute__((ext_vector_type(2)));
typedef unsigned short u16;
typedef unsigned long long u64;
#define LAS __attribute__((address_space(3)))
#define GAS __attribute__((address_space(1)))
#define DI __device__ __forceinline__
#define MFMA16(a, b, c) __builtin_amdgcn_mfma_f32_16x16x32_bf16((a), (b), (c), 0, 0, 0)
#define MFMA32(a, b, c) __builtin_amdgcn_mfma_f32_32x32x16_bf16((a), (b), (c), 0, 0, 0)
#define SBAR() __builtin_amdgcn_sched_barrier(0)
#define BAR() do { SBAR(); asm volatile("s_barrier" ::: "memory"); SBAR(); } while (0)
#define PIN(x) asm volatile("" : "+v"(x))
#define WAITL0() __builtin_amdgcn_s_waitcnt(0xC07F)
template <int N> DI void waitv() { asm volatile("s_waitcnt vmcnt(%0)" :: "i"(N) : "memory"); }
DI unsigned rfl(unsigned x) { return __builtin_amdgcn_readfirstlane(x); }
DI int lane_id() { int l; asm volatile("v_mbcnt_lo_u32_b32 %0, -1, 0\n\tv_mbcnt_hi_u32_b32 %0, -1, %0" : "=v"(l)); return l; }
DI unsigned cvtpk(float lo, float hi) { f32x2 v = {lo, hi}; bf16x2 b = __builtin_convertvector(v, bf16x2); return __builtin_bit_cast(unsigned, b); }
DI void dma16(unsigned m0v, unsigned voff, __amdgpu_buffer_rsrc_t srd, unsigned soff) {
    asm volatile("s_mov_b32 m0, %0\n\ts_nop 0\n\tbuffer_load_dwordx4 %1, %2, %3 offen lds" :: "s"(m0v), "v"(voff), "s"(srd), "s"(soff) : "memory");
}
DI void dma4(unsigned m0v, unsigned voff, __amdgpu_buffer_rsrc_t srd, unsigned soff) {
    asm volatile("s_mov_b32 m0, %0\n\ts_nop 0\n\tbuffer_load_dword %1, %2, %3 offen lds" :: "s"(m0v), "v"(voff), "s"(srd), "s"(soff) : "memory");
}
DI s16x4 trread(LAS const char* p) { return __builtin_amdgcn_ds_read_tr16_b64_v4i16((LAS s16x4*)p); }
DI bf16x8 ldsfrag(LAS const char* p) { return *(LAS const bf16x8*)p; }
DI float max3f(float a, float b, float c) { return __builtin_fmaxf(__builtin_fmaxf(a, b), c); }
DI float fadd_s(float a, float b) { return a + b; }
DI float fsub_s(float a, float b) { return a - b; }
DI float fmul_s(float a, float b) { return a * b; }
DI bf16x8 pack8(float a, float b, float c, float d, float e, float f, float g, float h) { u32x4 v = {cvtpk(a, b), cvtpk(c, d), cvtpk(e, f), cvtpk(g, h)}; return __builtin_bit_cast(bf16x8, v); }

#define SW64(a, b) "v_mov_b64 %[t], %" #a "\n\tv_mov_b64 %" #a ", %" #b "\n\tv_mov_b64 %" #b ", %[t]\n\t"
template <int F> DI void swap_q(int w, bf16x8 (&q)[2][2], float& n0, float& n1, f32x16& cm, float aone) {
    f64x2 a = __builtin_bit_cast(f64x2, q[0][0]), b = __builtin_bit_cast(f64x2, q[0][1]), c = __builtin_bit_cast(f64x2, q[1][0]), d = __builtin_bit_cast(f64x2, q[1][1]); double t;
    asm volatile("s_cmp_lg_u32 %[w], %[f]\n\ts_cbranch_scc1 1f\n\tv_swap_b32 %8, %9\n\t" SW64(0, 4) SW64(1, 5) "v_mfma_f32_32x32x2_f32 %10, %[one], %8, 0\n\t" SW64(2, 6) SW64(3, 7) "s_nop 1\n1:"
        : "+v"(a[0]), "+v"(a[1]), "+v"(b[0]), "+v"(b[1]), "+v"(c[0]), "+v"(c[1]), "+v"(d[0]), "+v"(d[1]), "+v"(n0), "+v"(n1), "+v"(cm), [t] "=&v"(t) : [w] "s"(w), [f] "i"(F), [one] "v"(aone) : "scc");
    q[0][0] = __builtin_bit_cast(bf16x8, a); q[0][1] = __builtin_bit_cast(bf16x8, b); q[1][0] = __builtin_bit_cast(bf16x8, c); q[1][1] = __builtin_bit_cast(bf16x8, d);
}
template <int F> DI void pv_sel(int w, f32x16& lo, f32x16& hi, bf16x8 a, u32x4 b) {
    asm volatile("s_cmp_gt_u32 %[w], %[f]\n\ts_cbranch_scc1 1f\n\tv_mfma_f32_32x32x16_bf16 %1, %2, %3, %1\n\ts_branch 2f\n1:\n\tv_mfma_f32_32x32x16_bf16 %0, %2, %3, %0\n2:"
        : "+v"(lo), "+v"(hi) : "v"(a), "v"(b), [w] "s"(w), [f] "i"(F) : "scc");
}
template <int F> DI void ls_sel(int w, f32x4& lo, f32x4& hi, bf16x8 a, u32x4 b) {
    asm volatile("s_cmp_gt_u32 %[w], %[f]\n\ts_cbranch_scc1 1f\n\tv_mfma_f32_16x16x32_bf16 %1, %2, %3, %1\n\ts_branch 2f\n1:\n\tv_mfma_f32_16x16x32_bf16 %0, %2, %3, %0\n2:"
        : "+v"(lo), "+v"(hi) : "v"(a), "v"(b), [w] "s"(w), [f] "i"(F) : "scc");
}
constexpr int LDS_A0 = 0, LDS_A1 = 32768, LDS_B = 81920, A_SLOT = 16384, B_SLOT = 24576;
constexpr int LDS_TOTAL = 163840;
constexpr size_t WS_FKV = 0, WS_EKV = 32768, WS_FO = 65536, WS_EO = 98304, WS_KV = 1 << 20, WS_O = 17 << 20, WS_END = 25 << 20, WS_ST = 32 << 20;
constexpr u64 TAGK = 0x9E3779B97F4A7C15ull;
constexpr int SPIN_CAP = 400000;
constexpr float QSCALE = 0.17677669529663687f * 1.4426950408889634f;

#define PUBJ 3
#define POLJ 5
#define WRJ 9
#define SPJ 4
#define OW 5
#define PA0 1
#define WPRE 4
#define LSAFE 1.152921504606847e18f
#define KROT 0
#define CODEPF 32
constexpr int LDS_SCR = 155648;
struct Args { const u16* x[2]; const u16* w[2]; const u16* wo[2]; u16* out; char* ws; };

#define STAMP(i) do {} while (0)

__global__ __launch_bounds__(512, 2) void fused_kernel(Args a) {
    __shared__ __attribute__((aligned(16))) char smem[LDS_TOTAL];
    LAS char* lds = (LAS char*)smem;
    const unsigned ldsb = (unsigned)(size_t)smem;
    const int tid = threadIdx.x, lane = tid & 63;
    const int wid = rfl(tid >> 6), half = wid >> 2, w4 = wid & 3;
    const int bid = blockIdx.x, g = bid & 7, j = bid >> 3;
    const int br = g >> 2, rb = ((g >> 1) & 1) * 4 + (j & 3), hp = (g & 1) * 8 + (j >> 2);
    const int me = (br * 8 + rb) * 16 + hp, grp = me & ~15;
    STAMP(0);
    if (CODEPF && wid == 7) {
        u64 pc; asm volatile("s_getpc_b64 %0" : "=s"(pc));
        const __amdgpu_buffer_rsrc_t srdC = __builtin_amdgcn_make_buffer_rsrc((void*)pc, (short)0, CODEPF * 1024, 0x00020000);
        if (lane < 13) dma4(ldsb + LDS_SCR, (j * 13 + lane) * 128, srdC, 0);
    }
    char* wsp = a.ws; const u16* wop = a.wo[br]; u16* outb = a.out;
    asm volatile("" : "+s"(wsp), "+s"(wop), "+s"(outb));
    u64 ew = __hip_atomic_load((GAS u64*)(wsp + WS_EKV + (size_t)(lane == 0 || rb == 0 ? me : me - 16) * 128), __ATOMIC_RELAXED, __HIP_MEMORY_SCOPE_AGENT);
    const __amdgpu_buffer_rsrc_t srdA = __builtin_amdgcn_make_buffer_rsrc((void*)(a.x[br] + (size_t)rb * 256 * 1024), (short)0, 256 * 2048, 0x00020000);
    const __amdgpu_buffer_rsrc_t srdB = __builtin_amdgcn_make_buffer_rsrc((void*)(a.w[br]), (short)0, 1024 * 6144, 0x00020000);
    const int wr = 2 * half + (w4 >> 1), wc = w4 & 1;
    const int r16 = lane & 15, g4 = lane >> 4, q4 = (lane & 15) >> 2, p4 = lane & 3;
    f32x4 acc[4][6];
    const int r32 = lane & 31, hh = lane >> 5;
    f32x16 mk;
#pragma unroll
    for (int r = 0; r < 16; ++r) mk[r] = ((r & 3) + 8 * (r >> 2) <= r32 - 4 * hh) ? 0.f : -INFINITY;
    {
        const unsigned voffE = (lane >> 3) * 2048 + (((lane & 7) ^ (lane >> 4)) << 4);
        const unsigned voffO = (8 + (lane >> 3)) * 2048 + (((lane & 7) ^ (4 + (lane >> 4))) << 4);
        const unsigned voffB0 = (lane >> 3) * 6144 + (((lane & 7) ^ (((lane >> 4) & 1) << 1)) << 4);
        const unsigned voffB1 = (lane >> 3) * 6144 + (((lane & 7) ^ ((((lane >> 4) & 1) << 1) | 4)) << 4);
        const int gt0 = (w4 & 1) ? 7 - (w4 >> 1) : (w4 >> 1);
        const int gt1 = (w4 & 1) ? 5 - (w4 >> 1) : 2 + (w4 >> 1);
        const unsigned sA0 = gt0 * 32 * 2048, sA1 = gt1 * 32 * 2048;
        const unsigned sB = hp * 128 + (2 * w4) * 8 * 6144;
        unsigned aoff[2], boff[2];
#pragma unroll
        for (int s = 0; s < 2; ++s) aoff[s] = (half ? LDS_A1 : LDS_A0) + ((w4 >> 1) * 64 + r16) * 128 + (((4 * s + g4) ^ ((r16 >> 1) & 7)) << 4);
#pragma unroll
        for (int c = 0; c < 2; ++c) boff[c] = LDS_B + (8 * g4 + q4) * 128 + (((wc * 2 + c) ^ ((q4 >> 1) | ((g4 & 1) << 1))) << 5) + p4 * 8;
#pragma unroll
        for (int i = 0; i < 4; ++i)
#pragma unroll
            for (int s = 0; s < 6; ++s) acc[i][s] = (f32x4){0.f, 0.f, 0.f, 0.f};
        bf16x8 af[4][2], bfr[6][2];
        const unsigned voffT = (16 * (rb & 3) + 4 * w4 + lane / 3) * 6144 + (lane % 3) * 2048 + hp * 128;
        const bool slowc = ((((size_t)a.w[br] >> 7) + hp) & 7) == 3;
        auto run = [&](auto HC, auto TC) {
            constexpr int H = decltype(HC)::value;
            constexpr int TW = decltype(TC)::value;
            auto touch = [&](int kt) { if (lane < 12) dma4(ldsb + LDS_SCR, voffT, srdB, kt * (64 * 6144)); };
            auto piece = [&](int kt0, int s2, int s3i, int i) {
                const int kt = KROT ? (kt0 + KROT * g) & 15 : kt0;
                const unsigned soB = sB + kt * (64 * 6144), dB = ldsb + LDS_B + s3i * B_SLOT + w4 * 2048;
                const unsigned soA0 = sA0 + kt * 128, dA0 = ldsb + LDS_A0 + s2 * A_SLOT + w4 * 4096;
                const unsigned soA1 = sA1 + kt * 128, dA1 = ldsb + LDS_A1 + s3i * A_SLOT + w4 * 4096;
                if (H == 0) {
                    if (i == 0) dma16(dA1 + 3 * 1024, voffO, srdA, soA1 + 32768);
                    else { const int k = i - 1; dma16(dB + (k >> 1) * 8192 + (k & 1) * 1024, (k & 1) ? voffB1 : voffB0, srdB, soB + (k >> 1) * 2048 + (k & 1) * 8 * 6144); }
                } else dma16((i < 4 ? dA0 : dA1) + (i & 3) * 1024, (i & 1) ? voffO : voffE, srdA, (i < 4 ? soA0 : soA1) + ((i >> 1) & 1) * 32768);
            };
#pragma unroll
            for (int i = 0; i < 7; ++i) piece(0, 0, 0, i);
#pragma unroll
            for (int i = 0; i < 7; ++i) piece(1, 1, 1, i);
#pragma unroll
            for (int i = 0; i < 4; ++i) asm volatile("" : "+v"(acc[i][0]), "+v"(acc[i][1]), "+v"(acc[i][2]), "+v"(acc[i][3]), "+v"(acc[i][4]), "+v"(acc[i][5]));
            PIN(mk);
            if (H == 0) waitv<7>(); else waitv<10>();
            BAR();
            if (TW) {
#pragma unroll
                for (int kt = 3; kt < PD; ++kt) touch(kt);
            }
            if (H == 1) { waitv<7>(); BAR(); }
            STAMP(1);
            int s3 = 0;
            auto Lseg = [&](int t, bool issue) {
                const unsigned aslot = H ? s3 * A_SLOT : (t & 1) * A_SLOT;
                const unsigned bslot = s3 * B_SLOT;
                const int s3n = (s3 == 0) ? 2 : s3 - 1;
                auto rdA = [&](int i) { LAS const char* pa = lds + aoff[i >> 1] + aslot + (i & 1) * 4096; af[(i & 1) * 2][i >> 1] = ldsfrag(pa); af[(i & 1) * 2 + 1][i >> 1] = ldsfrag(pa + 2048); };
                auto rdB = [&](int i) { const int s = i / 6, seg = (i % 6) >> 1, c = i & 1; LAS const char* pb = lds + boff[c] + bslot;
                    s16x4 lo = trread(pb + seg * 8192 + s * 4096), hi = trread(pb + seg * 8192 + s * 4096 + 512);
                    bfr[seg * 2 + c][s] = __builtin_shufflevector(lo, hi, 0, 1, 2, 3, 4, 5, 6, 7); };
                if (TW && TOUCH == 2 && issue) touch(t + PD);
#pragma unroll
                for (int i = 0; i < 16; ++i) {
                    if (issue && (i & 1) == 0 && i / 2 < 7) piece(t + 2, t & 1, s3n, i / 2);
                    if (TW && TOUCH == 3 && issue && i == 13) touch(t + PD);
                    if (i < 4) rdA(i); else rdB(i - 4);
                }
                if (TW && TOUCH == 1 && issue) touch(t + PD);
                s3 = (s3 == 2) ? 0 : s3 + 1;
            };
            auto Mseg = [&]() {
                __builtin_amdgcn_s_setprio(1);
#pragma unroll
                for (int s = 0; s < 2; ++s)
#pragma unroll
                    for (int m = 0; m < 4; ++m)
#pragma unroll
                        for (int n = 0; n < 6; ++n) acc[m][n] = n < 4 ? MFMA16(bfr[n][s], af[m][s], acc[m][n]) : MFMA16(af[m][s], bfr[n][s], acc[m][n]);
                __builtin_amdgcn_s_setprio(0);
            };
            if (TW && TOUCH != 2) { Lseg(0, true); WAITL0(); BAR(); Mseg(); waitv<8 + PD - 3>(); BAR(); }
            for (int t = (TW && TOUCH != 2) ? 1 : 0; t < 14; ++t) {
                Lseg(t, true);
                if (H == 1) waitv<10>();
                WAITL0(); BAR();
                Mseg();
                waitv<7 + (TW ? (TOUCH == 2 ? 1 : 2) : 0)>();
                BAR();
            }
            Lseg(14, false); if (H == 1) waitv<3>(); WAITL0(); BAR(); Mseg(); waitv<0>(); BAR();
            Lseg(15, false); WAITL0(); BAR(); Mseg(); if (H == 0) BAR();
        };
        if (half == 1) run(std::integral_constant<int, 1>{}, std::integral_constant<int, 0>{});
        else if (TOUCH && slowc) run(std::integral_constant<int, 0>{}, std::integral_constant<int, 1>{});
        else run(std::integral_constant<int, 0>{}, std::integral_constant<int, 0>{});
    }
    STAMP(2);
    const u64 tagp = (((u64)__builtin_amdgcn_readlane((unsigned)(ew >> 32), 0) << 32) | __builtin_amdgcn_readlane((unsigned)ew, 0)) + TAGK;
    const u64 tagc = (((u64)__builtin_amdgcn_readlane((unsigned)(ew >> 32), 1) << 32) | __builtin_amdgcn_readlane((unsigned)ew, 1)) + TAGK;
    const __amdgpu_buffer_rsrc_t srdW = __builtin_amdgcn_make_buffer_rsrc((void*)wsp, (short)0, (int)WS_END, 0x00020000);
    const __amdgpu_buffer_rsrc_t srdWo = __builtin_amdgcn_make_buffer_rsrc((void*)(wop), (short)0, 1024 * 2048, 0x00020000);
    const int ct = hp & 7, th = hp >> 3;
    const unsigned sW = ct * 256 + (half * 512 + w4 * 8) * 2048;
    auto wo_steps = [&](int s0, int s1) {
        const int ln = lane_id();
        const unsigned voffW0 = (ln >> 4) * 2048 + (((ln & 15) ^ ((ln >> 4) << 1)) << 4), voffW1 = (4 + (ln >> 4)) * 2048 + (((ln & 15) ^ ((4 + (ln >> 4)) << 1)) << 4);
#pragma unroll
        for (int s = s0; s < s1; ++s) { const unsigned d = ldsb + ((s + 8) % 10) * 16384 + wid * 2048; dma16(d, voffW0, srdWo, sW + s * 65536); dma16(d + 1024, voffW1, srdWo, sW + s * 65536); } };
    bf16x8 qf[2][2];
    u32x4 kv[2][2], vv[2][2];
    const unsigned kpos = (g4 >> 1) * 1024 + (g4 & 1) * 512 + r16 * 16;
    const unsigned vpos = 2048 + ((g4 & 1) * 32 + (g4 >> 1) * 16 + r16) * 16;
    const unsigned gkv = (unsigned)WS_KV + me * 65536 + wc * 32768;
    auto kvstore = [&](int i) { const int mi = i >> 2, s = (i >> 1) & 1; const unsigned so = gkv + (mi ? 7 - wr : wr) * 4096;
        if (i & 1) __builtin_amdgcn_raw_buffer_store_b128(vv[mi][s], srdW, so + vpos + s * 1024, 0, 16);
        else __builtin_amdgcn_raw_buffer_store_b128(kv[mi][s], srdW, so + kpos + s * 256, 0, 16); };
    {
#pragma unroll
        for (int mi = 0; mi < 2; ++mi)
#pragma unroll
            for (int s = 0; s < 2; ++s) {
                const f32x4 k0 = acc[2 * mi + s][2], k1 = acc[2 * mi + s][3], v0 = acc[2 * mi + s][4], v1 = acc[2 * mi + s][5];
                kv[mi][s] = (u32x4){cvtpk(k0[0], k0[1]), cvtpk(k0[2], k0[3]), cvtpk(k1[0], k1[1]), cvtpk(k1[2], k1[3])};
                const auto ra = __builtin_amdgcn_permlane32_swap(cvtpk(v0[0], v0[1]), cvtpk(v1[0], v1[1]), false, false);
                const auto rbb = __builtin_amdgcn_permlane32_swap(cvtpk(v0[2], v0[3]), cvtpk(v1[2], v1[3]), false, false);
                vv[mi][s] = (u32x4){ra[0], rbb[0], ra[1], rbb[1]};
                const unsigned blk = wc * 65536 + 32768 + (mi ? 7 - wr : wr) * 4096;
                *(LAS u32x4*)(lds + blk + kpos + s * 256) = kv[mi][s];
                *(LAS u32x4*)(lds + blk + vpos + s * 1024) = vv[mi][s];
            }
        if (rb < 7 && half == 0) {
#pragma unroll
            for (int i = 0; i < 8; ++i) kvstore(i);
        }
#pragma unroll
        for (int mi = 0; mi < 2; ++mi) {
            u32x4 x[2], y0, y1;
#pragma unroll
            for (int s = 0; s < 2; ++s) { const f32x4 q0 = acc[2 * mi + s][0] * QSCALE, q1 = acc[2 * mi + s][1] * QSCALE;
                x[s] = (u32x4){cvtpk(q0[0], q0[1]), cvtpk(q0[2], q0[3]), cvtpk(q1[0], q1[1]), cvtpk(q1[2], q1[3])}; }
#pragma unroll
            for (int i = 0; i < 4; ++i) { const auto u = __builtin_amdgcn_permlane16_swap(x[0][i], x[1][i], false, false);
                const auto v = __builtin_amdgcn_permlane32_swap(u[0], u[1], false, false); y0[i] = v[0]; y1[i] = v[1]; }
            qf[mi][0] = __builtin_bit_cast(bf16x8, y0); qf[mi][1] = __builtin_bit_cast(bf16x8, y1);
        }
    }
    f32x16 o[2]; float nm[2]; f32x4 l[2];
    LAS const char* kvb = lds + wc * 65536 + lane * 16;
    bf16x8 ones;
    { const short v = ((lane ^ (lane >> 4)) & 1) ? (short)0 : (short)0x3f80;
#pragma unroll
      for (int i = 0; i < 8; ++i) ones[i] = v; }
    auto jmax = [&](const f32x16& s, float m) {
#pragma unroll
        for (int r = 0; r < 16; r += 2) m = max3f(m, s[r], s[r + 1]);
        return m; };
    auto xmax = [&](float m) { const auto r = __builtin_amdgcn_permlane32_swap(__float_as_uint(m), __float_as_uint(m), false, false); return __builtin_fmaxf(__uint_as_float(r[0]), __uint_as_float(r[1])); };
    auto expo = [&](const f32x16& s, bf16x8& p0, bf16x8& p1) {
        float t[16];
#pragma unroll
        for (int r = 0; r < 16; ++r) t[r] = __builtin_amdgcn_exp2f(s[r]);
        p0 = pack8(t[0], t[1], t[2], t[3], t[4], t[5], t[6], t[7]); p1 = pack8(t[8], t[9], t[10], t[11], t[12], t[13], t[14], t[15]); };
    auto flip = [&](const f32x16& m) { f32x16 c;
#pragma unroll
        for (int r = 0; r < 16; ++r) c[r] = __uint_as_float(__float_as_uint(m[r]) ^ 0xff800000u);
        return c; };
    {
        f32x16 s[2];
#pragma unroll
        for (int d = 0; d < 2; ++d) {
            LAS const char* p = kvb + (8 + (d ? 7 - wr : wr)) * 4096;
            s[d] = MFMA32(ldsfrag(p), qf[d][0], mk); s[d] = MFMA32(ldsfrag(p + 1024), qf[d][1], s[d]);
        }
        wo_steps(0, 2);
#pragma unroll
        for (int d = 0; d < 2; ++d) {
            nm[d] = -xmax(jmax(s[d], -INFINITY)); PIN(nm[d]);
        }
    }
    WAITL0(); BAR();
    STAMP(3);
    u32x4 pld[8]; u64 pv = 0;
    const bool st1 = half == 1 && rb < 7;
    const float aone = lane < 32 ? 1.f : 0.f;
    auto jobs = [&](auto PART) {
        constexpr int P2 = decltype(PART)::value;
        constexpr int NJ = 9, F0 = P2 ? 1 : 2;
        auto SET = [](int j) { return (!P2 && j == 0) ? 1 : 0; };
        auto MSK = [](int j) { return P2 ? (j == 0 || j == 8) : j < 2; };
        auto badr = [&](int j) -> LAS const char* {
            const int f = j - F0;
            const int b = P2 ? (j == 0 ? 7 - wr : j == 8 ? wr : (f < wr ? 8 - wr + f : f + 1)) : 8 + (j == 0 ? 7 - wr : j == 1 ? wr : (f < wr ? f : f - wr));
            return kvb + b * 4096; };
        f32x16 S[2], cm, zero16; bf16x8 kf[2][2], vf[2][2]; u32x4 p0, p1; LAS const char* ad[3];
#pragma unroll
        for (int r = 0; r < 16; ++r) zero16[r] = 0.f;
        auto rdk = [&](int j) { ad[j % 3] = badr(j); kf[j & 1][0] = ldsfrag(ad[j % 3]); kf[j & 1][1] = ldsfrag(ad[j % 3] + 1024); };
        auto mkc = [&](int j) {
            if (MSK(j)) {
#pragma unroll
                for (int r = 0; r < 16; ++r) cm[r] = fadd_s(mk[r], nm[SET(j)]);
            } else cm = __builtin_amdgcn_mfma_f32_32x32x2f32(aone, nm[SET(j)], zero16, 0, 0, 0);
            PIN(cm); };
        rdk(0); rdk(1); vf[0][0] = ldsfrag(ad[0] + 2048);
        mkc(0);
        S[0] = MFMA32(kf[0][0], qf[SET(0)][0], cm); S[0] = MFMA32(kf[0][1], qf[SET(0)][1], S[0]);
        SBAR();
        auto job = [&](auto JC) {
            constexpr int j = decltype(JC)::value;
            f32x16& s = S[j & 1];
            auto pvm = [&](auto JJ, bf16x8 a, u32x4 b, bool first) { constexpr int jj = decltype(JJ)::value, f = jj - F0, T = P2 ? (jj == 0 ? 1 : 0) : (jj == 1 ? 0 : 1);
                if constexpr (f >= 0 && f < 3) pv_sel<f>(wr, o[P2 ? 1 : 0], o[P2 ? 0 : 1], a, b);
                else o[T] = MFMA32(a, __builtin_bit_cast(bf16x8, b), first ? zero16 : o[T]); };
            auto lsm = [&](auto JJ, u32x4 b, bool first) { constexpr int jj = decltype(JJ)::value, f = jj - F0, T = P2 ? (jj == 0 ? 1 : 0) : (jj == 1 ? 0 : 1);
                if constexpr (f >= 0 && f < 3) ls_sel<f>(wr, l[P2 ? 1 : 0], l[P2 ? 0 : 1], ones, b);
                else l[T] = MFMA16(ones, __builtin_bit_cast(bf16x8, b), first ? ((f32x4){0.f, 0.f, 0.f, 0.f}) : l[T]); };
#define EX(i) s[i] = __builtin_amdgcn_exp2f(s[i])
            if (!P2 && j == POLJ) pv = __hip_atomic_load((GAS u64*)(wsp + WS_FKV + (size_t)(rb > 0 ? me - 16 : me) * 128), __ATOMIC_RELAXED, __HIP_MEMORY_SCOPE_AGENT);
            if (j + 1 < NJ && (MSK(j + 1) || j + 1 == F0)) mkc(j + 1);
            if (j + 1 == F0) swap_q<0>(wr, qf, nm[0], nm[1], cm, aone);
            if (j + 1 == F0 + 1) swap_q<1>(wr, qf, nm[0], nm[1], cm, aone);
            if (j + 1 == F0 + 2) swap_q<2>(wr, qf, nm[0], nm[1], cm, aone);
            if (j + 1 == F0 + 3) swap_q<3>(wr, qf, nm[0], nm[1], cm, aone);
            if (j + 1 < NJ) S[(j + 1) & 1] = MFMA32(kf[(j + 1) & 1][0], qf[0][0], cm);
            SBAR();
            vf[j & 1][1] = ldsfrag(ad[j % 3] + 3072);
            EX(0); EX(1); EX(2); PIN(s); SBAR();
            if constexpr (j > 0) pvm(std::integral_constant<int, j - 1>{}, vf[(j - 1) & 1][1], p1, false);
            SBAR();
            if (!P2 && j < 8 / SPJ && st1) {
#pragma unroll
                for (int k = 0; k < SPJ / 2; ++k) kvstore(j * SPJ + k); }
            EX(3); EX(4); EX(5); PIN(s); SBAR();
            if constexpr (j > 0) lsm(std::integral_constant<int, j - 1>{}, p1, false);
            SBAR();
            EX(6); EX(7);
            p0 = (u32x4){cvtpk(s[0], s[1]), cvtpk(s[2], s[3]), cvtpk(s[4], s[5]), cvtpk(s[6], s[7])}; PIN(p0); SBAR();
            if (j + 1 < NJ) S[(j + 1) & 1] = MFMA32(kf[(j + 1) & 1][1], qf[0][1], S[(j + 1) & 1]);
            SBAR();
            if (j + 1 < NJ) vf[(j + 1) & 1][0] = ldsfrag(ad[(j + 1) % 3] + 2048);
            EX(8); EX(9); EX(10); PIN(s); SBAR();
            pvm(JC, vf[j & 1][0], p0, !P2 && j < 2);
            SBAR();
            if (j + 2 < NJ) rdk(j + 2);
            if (!P2 && j < 8 / SPJ && st1) {
#pragma unroll
                for (int k = SPJ / 2; k < SPJ; ++k) kvstore(j * SPJ + k); }
            EX(11); EX(12); EX(13); PIN(s); SBAR();
            lsm(JC, p0, !P2 && j < 2);
            SBAR();
            EX(14); EX(15);
            p1 = (u32x4){cvtpk(s[8], s[9]), cvtpk(s[10], s[11]), cvtpk(s[12], s[13]), cvtpk(s[14], s[15])}; PIN(p1); SBAR();
#undef EX
            if (j == NJ - 1) { pvm(JC, vf[j & 1][1], p1, false); lsm(JC, p1, false); }
            if (!P2 && j == 1) { mk = flip(mk); PIN(mk); }
            if (!P2 && j == PUBJ) {
                waitv<0>(); BAR();
                if (wid == 0 && lane == 0 && rb < 7) __hip_atomic_store((GAS u64*)(wsp + WS_FKV + (size_t)me * 128), tagp, __ATOMIC_RELAXED, __HIP_MEMORY_SCOPE_AGENT);
                STAMP(12);
            }
            if (!P2 && j == POLJ && rb > 0) {
                if (wid == 0) {
                    GAS u64* f = (GAS u64*)(wsp + WS_FKV + (size_t)(me - 16) * 128);
#pragma nounroll
                    for (int spin = 0; spin < SPIN_CAP; ++spin) {
                        if (rfl((unsigned)pv) == (unsigned)tagc && rfl((unsigned)(pv >> 32)) == (unsigned)(tagc >> 32)) break;
                        __builtin_amdgcn_s_sleep(1);
                        pv = __hip_atomic_load(f, __ATOMIC_RELAXED, __HIP_MEMORY_SCOPE_AGENT);
                    }
                    if (lane == 0) __hip_atomic_store((GAS u64*)(wsp + WS_EKV + (size_t)(me - 16) * 128), tagc, __ATOMIC_RELAXED, __HIP_MEMORY_SCOPE_AGENT);
                }
                BAR();
                STAMP(5);
#pragma unroll
                for (int k = 0; k < 8; ++k) pld[k] = __builtin_amdgcn_raw_buffer_load_b128(srdW, (unsigned)(lane * 16 + k * 1024), (unsigned)WS_KV + (me - 16) * 65536 + wid * 8192, 16);
                SBAR();
            }
        };
#define JOB(n) job(std::integral_constant<int, n>{})
        JOB(0); JOB(1); JOB(2); JOB(3); JOB(4); JOB(5); JOB(6); JOB(7); JOB(8);
#undef JOB
    };
    jobs(std::integral_constant<int, 0>{});
    STAMP(4);
    if (rb > 0) {
#pragma unroll
        for (int i = 0; i < 8; ++i) *(LAS u32x4*)(lds + half * 65536 + w4 * 8192 + i * 1024 + lane * 16) = pld[i];
        WAITL0(); BAR();
        STAMP(6);
        jobs(std::integral_constant<int, 1>{});
    }
#pragma unroll
    for (int d = 0; d < 2; ++d) {
        const int gt = d ? 7 - wr : wr;
        float lt = (lane & 16) ? l[d][1] : l[d][0];
        if (__builtin_expect(__any(!(lt <= LSAFE)), 0)) {
            const f32x16 mhi = mk, mlo = flip(mk);
            const bf16x8 q0 = rb > 0 ? qf[d][0] : qf[1 - d][0], q1 = rb > 0 ? qf[d][1] : qf[1 - d][1];
            const int b0 = rb > 0 ? gt : 8, b1 = 8 + gt;
            auto sc = [&](int b, float ref) { f32x16 c;
#pragma unroll
                for (int r = 0; r < 16; ++r) c[r] = (b == b1 ? mlo[r] : b == gt ? mhi[r] : 0.f) - ref;
                c = MFMA32(ldsfrag(kvb + b * 4096), q0, c); return MFMA32(ldsfrag(kvb + b * 4096 + 1024), q1, c); };
            float m = -INFINITY;
#pragma nounroll
            for (int b = b0; b <= b1; ++b) m = jmax(sc(b, 0.f), m);
            m = xmax(m);
            f32x4 ls = {0.f, 0.f, 0.f, 0.f};
#pragma unroll
            for (int r = 0; r < 16; ++r) o[d][r] = 0.f;
#pragma nounroll
            for (int b = b0; b <= b1; ++b) { bf16x8 p0, p1; expo(sc(b, m), p0, p1);
                o[d] = MFMA32(ldsfrag(kvb + b * 4096 + 2048), p0, o[d]); o[d] = MFMA32(ldsfrag(kvb + b * 4096 + 3072), p1, o[d]);
                ls = MFMA16(ones, p0, ls); ls = MFMA16(ones, p1, ls); }
            lt = (lane & 16) ? ls[1] : ls[0];
        }
        const float inv = __builtin_amdgcn_rcpf(lt);
        const unsigned base = (unsigned)WS_O + (((br * 128 + rb * 16 + gt * 2) * 32) + hp * 2 + wc) * 1024;
#pragma unroll
        for (int e = 0; e < 2; ++e) {
            const f32x16& x = o[d];
            const u32x4 v = {cvtpk(fmul_s(x[8 * e], inv), fmul_s(x[8 * e + 1], inv)), cvtpk(fmul_s(x[8 * e + 2], inv), fmul_s(x[8 * e + 3], inv)), cvtpk(fmul_s(x[8 * e + 4], inv), fmul_s(x[8 * e + 5], inv)), cvtpk(fmul_s(x[8 * e + 6], inv), fmul_s(x[8 * e + 7], inv))};
            __builtin_amdgcn_raw_buffer_store_b128(v, srdW, base + (r32 >> 4) * 32768 + hh * 256 + (r32 & 15) * 16 + e * 512, 0, 16);
        }
    }
    STAMP(7);
    auto phase3 = [&](auto HC) {
        constexpr int H = decltype(HC)::value;
        const int lane = lane_id(), r16 = lane & 15, g4 = lane >> 4, q4 = (lane & 15) >> 2, p4 = lane & 3;
        GAS char* ep = (GAS char*)wsp + WS_EO + (lane < 16 ? (size_t)(grp + lane) * 128 + hp * 8 : (size_t)me * 128 + (lane & 15) * 8);
        u64 eo;
        asm volatile("global_load_dwordx2 %0, %1, off sc1" : "=v"(eo) : "v"(ep) : "memory");
        unsigned woff[8];
        { const int row = 16 * (g4 >> 1) + 4 * (g4 & 1) + q4;
#pragma unroll
          for (int n = 0; n < 8; ++n) woff[n] = 131072 + H * 8192 + row * 256 + ((n ^ (row & 7)) << 5) + p4 * 8; }
        bf16x8 wf[2][8];
        auto wread = [&](int s, int n) {
            if (s == 2 || s == 12) { woff[n] -= 131072; PIN(woff[n]); } else if (s == 6 || s == 10) { woff[n] += 65536; PIN(woff[n]); }
            LAS const char* p = lds + woff[n] + (((s + 8) % 10) & 3) * 16384;
            const s16x4 lo = trread(p), hi = trread(p + 2048); wf[s & 1][n] = __builtin_shufflevector(lo, hi, 0, 1, 2, 3, 4, 5, 6, 7); };
        waitv<5>(); WAITL0(); BAR();
        wo_steps(2, 2 + WPRE);
#pragma unroll
        for (int n = 0; n < 8; ++n) { wread(0, n); wread(1, n); }
        waitv<2 * WPRE>(); PIN(eo); WAITL0(); BAR();
        if (H == 0 && wid == 0) {
            const u64 tag = eo + TAGK;
            if (lane < 16) __hip_atomic_store((GAS u64*)(ep - WS_EO + WS_FO), tag, __ATOMIC_RELAXED, __HIP_MEMORY_SCOPE_AGENT);
        }
        wo_steps(2 + WPRE, 12);
        if (H == 0 && wid == 0) {
            const u64 tag = eo + TAGK;
            GAS u64* f = (GAS u64*)(wsp + WS_FO + (size_t)me * 128 + (lane & 15) * 8);
            const bool mine = lane >= 16 && lane < 32;
#pragma nounroll
            for (int spin = 0; spin < SPIN_CAP; ++spin) {
                const u64 v = __hip_atomic_load(f, __ATOMIC_RELAXED, __HIP_MEMORY_SCOPE_AGENT);
                if (__all(!mine || v == tag)) break;
                __builtin_amdgcn_s_sleep(1);
            }
            if (mine) __hip_atomic_store((GAS u64*)ep, tag, __ATOMIC_RELAXED, __HIP_MEMORY_SCOPE_AGENT);
        }
        waitv<0>(); BAR();
        STAMP(8);
        bf16x8 of[OW][2];
        const unsigned obase = (unsigned)WS_O + ((br * 128 + rb * 16 + th * 8 + w4 * 2) * 32 + H * 16) * 1024 + lane * 16;
        auto oload = [&](int s, int t) { of[s % OW][t] = __builtin_bit_cast(bf16x8, __builtin_amdgcn_raw_buffer_load_b128(srdW, obase + t * 32768 + s * 1024, 0, 16)); };
#pragma unroll
        for (int s = 0; s < OW - 1; ++s) { oload(s, 0); oload(s, 1); SBAR(); }
        f32x4 c2[2][8];
#pragma unroll
        for (int t = 0; t < 2; ++t)
#pragma unroll
            for (int n = 0; n < 8; ++n) c2[t][n] = (f32x4){0.f, 0.f, 0.f, 0.f};
        const unsigned voffW0 = (lane >> 4) * 2048 + (((lane & 15) ^ ((lane >> 4) << 1)) << 4), voffW1 = (4 + (lane >> 4)) * 2048 + (((lane & 15) ^ ((4 + (lane >> 4)) << 1)) << 4);
        {
            auto step = [&](int s) {
#pragma unroll
                for (int n = 0; n < 8; ++n) {
                    c2[0][n] = MFMA16(wf[s & 1][n], of[s % OW][0], c2[0][n]); c2[1][n] = MFMA16(wf[s & 1][n], of[s % OW][1], c2[1][n]);
                    SBAR();
                    if (s + 2 < 16) wread(s + 2, n);
                    if (n == PA0 + 2 * H && s + OW - 1 < 16) oload(s + OW - 1, 0);
                    if (n == PA0 + 4 + 2 * H && s + OW - 1 < 16) oload(s + OW - 1, 1);
                    if (s >= 4 && s < 8 && (n == PA0 + 2 - 2 * H || n == PA0 + 6 - 2 * H)) dma16(ldsb + ((s + 16) % 10) * 16384 + wid * 2048 + (n >= 4) * 1024, n >= 4 ? voffW1 : voffW0, srdWo, sW + (s + 8) * 65536);
                    SBAR();
                } };
#pragma unroll
            for (int s = 0; s < 4; ++s) { step(s); if (s == 0) STAMP(11); }
            WAITL0(); BAR();
#pragma unroll
            for (int s = 4; s < 10; ++s) step(s);
            STAMP(13);
            waitv<4>(); BAR();
            STAMP(14);
#pragma unroll
            for (int s = 10; s < 15; ++s) step(s);
#pragma unroll
            for (int n = 4 - 4 * H; n < 8 - 4 * H; ++n) { c2[0][n] = MFMA16(wf[1][n], of[15 % OW][0], c2[0][n]); c2[1][n] = MFMA16(wf[1][n], of[15 % OW][1], c2[1][n]); }
            SBAR();
#pragma unroll
            for (int n = 0; n < 4; ++n) {
                c2[0][4 * H + n] = MFMA16(wf[1][4 * H + n], of[15 % OW][0], c2[0][4 * H + n]); c2[1][4 * H + n] = MFMA16(wf[1][4 * H + n], of[15 % OW][1], c2[1][4 * H + n]);
                SBAR();
#pragma unroll
                for (int t = 0; t < 2; ++t) *(LAS f32x4*)(lds + 65536 + wid * 8192 + (t * 4 + n) * 1024 + lane * 16) = c2[t][4 - 4 * H + n];
                SBAR();
            }
            STAMP(9);
            WAITL0(); BAR();
            STAMP(15);
            GAS u16* outp = (GAS u16*)outb + (size_t)(rb * 256 + th * 128 + w4 * 32 + r16) * 2048 + br * 1024 + ct * 128 + H * 64 + (g4 & 1) * 16 + (g4 >> 1) * 8;
#pragma unroll
            for (int t = 0; t < 2; ++t)
#pragma unroll
                for (int n = 0; n < 4; n += 2) {
                    const f32x4 x = c2[t][4 * H + n] + *(LAS const f32x4*)(lds + 65536 + (wid ^ 4) * 8192 + (t * 4 + n) * 1024 + lane * 16);
                    const f32x4 y = c2[t][4 * H + n + 1] + *(LAS const f32x4*)(lds + 65536 + (wid ^ 4) * 8192 + (t * 4 + n + 1) * 1024 + lane * 16);
                    const auto u0 = __builtin_amdgcn_permlane16_swap(cvtpk(x[0], x[1]), cvtpk(y[0], y[1]), false, false);
                    const auto u1 = __builtin_amdgcn_permlane16_swap(cvtpk(x[2], x[3]), cvtpk(y[2], y[3]), false, false);
                    { const u32x4 vv = {u0[0], u1[0], u0[1], u1[1]}; GAS u16* pp = outp + (size_t)t * 16 * 2048 + n * 16; asm volatile("global_store_dwordx4 %0, %1, off sc0 sc1\n\ts_nop 1" :: "v"(pp), "v"(vv) : "memory"); }
                }
        }
    };
    if (half == 0) phase3(std::integral_constant<int, 0>{}); else phase3(std::integral_constant<int, 1>{});
}

extern "C" void kernel_launch(void* const* d_in, const int* in_sizes, int n_in, void* d_out, int out_size, void* d_ws, size_t ws_size, hipStream_t stream) {
    Args a;
    a.x[0] = (const u16*)d_in[0]; a.w[0] = (const u16*)d_in[1]; a.wo[0] = (const u16*)d_in[2];
    a.x[1] = (const u16*)d_in[3]; a.w[1] = (const u16*)d_in[4]; a.wo[1] = (const u16*)d_in[5];
    a.out = (u16*)d_out; a.ws = (char*)d_ws;
    fused_kernel<<<256, 512, 0, stream>>>(a);
}
```

```cpp
#define PT3W 1
#include <hip/hip_runtime.h>
#include <hip/hip_bf16.h>
#include <type_traits>
typedef short bf16x8 __attribute__((ext_vector_type(8)));
typedef short s16x4 __attribute__((ext_vector_type(4)));
typedef float f32x16 __attribute__((ext_vector_type(16)));
typedef float f32x4 __attribute__((ext_vector_type(4)));
typedef float f32x2 __attribute__((ext_vector_type(2)));
typedef __bf16 bf16x2 __attribute__((ext_vector_type(2)));
typedef unsigned u32x4 __attribute__((ext_vector_type(4)));
typedef double f64x8 __attribute__((ext_vector_type(8)));
typedef double f64x2 __attribute__((ext_vector_type(2)));
typedef unsigned short u16;
typedef unsigned long long u64;
#define LAS __attribute__((address_space(3)))
#define GAS __attribute__((address_space(1)))
#define DI __device__ __forceinline__
#define MFMA16(a, b, c) __builtin_amdgcn_mfma_f32_16x16x32_bf16((a), (b), (c), 0, 0, 0)
#define MFMA32(a, b, c) __builtin_amdgcn_mfma_f32_32x32x16_bf16((a), (b), (c), 0, 0, 0)
#define SBAR() __builtin_amdgcn_sched_barrier(0)
#define BAR() do { SBAR(); asm volatile("s_barrier" ::: "memory"); SBAR(); } while (0)
#define PIN(x) asm volatile("" : "+v"(x))
#define WAITL0() __builtin_amdgcn_s_waitcnt(0xC07F)
template <int N> DI void waitv() { asm volatile("s_waitcnt vmcnt(%0)" :: "i"(N) : "memory"); }
DI unsigned rfl(unsigned x) { return __builtin_amdgcn_readfirstlane(x); }
DI int lane_id() { int l; asm volatile("v_mbcnt_lo_u32_b32 %0, -1, 0\n\tv_mbcnt_hi_u32_b32 %0, -1, %0" : "=v"(l)); return l; }
DI unsigned cvtpk(float lo, float hi) { f32x2 v = {lo, hi}; bf16x2 b = __builtin_convertvector(v, bf16x2); return __builtin_bit_cast(unsigned, b); }
DI void dma16(unsigned m0v, unsigned voff, __amdgpu_buffer_rsrc_t srd, unsigned soff) {
    asm volatile("s_mov_b32 m0, %0\n\ts_nop 0\n\tbuffer_load_dwordx4 %1, %2, %3 offen lds" :: "s"(m0v), "v"(voff), "s"(srd), "s"(soff) : "memory");
}
DI void dma4(unsigned m0v, unsigned voff, __amdgpu_buffer_rsrc_t srd, unsigned soff) {
    asm volatile("s_mov_b32 m0, %0\n\ts_nop 0\n\tbuffer_load_dword %1, %2, %3 offen lds" :: "s"(m0v), "v"(voff), "s"(srd), "s"(soff) : "memory");
}
DI s16x4 trread(LAS const char* p) { return __builtin_amdgcn_ds_read_tr16_b64_v4i16((LAS s16x4*)p); }
DI bf16x8 ldsfrag(LAS const char* p) { return *(LAS const bf16x8*)p; }
DI float max3f(float a, float b, float c) { return __builtin_fmaxf(__builtin_fmaxf(a, b), c); }
DI float fadd_s(float a, float b) { return a + b; }
DI float fsub_s(float a, float b) { return a - b; }
DI float fmul_s(float a, float b) { return a * b; }
DI bf16x8 pack8(float a, float b, float c, float d, float e, float f, float g, float h) { u32x4 v = {cvtpk(a, b), cvtpk(c, d), cvtpk(e, f), cvtpk(g, h)}; return __builtin_bit_cast(bf16x8, v); }

#define SW64(a, b) "v_mov_b64 %[t], %" #a "\n\tv_mov_b64 %" #a ", %" #b "\n\tv_mov_b64 %" #b ", %[t]\n\t"
template <int F> DI void swap_q(int w, bf16x8 (&q)[2][2], float& n0, float& n1, f32x16& cm, float aone) {
    f64x2 a = __builtin_bit_cast(f64x2, q[0][0]), b = __builtin_bit_cast(f64x2, q[0][1]), c = __builtin_bit_cast(f64x2, q[1][0]), d = __builtin_bit_cast(f64x2, q[1][1]); double t;
    asm volatile("s_cmp_lg_u32 %[w], %[f]\n\ts_cbranch_scc1 1f\n\tv_swap_b32 %8, %9\n\t" SW64(0, 4) SW64(1, 5) "v_mfma_f32_32x32x2_f32 %10, %[one], %8, 0\n\t" SW64(2, 6) SW64(3, 7) "s_nop 1\n1:"
        : "+v"(a[0]), "+v"(a[1]), "+v"(b[0]), "+v"(b[1]), "+v"(c[0]), "+v"(c[1]), "+v"(d[0]), "+v"(d[1]), "+v"(n0), "+v"(n1), "+v"(cm), [t] "=&v"(t) : [w] "s"(w), [f] "i"(F), [one] "v"(aone) : "scc");
    q[0][0] = __builtin_bit_cast(bf16x8, a); q[0][1] = __builtin_bit_cast(bf16x8, b); q[1][0] = __builtin_bit_cast(bf16x8, c); q[1][1] = __builtin_bit_cast(bf16x8, d);
}
template <int F> DI void pv_sel(int w, f32x16& lo, f32x16& hi, bf16x8 a, u32x4 b) {
    asm volatile("s_cmp_gt_u32 %[w], %[f]\n\ts_cbranch_scc1 1f\n\tv_mfma_f32_32x32x16_bf16 %1, %2, %3, %1\n\ts_branch 2f\n1:\n\tv_mfma_f32_32x32x16_bf16 %0, %2, %3, %0\n2:"
        : "+v"(lo), "+v"(hi) : "v"(a), "v"(b), [w] "s"(w), [f] "i"(F) : "scc");
}
template <int F> DI void ls_sel(int w, f32x4& lo, f32x4& hi, bf16x8 a, u32x4 b) {
    asm volatile("s_cmp_gt_u32 %[w], %[f]\n\ts_cbranch_scc1 1f\n\tv_mfma_f32_16x16x32_bf16 %1, %2, %3, %1\n\ts_branch 2f\n1:\n\tv_mfma_f32_16x16x32_bf16 %0, %2, %3, %0\n2:"
        : "+v"(lo), "+v"(hi) : "v"(a), "v"(b), [w] "s"(w), [f] "i"(F) : "scc");
}
constexpr int LDS_A0 = 0, LDS_A1 = 32768, LDS_B = 81920, A_SLOT = 16384, B_SLOT = 24576;
constexpr int LDS_TOTAL = 163840;
constexpr size_t WS_FKV = 0, WS_EKV = 32768, WS_FO = 65536, WS_EO = 98304, WS_KV = 1 << 20, WS_O = 17 << 20, WS_END = 25 << 20, WS_ST = 32 << 20;
constexpr u64 TAGK = 0x9E3779B97F4A7C15ull;
constexpr int SPIN_CAP = 400000;
constexpr float QSCALE = 0.17677669529663687f * 1.4426950408889634f;

#define PUBJ 3
#define POLJ 5
#define WRJ 9
#define SPJ 4
#define OW 5
#define PA0 1
#define WPRE 4
#define LSAFE 1.152921504606847e18f
#define KROT 0
#define CODEPF 32
#define TOUCH 0
#define PD 5
constexpr int LDS_SCR = 155648;
#define PT3X 0
struct Args { const u16* x[2]; const u16* w[2]; const u16* wo[2]; u16* out; char* ws; };

#define STAMP(i) do {} while (0)

__global__ __launch_bounds__(512, 2) void fused_kernel(Args a) {
    __shared__ __attribute__((aligned(16))) char smem[LDS_TOTAL];
    LAS char* lds = (LAS char*)smem;
    const unsigned ldsb = (unsigned)(size_t)smem;
    const int tid = threadIdx.x, lane = tid & 63;
    const int wid = rfl(tid >> 6), half = wid >> 2, w4 = wid & 3;
    const int bid = blockIdx.x, g = bid & 7, j = bid >> 3;
    const int br = g >> 2, rb = ((g >> 1) & 1) * 4 + (j & 3), hp = (g & 1) * 8 + (j >> 2);
    const int me = (br * 8 + rb) * 16 + hp, grp = me & ~15;
    STAMP(0);
    if (CODEPF && wid == 7) {
        u64 pc; asm volatile("s_getpc_b64 %0" : "=s"(pc));
        const __amdgpu_buffer_rsrc_t srdC = __builtin_amdgcn_make_buffer_rsrc((void*)pc, (short)0, CODEPF * 1024, 0x00020000);
        if (lane < 13) dma4(ldsb + LDS_SCR, (j * 13 + lane) * 128, srdC, 0);
    }
    char* wsp = a.ws; const u16* wop = a.wo[br]; u16* outb = a.out;
    asm volatile("" : "+s"(wsp), "+s"(wop), "+s"(outb));
    u64 ew = __hip_atomic_load((GAS u64*)(wsp + WS_EKV + (size_t)(lane == 0 || rb == 0 ? me : me - 16) * 128), __ATOMIC_RELAXED, __HIP_MEMORY_SCOPE_AGENT);
    const __amdgpu_buffer_rsrc_t srdA = __builtin_amdgcn_make_buffer_rsrc((void*)(a.x[br] + (size_t)rb * 256 * 1024), (short)0, 256 * 2048, 0x00020000);
    const __amdgpu_buffer_rsrc_t srdB = __builtin_amdgcn_make_buffer_rsrc((void*)(a.w[br]), (short)0, 1024 * 6144, 0x00020000);
    const int wr = 2 * half + (w4 >> 1), wc = w4 & 1;
    const int r16 = lane & 15, g4 = lane >> 4, q4 = (lane & 15) >> 2, p4 = lane & 3;
    f32x4 acc[4][6];
    const int r32 = lane & 31, hh = lane >> 5;
    f32x16 mk;
#pragma unroll
    for (int r = 0; r < 16; ++r) mk[r] = ((r & 3) + 8 * (r >> 2) <= r32 - 4 * hh) ? 0.f : -INFINITY;
    {
        const unsigned voffE = (lane >> 3) * 2048 + (((lane & 7) ^ (lane >> 4)) << 4);
        const unsigned voffO = (8 + (lane >> 3)) * 2048 + (((lane & 7) ^ (4 + (lane >> 4))) << 4);
        const unsigned voffB0 = (lane >> 3) * 6144 + (((lane & 7) ^ (((lane >> 4) & 1) << 1)) << 4);
        const unsigned voffB1 = (lane >> 3) * 6144 + (((lane & 7) ^ ((((lane >> 4) & 1) << 1) | 4)) << 4);
        const int gt0 = (w4 & 1) ? 7 - (w4 >> 1) : (w4 >> 1);
        const int gt1 = (w4 & 1) ? 5 - (w4 >> 1) : 2 + (w4 >> 1);
        const unsigned sA0 = gt0 * 32 * 2048, sA1 = gt1 * 32 * 2048;
        const unsigned sB = hp * 128 + (2 * w4) * 8 * 6144;
        unsigned aoff[2], boff[2];
#pragma unroll
        for (int s = 0; s < 2; ++s) aoff[s] = (half ? LDS_A1 : LDS_A0) + ((w4 >> 1) * 64 + r16) * 128 + (((4 * s + g4) ^ ((r16 >> 1) & 7)) << 4);
#pragma unroll
        for (int c = 0; c < 2; ++c) boff[c] = LDS_B + (8 * g4 + q4) * 128 + (((wc * 2 + c) ^ ((q4 >> 1) | ((g4 & 1) << 1))) << 5) + p4 * 8;
#pragma unroll
        for (int i = 0; i < 4; ++i)
#pragma unroll
            for (int s = 0; s < 6; ++s) acc[i][s] = (f32x4){0.f, 0.f, 0.f, 0.f};
        bf16x8 af[4][2], bfr[6][2];
        const unsigned voffT = (16 * (rb & 3) + 4 * w4 + lane / 3) * 6144 + (lane % 3) * 2048 + hp * 128;
        const bool slowc = ((((size_t)a.w[br] >> 7) + hp) & 7) == 3;
        auto run = [&](auto HC, auto TC) {
            constexpr int H = decltype(HC)::value;
            constexpr int TW = decltype(TC)::value;
            auto touch = [&](int kt) { if (lane < 12) dma4(ldsb + LDS_SCR, voffT, srdB, kt * (64 * 6144)); };
            auto piece = [&](int kt0, int s2, int s3i, int i) {
                const int kt = KROT ? (kt0 + KROT * g) & 15 : kt0;
                const unsigned soB = sB + kt * (64 * 6144), dB = ldsb + LDS_B + s3i * B_SLOT + w4 * 2048;
                const unsigned soA0 = sA0 + kt * 128, dA0 = ldsb + LDS_A0 + s2 * A_SLOT + w4 * 4096;
                const unsigned soA1 = sA1 + kt * 128, dA1 = ldsb + LDS_A1 + s3i * A_SLOT + w4 * 4096;
                if (H == 0) {
                    if (i == 0) dma16(dA1 + 3 * 1024, voffO, srdA, soA1 + 32768);
                    else { const int k = i - 1; dma16(dB + (k >> 1) * 8192 + (k & 1) * 1024, (k & 1) ? voffB1 : voffB0, srdB, soB + (k >> 1) * 2048 + (k & 1) * 8 * 6144); }
                } else dma16((i < 4 ? dA0 : dA1) + (i & 3) * 1024, (i & 1) ? voffO : voffE, srdA, (i < 4 ? soA0 : soA1) + ((i >> 1) & 1) * 32768);
            };
#pragma unroll
            for (int i = 0; i < 7; ++i) piece(0, 0, 0, i);
#pragma unroll
            for (int i = 0; i < 7; ++i) piece(1, 1, 1, i);
            if (H == 1) {
                if (PT3W && w4 < 2 && lane < 48) { const int hp3 = (g & 1) * 8 + ((3 - (int)((size_t)a.w[br] >> 7)) & 7);
                    dma4(ldsb + LDS_SCR, (32 * j + 16 * w4 + lane / 3) * 6144 + (lane % 3) * 2048 + hp3 * 128, srdB, 0); }
                if (PT3X && w4 == 2) { const int kt3 = (3 - (int)((size_t)a.x[br] >> 7)) & 7;
                    dma4(ldsb + LDS_SCR, (32 * (hp & 7) + (lane & 31)) * 2048 + (kt3 + 8 * (lane >> 5)) * 128, srdA, 0); }
            }
#pragma unroll
            for (int i = 0; i < 4; ++i) asm volatile("" : "+v"(acc[i][0]), "+v"(acc[i][1]), "+v"(acc[i][2]), "+v"(acc[i][3]), "+v"(acc[i][4]), "+v"(acc[i][5]));
            PIN(mk);
            if (H == 0) waitv<7>(); else waitv<10>();
            BAR();
            if (TW) {
#pragma unroll
                for (int kt = 3; kt < PD; ++kt) touch(kt);
            }
            if (H == 1) { waitv<7>(); BAR(); }
            STAMP(1);
            int s3 = 0;
            auto Lseg = [&](int t, bool issue) {
                const unsigned aslot = H ? s3 * A_SLOT : (t & 1) * A_SLOT;
                const unsigned bslot = s3 * B_SLOT;
                const int s3n = (s3 == 0) ? 2 : s3 - 1;
                auto rdA = [&](int i) { LAS const char* pa = lds + aoff[i >> 1] + aslot + (i & 1) * 4096; af[(i & 1) * 2][i >> 1] = ldsfrag(pa); af[(i & 1) * 2 + 1][i >> 1] = ldsfrag(pa + 2048); };
                auto rdB = [&](int i) { const int s = i / 6, seg = (i % 6) >> 1, c = i & 1; LAS const char* pb = lds + boff[c] + bslot;
                    s16x4 lo = trread(pb + seg * 8192 + s * 4096), hi = trread(pb + seg * 8192 + s * 4096 + 512);
                    bfr[seg * 2 + c][s] = __builtin_shufflevector(lo, hi, 0, 1, 2, 3, 4, 5, 6, 7); };
                if (TW && TOUCH == 2 && issue) touch(t + PD);
#pragma unroll
                for (int i = 0; i < 16; ++i) {
                    if (issue && (i & 1) == 0 && i / 2 < 7) piece(t + 2, t & 1, s3n, i / 2);
                    if (TW && TOUCH == 3 && issue && i == 13) touch(t + PD);
                    if (i < 4) rdA(i); else rdB(i - 4);
                }
                if (TW && TOUCH == 1 && issue) touch(t + PD);
                s3 = (s3 == 2) ? 0 : s3 + 1;
            };
            auto Mseg = [&]() {
                __builtin_amdgcn_s_setprio(1);
#pragma unroll
                for (int s = 0; s < 2; ++s)
#pragma unroll
                    for (int m = 0; m < 4; ++m)
#pragma unroll
                        for (int n = 0; n < 6; ++n) acc[m][n] = n < 4 ? MFMA16(bfr[n][s], af[m][s], acc[m][n]) : MFMA16(af[m][s], bfr[n][s], acc[m][n]);
                __builtin_amdgcn_s_setprio(0);
            };
            if (TW && TOUCH != 2) { Lseg(0, true); WAITL0(); BAR(); Mseg(); waitv<8 + PD - 3>(); BAR(); }
            for (int t = (TW && TOUCH != 2) ? 1 : 0; t < 14; ++t) {
                Lseg(t, true);
                if (H == 1) waitv<10>();
                WAITL0(); BAR();
                Mseg();
                waitv<7 + (TW ? (TOUCH == 2 ? 1 : 2) : 0)>();
                BAR();
            }
            Lseg(14, false); if (H == 1) waitv<3>(); WAITL0(); BAR(); Mseg(); waitv<0>(); BAR();
            Lseg(15, false); WAITL0(); BAR(); Mseg(); if (H == 0) BAR();
        };
        if (half == 1) run(std::integral_constant<int, 1>{}, std::integral_constant<int, 0>{});
        else if (TOUCH && slowc) run(std::integral_constant<int, 0>{}, std::integral_constant<int, 1>{});
        else run(std::integral_constant<int, 0>{}, std::integral_constant<int, 0>{});
    }
    STAMP(2);
    const u64 tagp = (((u64)__builtin_amdgcn_readlane((unsigned)(ew >> 32), 0) << 32) | __builtin_amdgcn_readlane((unsigned)ew, 0)) + TAGK;
    const u64 tagc = (((u64)__builtin_amdgcn_readlane((unsigned)(ew >> 32), 1) << 32) | __builtin_amdgcn_readlane((unsigned)ew, 1)) + TAGK;
    const __amdgpu_buffer_rsrc_t srdW = __builtin_amdgcn_make_buffer_rsrc((void*)wsp, (short)0, (int)WS_END, 0x00020000);
    const __amdgpu_buffer_rsrc_t srdWo = __builtin_amdgcn_make_buffer_rsrc((void*)(wop), (short)0, 1024 * 2048, 0x00020000);
    const int ct = hp & 7, th = hp >> 3;
    const unsigned sW = ct * 256 + (half * 512 + w4 * 8) * 2048;
    auto wo_steps = [&](int s0, int s1) {
        const int ln = lane_id();
        const unsigned voffW0 = (ln >> 4) * 2048 + (((ln & 15) ^ ((ln >> 4) << 1)) << 4), voffW1 = (4 + (ln >> 4)) * 2048 + (((ln & 15) ^ ((4 + (ln >> 4)) << 1)) << 4);
#pragma unroll
        for (int s = s0; s < s1; ++s) { const unsigned d = ldsb + ((s + 8) % 10) * 16384 + wid * 2048; dma16(d, voffW0, srdWo, sW + s * 65536); dma16(d + 1024, voffW1, srdWo, sW + s * 65536); } };
    bf16x8 qf[2][2];
    u32x4 kv[2][2], vv[2][2];
    const unsigned kpos = (g4 >> 1) * 1024 + (g4 & 1) * 512 + r16 * 16;
    const unsigned vpos = 2048 + ((g4 & 1) * 32 + (g4 >> 1) * 16 + r16) * 16;
    const unsigned gkv = (unsigned)WS_KV + me * 65536 + wc * 32768;
    auto kvstore = [&](int i) { const int mi = i >> 2, s = (i >> 1) & 1; const unsigned so = gkv + (mi ? 7 - wr : wr) * 4096;
        if (i & 1) __builtin_amdgcn_raw_buffer_store_b128(vv[mi][s], srdW, so + vpos + s * 1024, 0, 16);
        else __builtin_amdgcn_raw_buffer_store_b128(kv[mi][s], srdW, so + kpos + s * 256, 0, 16); };
    {
#pragma unroll
        for (int mi = 0; mi < 2; ++mi)
#pragma unroll
            for (int s = 0; s < 2; ++s) {
                const f32x4 k0 = acc[2 * mi + s][2], k1 = acc[2 * mi + s][3], v0 = acc[2 * mi + s][4], v1 = acc[2 * mi + s][5];
                kv[mi][s] = (u32x4){cvtpk(k0[0], k0[1]), cvtpk(k0[2], k0[3]), cvtpk(k1[0], k1[1]), cvtpk(k1[2], k1[3])};
                const auto ra = __builtin_amdgcn_permlane32_swap(cvtpk(v0[0], v0[1]), cvtpk(v1[0], v1[1]), false, false);
                const auto rbb = __builtin_amdgcn_permlane32_swap(cvtpk(v0[2], v0[3]), cvtpk(v1[2], v1[3]), false, false);
                vv[mi][s] = (u32x4){ra[0], rbb[0], ra[1], rbb[1]};
                const unsigned blk = wc * 65536 + 32768 + (mi ? 7 - wr : wr) * 4096;
                *(LAS u32x4*)(lds + blk + kpos + s * 256) = kv[mi][s];
                *(LAS u32x4*)(lds + blk + vpos + s * 1024) = vv[mi][s];
            }
        if (rb < 7 && half == 0) {
#pragma unroll
            for (int i = 0; i < 8; ++i) kvstore(i);
        }
#pragma unroll
        for (int mi = 0; mi < 2; ++mi) {
            u32x4 x[2], y0, y1;
#pragma unroll
            for (int s = 0; s < 2; ++s) { const f32x4 q0 = acc[2 * mi + s][0] * QSCALE, q1 = acc[2 * mi + s][1] * QSCALE;
                x[s] = (u32x4){cvtpk(q0[0], q0[1]), cvtpk(q0[2], q0[3]), cvtpk(q1[0], q1[1]), cvtpk(q1[2], q1[3])}; }
#pragma unroll
            for (int i = 0; i < 4; ++i) { const auto u = __builtin_amdgcn_permlane16_swap(x[0][i], x[1][i], false, false);
                const auto v = __builtin_amdgcn_permlane32_swap(u[0], u[1], false, false); y0[i] = v[0]; y1[i] = v[1]; }
            qf[mi][0] = __builtin_bit_cast(bf16x8, y0); qf[mi][1] = __builtin_bit_cast(bf16x8, y1);
        }
    }
    f32x16 o[2]; float nm[2]; f32x4 l[2];
    LAS const char* kvb = lds + wc * 65536 + lane * 16;
    bf16x8 ones;
    { const short v = ((lane ^ (lane >> 4)) & 1) ? (short)0 : (short)0x3f80;
#pragma unroll
      for (int i = 0; i < 8; ++i) ones[i] = v; }
    auto jmax = [&](const f32x16& s, float m) {
#pragma unroll
        for (int r = 0; r < 16; r += 2) m = max3f(m, s[r], s[r + 1]);
        return m; };
    auto xmax = [&](float m) { const auto r = __builtin_amdgcn_permlane32_swap(__float_as_uint(m), __float_as_uint(m), false, false); return __builtin_fmaxf(__uint_as_float(r[0]), __uint_as_float(r[1])); };
    auto expo = [&](const f32x16& s, bf16x8& p0, bf16x8& p1) {
        float t[16];
#pragma unroll
        for (int r = 0; r < 16; ++r) t[r] = __builtin_amdgcn_exp2f(s[r]);
        p0 = pack8(t[0], t[1], t[2], t[3], t[4], t[5], t[6], t[7]); p1 = pack8(t[8], t[9], t[10], t[11], t[12], t[13], t[14], t[15]); };
    auto flip = [&](const f32x16& m) { f32x16 c;
#pragma unroll
        for (int r = 0; r < 16; ++r) c[r] = __uint_as_float(__float_as_uint(m[r]) ^ 0xff800000u);
        return c; };
    {
        f32x16 s[2];
#pragma unroll
        for (int d = 0; d < 2; ++d) {
            LAS const char* p = kvb + (8 + (d ? 7 - wr : wr)) * 4096;
            s[d] = MFMA32(ldsfrag(p), qf[d][0], mk); s[d] = MFMA32(ldsfrag(p + 1024), qf[d][1], s[d]);
        }
        wo_steps(0, 2);
#pragma unroll
        for (int d = 0; d < 2; ++d) {
            nm[d] = -xmax(jmax(s[d], -INFINITY)); PIN(nm[d]);
        }
    }
    WAITL0(); BAR();
    STAMP(3);
    u32x4 pld[8]; u64 pv = 0;
    const bool st1 = half == 1 && rb < 7;
    const float aone = lane < 32 ? 1.f : 0.f;
    auto jobs = [&](auto PART) {
        constexpr int P2 = decltype(PART)::value;
        constexpr int NJ = 9, F0 = P2 ? 1 : 2;
        auto SET = [](int j) { return (!P2 && j == 0) ? 1 : 0; };
        auto MSK = [](int j) { return P2 ? (j == 0 || j == 8) : j < 2; };
        auto badr = [&](int j) -> LAS const char* {
            const int f = j - F0;
            const int b = P2 ? (j == 0 ? 7 - wr : j == 8 ? wr : (f < wr ? 8 - wr + f : f + 1)) : 8 + (j == 0 ? 7 - wr : j == 1 ? wr : (f < wr ? f : f - wr));
            return kvb + b * 4096; };
        f32x16 S[2], cm, zero16; bf16x8 kf[2][2], vf[2][2]; u32x4 p0, p1; LAS const char* ad[3];
#pragma unroll
        for (int r = 0; r < 16; ++r) zero16[r] = 0.f;
        auto rdk = [&](int j) { ad[j % 3] = badr(j); kf[j & 1][0] = ldsfrag(ad[j % 3]); kf[j & 1][1] = ldsfrag(ad[j % 3] + 1024); };
        auto mkc = [&](int j) {
            if (MSK(j)) {
#pragma unroll
                for (int r = 0; r < 16; ++r) cm[r] = fadd_s(mk[r], nm[SET(j)]);
            } else cm = __builtin_amdgcn_mfma_f32_32x32x2f32(aone, nm[SET(j)], zero16, 0, 0, 0);
            PIN(cm); };
        rdk(0); rdk(1); vf[0][0] = ldsfrag(ad[0] + 2048);
        mkc(0);
        S[0] = MFMA32(kf[0][0], qf[SET(0)][0], cm); S[0] = MFMA32(kf[0][1], qf[SET(0)][1], S[0]);
        SBAR();
        auto job = [&](auto JC) {
            constexpr int j = decltype(JC)::value;
            f32x16& s = S[j & 1];
            auto pvm = [&](auto JJ, bf16x8 a, u32x4 b, bool first) { constexpr int jj = decltype(JJ)::value, f = jj - F0, T = P2 ? (jj == 0 ? 1 : 0) : (jj == 1 ? 0 : 1);
                if constexpr (f >= 0 && f < 3) pv_sel<f>(wr, o[P2 ? 1 : 0], o[P2 ? 0 : 1], a, b);
                else o[T] = MFMA32(a, __builtin_bit_cast(bf16x8, b), first ? zero16 : o[T]); };
            auto lsm = [&](auto JJ, u32x4 b, bool first) { constexpr int jj = decltype(JJ)::value, f = jj - F0, T = P2 ? (jj == 0 ? 1 : 0) : (jj == 1 ? 0 : 1);
                if constexpr (f >= 0 && f < 3) ls_sel<f>(wr, l[P2 ? 1 : 0], l[P2 ? 0 : 1], ones, b);
                else l[T] = MFMA16(ones, __builtin_bit_cast(bf16x8, b), first ? ((f32x4){0.f, 0.f, 0.f, 0.f}) : l[T]); };
#define EX(i) s[i] = __builtin_amdgcn_exp2f(s[i])
            if (!P2 && j == POLJ) pv = __hip_atomic_load((GAS u64*)(wsp + WS_FKV + (size_t)(rb > 0 ? me - 16 : me) * 128), __ATOMIC_RELAXED, __HIP_MEMORY_SCOPE_AGENT);
            if (j + 1 < NJ && (MSK(j + 1) || j + 1 == F0)) mkc(j + 1);
            if (j + 1 == F0) swap_q<0>(wr, qf, nm[0], nm[1], cm, aone);
            if (j + 1 == F0 + 1) swap_q<1>(wr, qf, nm[0], nm[1], cm, aone);
            if (j + 1 == F0 + 2) swap_q<2>(wr, qf, nm[0], nm[1], cm, aone);
            if (j + 1 == F0 + 3) swap_q<3>(wr, qf, nm[0], nm[1], cm, aone);
            if (j + 1 < NJ) S[(j + 1) & 1] = MFMA32(kf[(j + 1) & 1][0], qf[0][0], cm);
            SBAR();
            vf[j & 1][1] = ldsfrag(ad[j % 3] + 3072);
            EX(0); EX(1); EX(2); PIN(s); SBAR();
            if constexpr (j > 0) pvm(std::integral_constant<int, j - 1>{}, vf[(j - 1) & 1][1], p1, false);
            SBAR();
            if (!P2 && j < 8 / SPJ && st1) {
#pragma unroll
                for (int k = 0; k < SPJ / 2; ++k) kvstore(j * SPJ + k); }
            EX(3); EX(4); EX(5); PIN(s); SBAR();
            if constexpr (j > 0) lsm(std::integral_constant<int, j - 1>{}, p1, false);
            SBAR();
            EX(6); EX(7);
            p0 = (u32x4){cvtpk(s[0], s[1]), cvtpk(s[2], s[3]), cvtpk(s[4], s[5]), cvtpk(s[6], s[7])}; PIN(p0); SBAR();
            if (j + 1 < NJ) S[(j + 1) & 1] = MFMA32(kf[(j + 1) & 1][1], qf[0][1], S[(j + 1) & 1]);
            SBAR();
            if (j + 1 < NJ) vf[(j + 1) & 1][0] = ldsfrag(ad[(j + 1) % 3] + 2048);
            EX(8); EX(9); EX(10); PIN(s); SBAR();
            pvm(JC, vf[j & 1][0], p0, !P2 && j < 2);
            SBAR();
            if (j + 2 < NJ) rdk(j + 2);
            if (!P2 && j < 8 / SPJ && st1) {
#pragma unroll
                for (int k = SPJ / 2; k < SPJ; ++k) kvstore(j * SPJ + k); }
            EX(11); EX(12); EX(13); PIN(s); SBAR();
            lsm(JC, p0, !P2 && j < 2);
            SBAR();
            EX(14); EX(15);
            p1 = (u32x4){cvtpk(s[8], s[9]), cvtpk(s[10], s[11]), cvtpk(s[12], s[13]), cvtpk(s[14], s[15])}; PIN(p1); SBAR();
#undef EX
            if (j == NJ - 1) { pvm(JC, vf[j & 1][1], p1, false); lsm(JC, p1, false); }
            if (!P2 && j == 1) { mk = flip(mk); PIN(mk); }
            if (!P2 && j == PUBJ) {
                waitv<0>(); BAR();
                if (wid == 0 && lane == 0 && rb < 7) __hip_atomic_store((GAS u64*)(wsp + WS_FKV + (size_t)me * 128), tagp, __ATOMIC_RELAXED, __HIP_MEMORY_SCOPE_AGENT);
                STAMP(12);
            }
            if (!P2 && j == POLJ && rb > 0) {
                if (wid == 0) {
                    GAS u64* f = (GAS u64*)(wsp + WS_FKV + (size_t)(me - 16) * 128);
#pragma nounroll
                    for (int spin = 0; spin < SPIN_CAP; ++spin) {
                        if (rfl((unsigned)pv) == (unsigned)tagc && rfl((unsigned)(pv >> 32)) == (unsigned)(tagc >> 32)) break;
                        __builtin_amdgcn_s_sleep(1);
                        pv = __hip_atomic_load(f, __ATOMIC_RELAXED, __HIP_MEMORY_SCOPE_AGENT);
                    }
                    if (lane == 0) __hip_atomic_store((GAS u64*)(wsp + WS_EKV + (size_t)(me - 16) * 128), tagc, __ATOMIC_RELAXED, __HIP_MEMORY_SCOPE_AGENT);
                }
                BAR();
                STAMP(5);
#pragma unroll
                for (int k = 0; k < 8; ++k) pld[k] = __builtin_amdgcn_raw_buffer_load_b128(srdW, (unsigned)(lane * 16 + k * 1024), (unsigned)WS_KV + (me - 16) * 65536 + wid * 8192, 16);
                SBAR();
            }
        };
#define JOB(n) job(std::integral_constant<int, n>{})
        JOB(0); JOB(1); JOB(2); JOB(3); JOB(4); JOB(5); JOB(6); JOB(7); JOB(8);
#undef JOB
    };
    jobs(std::integral_constant<int, 0>{});
    STAMP(4);
    if (rb > 0) {
#pragma unroll
        for (int i = 0; i < 8; ++i) *(LAS u32x4*)(lds + half * 65536 + w4 * 8192 + i * 1024 + lane * 16) = pld[i];
        WAITL0(); BAR();
        STAMP(6);
        jobs(std::integral_constant<int, 1>{});
    }
#pragma unroll
    for (int d = 0; d < 2; ++d) {
        const int gt = d ? 7 - wr : wr;
        float lt = (lane & 16) ? l[d][1] : l[d][0];
        if (__builtin_expect(__any(!(lt <= LSAFE)), 0)) {
            const f32x16 mhi = mk, mlo = flip(mk);
            const bf16x8 q0 = rb > 0 ? qf[d][0] : qf[1 - d][0], q1 = rb > 0 ? qf[d][1] : qf[1 - d][1];
            const int b0 = rb > 0 ? gt : 8, b1 = 8 + gt;
            auto sc = [&](int b, float ref) { f32x16 c;
#pragma unroll
                for (int r = 0; r < 16; ++r) c[r] = (b == b1 ? mlo[r] : b == gt ? mhi[r] : 0.f) - ref;
                c = MFMA32(ldsfrag(kvb + b * 4096), q0, c); return MFMA32(ldsfrag(kvb + b * 4096 + 1024), q1, c); };
            float m = -INFINITY;
#pragma nounroll
            for (int b = b0; b <= b1; ++b) m = jmax(sc(b, 0.f), m);
            m = xmax(m);
            f32x4 ls = {0.f, 0.f, 0.f, 0.f};
#pragma unroll
            for (int r = 0; r < 16; ++r) o[d][r] = 0.f;
#pragma nounroll
            for (int b = b0; b <= b1; ++b) { bf16x8 p0, p1; expo(sc(b, m), p0, p1);
                o[d] = MFMA32(ldsfrag(kvb + b * 4096 + 2048), p0, o[d]); o[d] = MFMA32(ldsfrag(kvb + b * 4096 + 3072), p1, o[d]);
                ls = MFMA16(ones, p0, ls); ls = MFMA16(ones, p1, ls); }
            lt = (lane & 16) ? ls[1] : ls[0];
        }
        const float inv = __builtin_amdgcn_rcpf(lt);
        const unsigned base = (unsigned)WS_O + (((br * 128 + rb * 16 + gt * 2) * 32) + hp * 2 + wc) * 1024;
#pragma unroll
        for (int e = 0; e < 2; ++e) {
            const f32x16& x = o[d];
            const u32x4 v = {cvtpk(fmul_s(x[8 * e], inv), fmul_s(x[8 * e + 1], inv)), cvtpk(fmul_s(x[8 * e + 2], inv), fmul_s(x[8 * e + 3], inv)), cvtpk(fmul_s(x[8 * e + 4], inv), fmul_s(x[8 * e + 5], inv)), cvtpk(fmul_s(x[8 * e + 6], inv), fmul_s(x[8 * e + 7], inv))};
            __builtin_amdgcn_raw_buffer_store_b128(v, srdW, base + (r32 >> 4) * 32768 + hh * 256 + (r32 & 15) * 16 + e * 512, 0, 16);
        }
    }
    STAMP(7);
    auto phase3 = [&](auto HC) {
        constexpr int H = decltype(HC)::value;
        const int lane = lane_id(), r16 = lane & 15, g4 = lane >> 4, q4 = (lane & 15) >> 2, p4 = lane & 3;
        GAS char* ep = (GAS char*)wsp + WS_EO + (lane < 16 ? (size_t)(grp + lane) * 128 + hp * 8 : (size_t)me * 128 + (lane & 15) * 8);
        u64 eo;
        asm volatile("global_load_dwordx2 %0, %1, off sc1" : "=v"(eo) : "v"(ep) : "memory");
        unsigned woff[8];
        { const int row = 16 * (g4 >> 1) + 4 * (g4 & 1) + q4;
#pragma unroll
          for (int n = 0; n < 8; ++n) woff[n] = 131072 + H * 8192 + row * 256 + ((n ^ (row & 7)) << 5) + p4 * 8; }
        bf16x8 wf[2][8];
        auto wread = [&](int s, int n) {
            if (s == 2 || s == 12) { woff[n] -= 131072; PIN(woff[n]); } else if (s == 6 || s == 10) { woff[n] += 65536; PIN(woff[n]); }
            LAS const char* p = lds + woff[n] + (((s + 8) % 10) & 3) * 16384;
            const s16x4 lo = trread(p), hi = trread(p + 2048); wf[s & 1][n] = __builtin_shufflevector(lo, hi, 0, 1, 2, 3, 4, 5, 6, 7); };
        waitv<5>(); WAITL0(); BAR();
        wo_steps(2, 2 + WPRE);
#pragma unroll
        for (int n = 0; n < 8; ++n) { wread(0, n); wread(1, n); }
        waitv<2 * WPRE>(); PIN(eo); WAITL0(); BAR();
        if (H == 0 && wid == 0) {
            const u64 tag = eo + TAGK;
            if (lane < 16) __hip_atomic_store((GAS u64*)(ep - WS_EO + WS_FO), tag, __ATOMIC_RELAXED, __HIP_MEMORY_SCOPE_AGENT);
        }
        wo_steps(2 + WPRE, 12);
        if (H == 0 && wid == 0) {
            const u64 tag = eo + TAGK;
            GAS u64* f = (GAS u64*)(wsp + WS_FO + (size_t)me * 128 + (lane & 15) * 8);
            const bool mine = lane >= 16 && lane < 32;
#pragma nounroll
            for (int spin = 0; spin < SPIN_CAP; ++spin) {
                const u64 v = __hip_atomic_load(f, __ATOMIC_RELAXED, __HIP_MEMORY_SCOPE_AGENT);
                if (__all(!mine || v == tag)) break;
                __builtin_amdgcn_s_sleep(1);
            }
            if (mine) __hip_atomic_store((GAS u64*)ep, tag, __ATOMIC_RELAXED, __HIP_MEMORY_SCOPE_AGENT);
        }
        waitv<0>(); BAR();
        STAMP(8);
        bf16x8 of[OW][2];
        const unsigned obase = (unsigned)WS_O + ((br * 128 + rb * 16 + th * 8 + w4 * 2) * 32 + H * 16) * 1024 + lane * 16;
        auto oload = [&](int s, int t) { of[s % OW][t] = __builtin_bit_cast(bf16x8, __builtin_amdgcn_raw_buffer_load_b128(srdW, obase + t * 32768 + s * 1024, 0, 16)); };
#pragma unroll
        for (int s = 0; s < OW - 1; ++s) { oload(s, 0); oload(s, 1); SBAR(); }
        f32x4 c2[2][8];
#pragma unroll
        for (int t = 0; t < 2; ++t)
#pragma unroll
            for (int n = 0; n < 8; ++n) c2[t][n] = (f32x4){0.f, 0.f, 0.f, 0.f};
        const unsigned voffW0 = (lane >> 4) * 2048 + (((lane & 15) ^ ((lane >> 4) << 1)) << 4), voffW1 = (4 + (lane >> 4)) * 2048 + (((lane & 15) ^ ((4 + (lane >> 4)) << 1)) << 4);
        {
            auto step = [&](int s) {
#pragma unroll
                for (int n = 0; n < 8; ++n) {
                    c2[0][n] = MFMA16(wf[s & 1][n], of[s % OW][0], c2[0][n]); c2[1][n] = MFMA16(wf[s & 1][n], of[s % OW][1], c2[1][n]);
                    SBAR();
                    if (s + 2 < 16) wread(s + 2, n);
                    if (n == PA0 + 2 * H && s + OW - 1 < 16) oload(s + OW - 1, 0);
                    if (n == PA0 + 4 + 2 * H && s + OW - 1 < 16) oload(s + OW - 1, 1);
                    if (s >= 4 && s < 8 && (n == PA0 + 2 - 2 * H || n == PA0 + 6 - 2 * H)) dma16(ldsb + ((s + 16) % 10) * 16384 + wid * 2048 + (n >= 4) * 1024, n >= 4 ? voffW1 : voffW0, srdWo, sW + (s + 8) * 65536);
                    SBAR();
                } };
#pragma unroll
            for (int s = 0; s < 4; ++s) { step(s); if (s == 0) STAMP(11); }
            WAITL0(); BAR();
#pragma unroll
            for (int s = 4; s < 10; ++s) step(s);
            STAMP(13);
            waitv<4>(); BAR();
            STAMP(14);
#pragma unroll
            for (int s = 10; s < 15; ++s) step(s);
#pragma unroll
            for (int n = 4 - 4 * H; n < 8 - 4 * H; ++n) { c2[0][n] = MFMA16(wf[1][n], of[15 % OW][0], c2[0][n]); c2[1][n] = MFMA16(wf[1][n], of[15 % OW][1], c2[1][n]); }
            SBAR();
#pragma unroll
            for (int n = 0; n < 4; ++n) {
                c2[0][4 * H + n] = MFMA16(wf[1][4 * H + n], of[15 % OW][0], c2[0][4 * H + n]); c2[1][4 * H + n] = MFMA16(wf[1][4 * H + n], of[15 % OW][1], c2[1][4 * H + n]);
                SBAR();
#pragma unroll
                for (int t = 0; t < 2; ++t) *(LAS f32x4*)(lds + 65536 + wid * 8192 + (t * 4 + n) * 1024 + lane * 16) = c2[t][4 - 4 * H + n];
                SBAR();
            }
            STAMP(9);
            WAITL0(); BAR();
            STAMP(15);
            GAS u16* outp = (GAS u16*)outb + (size_t)(rb * 256 + th * 128 + w4 * 32 + r16) * 2048 + br * 1024 + ct * 128 + H * 64 + (g4 & 1) * 16 + (g4 >> 1) * 8;
#pragma unroll
            for (int t = 0; t < 2; ++t)
#pragma unroll
                for (int n = 0; n < 4; n += 2) {
                    const f32x4 x = c2[t][4 * H + n] + *(LAS const f32x4*)(lds + 65536 + (wid ^ 4) * 8192 + (t * 4 + n) * 1024 + lane * 16);
                    const f32x4 y = c2[t][4 * H + n + 1] + *(LAS const f32x4*)(lds + 65536 + (wid ^ 4) * 8192 + (t * 4 + n + 1) * 1024 + lane * 16);
                    const auto u0 = __builtin_amdgcn_permlane16_swap(cvtpk(x[0], x[1]), cvtpk(y[0], y[1]), false, false);
                    const auto u1 = __builtin_amdgcn_permlane16_swap(cvtpk(x[2], x[3]), cvtpk(y[2], y[3]), false, false);
                    { const u32x4 vv = {u0[0], u1[0], u0[1], u1[1]}; GAS u16* pp = outp + (size_t)t * 16 * 2048 + n * 16; asm volatile("global_store_dwordx4 %0, %1, off sc0 sc1\n\ts_nop 1" :: "v"(pp), "v"(vv) : "memory"); }
                }
        }
    };
    if (half == 0) phase3(std::integral_constant<int, 0>{}); else phase3(std::integral_constant<int, 1>{});
}

extern "C" void kernel_launch(void* const* d_in, const int* in_sizes, int n_in, void* d_out, int out_size, void* d_ws, size_t ws_size, hipStream_t stream) {
    Args a;
    a.x[0] = (const u16*)d_in[0]; a.w[0] = (const u16*)d_in[1]; a.wo[0] = (const u16*)d_in[2];
    a.x[1] = (const u16*)d_in[3]; a.w[1] = (const u16*)d_in[4]; a.wo[1] = (const u16*)d_in[5];
    a.out = (u16*)d_out; a.ws = (char*)d_ws;
    fused_kernel<<<256, 512, 0, stream>>>(a);
}
```

```cpp
#include <hip/hip_runtime.h>
#include <hip/hip_bf16.h>
#include <type_traits>
typedef short bf16x8 __attribute__((ext_vector_type(8)));
typedef short s16x4 __attribute__((ext_vector_type(4)));
typedef float f32x16 __attribute__((ext_vector_type(16)));
typedef float f32x4 __attribute__((ext_vector_type(4)));
typedef float f32x2 __attribute__((ext_vector_type(2)));
typedef __bf16 bf16x2 __attribute__((ext_vector_type(2)));
typedef unsigned u32x4 __attribute__((ext_vector_type(4)));
typedef double f64x8 __attribute__((ext_vector_type(8)));
typedef double f64x2 __attribute__((ext_vector_type(2)));
typedef unsigned short u16;
typedef unsigned long long u64;
#define LAS __attribute__((address_space(3)))
#define GAS __attribute__((address_space(1)))
#define DI __device__ __forceinline__
#define MFMA16(a, b, c) __builtin_amdgcn_mfma_f32_16x16x32_bf16((a), (b), (c), 0, 0, 0)
#define MFMA32(a, b, c) __builtin_amdgcn_mfma_f32_32x32x16_bf16((a), (b), (c), 0, 0, 0)
#define SBAR() __builtin_amdgcn_sched_barrier(0)
#define BAR() do { SBAR(); asm volatile("s_barrier" ::: "memory"); SBAR(); } while (0)
#define PIN(x) asm volatile("" : "+v"(x))
#define WAITL0() __builtin_amdgcn_s_waitcnt(0xC07F)
template <int N> DI void waitv() { asm volatile("s_waitcnt vmcnt(%0)" :: "i"(N) : "memory"); }
DI unsigned rfl(unsigned x) { return __builtin_amdgcn_readfirstlane(x); }
DI int lane_id() { int l; asm volatile("v_mbcnt_lo_u32_b32 %0, -1, 0\n\tv_mbcnt_hi_u32_b32 %0, -1, %0" : "=v"(l)); return l; }
DI unsigned cvtpk(float lo, float hi) { f32x2 v = {lo, hi}; bf16x2 b = __builtin_convertvector(v, bf16x2); return __builtin_bit_cast(unsigned, b); }
DI void dma16(unsigned m0v, unsigned voff, __amdgpu_buffer_rsrc_t srd, unsigned soff) {
    asm volatile("s_mov_b32 m0, %0\n\ts_nop 0\n\tbuffer_load_dwordx4 %1, %2, %3 offen lds" :: "s"(m0v), "v"(voff), "s"(srd), "s"(soff) : "memory");
}
DI void dma4(unsigned m0v, unsigned voff, __amdgpu_buffer_rsrc_t srd, unsigned soff) {
    asm volatile("s_mov_b32 m0, %0\n\ts_nop 0\n\tbuffer_load_dword %1, %2, %3 offen lds" :: "s"(m0v), "v"(voff), "s"(srd), "s"(soff) : "memory");
}
DI s16x4 trread(LAS const char* p) { return __builtin_amdgcn_ds_read_tr16_b64_v4i16((LAS s16x4*)p); }
DI bf16x8 ldsfrag(LAS const char* p) { return *(LAS const bf16x8*)p; }
DI float max3f(float a, float b, float c) { return __builtin_fmaxf(__builtin_fmaxf(a, b), c); }
DI float fadd_s(float a, float b) { return a + b; }
DI float fsub_s(float a, float b) { return a - b; }
DI float fmul_s(float a, float b) { return a * b; }
DI bf16x8 pack8(float a, float b, float c, float d, float e, float f, float g, float h) { u32x4 v = {cvtpk(a, b), cvtpk(c, d), cvtpk(e, f), cvtpk(g, h)}; return __builtin_bit_cast(bf16x8, v); }

constexpr int LDS_A0 = 0, LDS_A1 = 32768, LDS_B = 81920, A_SLOT = 16384, B_SLOT = 24576;
constexpr int LDS_TOTAL = 163840;
constexpr size_t WS_FKV = 0, WS_EKV = 32768, WS_FO = 65536, WS_EO = 98304, WS_KV = 1 << 20, WS_O = 17 << 20, WS_END = 25 << 20, WS_ST = 32 << 20;
constexpr u64 TAGK = 0x9E3779B97F4A7C15ull;
constexpr int SPIN_CAP = 400000;
constexpr float QSCALE = 0.17677669529663687f * 1.4426950408889634f;

#define PUBJ 3
#define POLJ 1
#define SPJ 4
#define OW 5
#define PA0 1
#define WPRE 4
#define LSAFE 1.152921504606847e18f
#define KROT 0
#define CODEPF 48
#define TOUCH 1
#define PD 5
constexpr int LDS_SCR = 155648;
#define PT3W 0
#define PT3X 0
#define KVT 0
#define MAP84 0
#define PGT 0
#define NEARLY 7
struct Args { const u16* x[2]; const u16* w[2]; const u16* wo[2]; u16* out; char* ws; };

#define STAMP(i) do {} while (0)

__global__ __launch_bounds__(512, 2) void fused_kernel(Args a) {
    __shared__ __attribute__((aligned(16))) char smem[LDS_TOTAL];
    LAS char* lds = (LAS char*)smem;
    const unsigned ldsb = (unsigned)(size_t)smem;
    const int tid = threadIdx.x, lane0 = tid & 63;
    u64 pc0; asm volatile("s_getpc_b64 %0" : "=s"(pc0));
    const int wid = rfl(tid >> 6), half = wid >> 2, w4 = wid & 3;
    const int bid = blockIdx.x, g = bid & 7, j = bid >> 3;
    const int br = g >> 2, rb = MAP84 ? (j & 7) : ((g >> 1) & 1) * 4 + (j & 3), hp = MAP84 ? (g & 3) * 4 + (j >> 3) : (g & 1) * 8 + (j >> 2);
    const int me = (br * 8 + rb) * 16 + hp, grp = me & ~15;
    STAMP(0);
    char* wsp = a.ws; const u16* wop = a.wo[br]; u16* outb = a.out; const u16* xp = a.x[br]; const u16* wp = a.w[br];
    asm volatile("" : "+s"(wsp), "+s"(wop), "+s"(outb), "+s"(xp), "+s"(wp));
    const char* ea = wsp + WS_EKV + (size_t)(lane0 == 0 || rb == 0 ? me : me - 16) * 128;
    if (PGT) ea = lane0 == 2 ? wsp + WS_KV + (size_t)me * 65536 : lane0 == 3 ? wsp + WS_O + (size_t)(br * 128 + rb * 16) * 32768 : lane0 == 4 ? (const char*)outb + (size_t)rb * 256 * 4096
                : lane0 == 5 ? (const char*)wop + hp * 131072 : ea;
    u64 ew = __hip_atomic_load((GAS u64*)ea, __ATOMIC_RELAXED, __HIP_MEMORY_SCOPE_AGENT);
    if (CODEPF && wid == 7) {
        const __amdgpu_buffer_rsrc_t srdC = __builtin_amdgcn_make_buffer_rsrc((void*)pc0, (short)0, CODEPF * 1024, 0x00020000);
        if (lane0 < 13) dma4(ldsb + LDS_SCR, (j * 13 + lane0) * 128, srdC, 0);
    }
    const __amdgpu_buffer_rsrc_t srdA = __builtin_amdgcn_make_buffer_rsrc((void*)(xp + (size_t)rb * 256 * 1024), (short)0, 256 * 2048, 0x00020000);
    const __amdgpu_buffer_rsrc_t srdB = __builtin_amdgcn_make_buffer_rsrc((void*)wp, (short)0, 1024 * 6144, 0x00020000);
    const int wr = 2 * half + (w4 >> 1), wc = w4 & 1;
    const unsigned voffE = (lane0 >> 3) * 2048 + (((lane0 & 7) ^ (lane0 >> 4)) << 4);
    const unsigned voffO = (8 + (lane0 >> 3)) * 2048 + (((lane0 & 7) ^ (4 + (lane0 >> 4))) << 4);
    const unsigned voffB0 = (lane0 >> 3) * 6144 + (((lane0 & 7) ^ (((lane0 >> 4) & 1) << 1)) << 4);
    const unsigned voffB1 = (lane0 >> 3) * 6144 + (((lane0 & 7) ^ ((((lane0 >> 4) & 1) << 1) | 4)) << 4);
    const int gt0 = (w4 & 1) ? 7 - (w4 >> 1) : (w4 >> 1);
    const int gt1 = (w4 & 1) ? 5 - (w4 >> 1) : 2 + (w4 >> 1);
    const unsigned sA0 = gt0 * 32 * 2048, sA1 = gt1 * 32 * 2048;
    const unsigned sB = hp * 128 + (2 * w4) * 8 * 6144;
    auto piece = [&](auto HC, int kt, int s2, int s3i, int i) {
        constexpr int H = decltype(HC)::value;
        const unsigned soB = sB + kt * (64 * 6144), dB = ldsb + LDS_B + s3i * B_SLOT + w4 * 2048;
        const unsigned soA0 = sA0 + kt * 128, dA0 = ldsb + LDS_A0 + s2 * A_SLOT + w4 * 4096;
        const unsigned soA1 = sA1 + kt * 128, dA1 = ldsb + LDS_A1 + s3i * A_SLOT + w4 * 4096;
        if (H == 0) {
            if (i == 6) dma16(dA1 + 3 * 1024, voffO, srdA, soA1 + 32768);
            else dma16(dB + (i >> 1) * 8192 + (i & 1) * 1024, (i & 1) ? voffB1 : voffB0, srdB, soB + (i >> 1) * 2048 + (i & 1) * 8 * 6144);
        } else dma16((i < 4 ? dA0 : dA1) + (i & 3) * 1024, (i & 1) ? voffO : voffE, srdA, (i < 4 ? soA0 : soA1) + ((i >> 1) & 1) * 32768);
    };
    if (half == 0) {
#pragma unroll
        for (int i = 0; i < NEARLY; ++i) piece(std::integral_constant<int, 0>{}, i / 7, i / 7, i / 7, i % 7);
    } else {
#pragma unroll
        for (int i = 0; i < NEARLY; ++i) piece(std::integral_constant<int, 1>{}, i / 7, i / 7, i / 7, i % 7);
    }
    SBAR();
    const int lane = lane_id();
    const int r16 = lane & 15, g4 = lane >> 4, q4 = (lane & 15) >> 2, p4 = lane & 3;
    f32x4 acc[4][6];
    const int r32 = lane & 31, hh = lane >> 5;
    f32x16 mk;
#pragma unroll
    for (int r = 0; r < 16; ++r) mk[r] = ((r & 3) + 8 * (r >> 2) <= r32 - 4 * hh) ? 0.f : -INFINITY;
    {
        unsigned aoff[2], boff[2];
#pragma unroll
        for (int s = 0; s < 2; ++s) aoff[s] = (half ? LDS_A1 : LDS_A0) + ((w4 >> 1) * 64 + r16) * 128 + (((4 * s + g4) ^ ((r16 >> 1) & 7)) << 4);
#pragma unroll
        for (int c = 0; c < 2; ++c) boff[c] = LDS_B + (8 * g4 + q4) * 128 + (((wc * 2 + c) ^ ((q4 >> 1) | ((g4 & 1) << 1))) << 5) + p4 * 8;
#pragma unroll
        for (int i = 0; i < 4; ++i)
#pragma unroll
            for (int s = 0; s < 6; ++s) acc[i][s] = (f32x4){0.f, 0.f, 0.f, 0.f};
        bf16x8 af[4][2], bfr[6][2];
        const unsigned voffT = (16 * (rb & 3) + 4 * w4 + lane / 3) * 6144 + (lane % 3) * 2048 + hp * 128;
        const bool slowc = ((((size_t)wp >> 7) + hp) & 7) == 3;
        auto run = [&](auto HC, auto TC) {
            constexpr int H = decltype(HC)::value;
            constexpr int TW = decltype(TC)::value;
            auto touch = [&](int kt) { if (lane < 12) dma4(ldsb + LDS_SCR, voffT, srdB, kt * (64 * 6144)); };
#pragma unroll
            for (int i = 0; i < 4; ++i) asm volatile("" : "+v"(acc[i][0]), "+v"(acc[i][1]), "+v"(acc[i][2]), "+v"(acc[i][3]), "+v"(acc[i][4]), "+v"(acc[i][5]));
            PIN(mk); PIN(aoff[0]); PIN(aoff[1]); PIN(boff[0]); PIN(boff[1]);
            SBAR();
#pragma unroll
            for (int i = NEARLY; i < 14; ++i) piece(HC, i / 7, i / 7, i / 7, i % 7);
            if (H == 0) waitv<7>(); else waitv<10>();
            BAR();
            if (TW) {
#pragma unroll
                for (int kt = 3; kt < PD; ++kt) touch(kt);
            }
            if (H == 1) { waitv<7>(); BAR(); }
            STAMP(1);
            int s3 = 0;
            auto Lseg = [&](int t, bool issue) {
                const unsigned aslot = H ? s3 * A_SLOT : (t & 1) * A_SLOT;
                const unsigned bslot = s3 * B_SLOT;
                const int s3n = (s3 == 0) ? 2 : s3 - 1;
                auto rdA = [&](int i) { LAS const char* pa = lds + aoff[i >> 1] + aslot + (i & 1) * 4096; af[(i & 1) * 2][i >> 1] = ldsfrag(pa); af[(i & 1) * 2 + 1][i >> 1] = ldsfrag(pa + 2048); };
                auto rdB = [&](int i) { const int s = i / 6, seg = (i % 6) >> 1, c = i & 1; LAS const char* pb = lds + boff[c] + bslot;
                    s16x4 lo = trread(pb + seg * 8192 + s * 4096), hi = trread(pb + seg * 8192 + s * 4096 + 512);
                    bfr[seg * 2 + c][s] = __builtin_shufflevector(lo, hi, 0, 1, 2, 3, 4, 5, 6, 7); };
                if (TW && TOUCH == 2 && issue) touch(t + PD);
#pragma unroll
                for (int i = 0; i < 16; ++i) {
                    if (issue && (i & 1) == 0 && i / 2 < 7) piece(HC, t + 2, t & 1, s3n, i / 2);
                    if (TW && TOUCH == 3 && issue && i == 13) touch(t + PD);
                    if (i < 4) rdA(i); else rdB(i - 4);
                }
                if (TW && TOUCH == 1 && issue) touch(t + PD);
                s3 = (s3 == 2) ? 0 : s3 + 1;
            };
            auto Mseg = [&]() {
                __builtin_amdgcn_s_setprio(1);
#pragma unroll
                for (int s = 0; s < 2; ++s)
#pragma unroll
                    for (int m = 0; m < 4; ++m)
#pragma unroll
                        for (int n = 0; n < 6; ++n) acc[m][n] = n < 4 ? MFMA16(bfr[n][s], af[m][s], acc[m][n]) : MFMA16(af[m][s], bfr[n][s], acc[m][n]);
                __builtin_amdgcn_s_setprio(0);
            };
            if (TW && TOUCH != 2) { Lseg(0, true); WAITL0(); BAR(); Mseg(); waitv<8 + PD - 3>(); BAR(); }
            for (int t = (TW && TOUCH != 2) ? 1 : 0; t < 14; ++t) {
                Lseg(t, true);
                if (H == 1) waitv<10>();
                WAITL0(); BAR();
                Mseg();
                waitv<7 + (TW ? (TOUCH == 2 ? 1 : 2) : 0)>();
                BAR();
            }
            Lseg(14, false); if (H == 1) waitv<3>(); WAITL0(); BAR(); Mseg(); waitv<0>(); BAR();
            Lseg(15, false); WAITL0(); BAR(); Mseg(); if (H == 0) BAR();
        };
        if (half == 1) run(std::integral_constant<int, 1>{}, std::integral_constant<int, 0>{});
        else if (TOUCH && slowc) run(std::integral_constant<int, 0>{}, std::integral_constant<int, 1>{});
        else run(std::integral_constant<int, 0>{}, std::integral_constant<int, 0>{});
    }
    STAMP(2);
    const u64 tagp = (((u64)__builtin_amdgcn_readlane((unsigned)(ew >> 32), 0) << 32) | __builtin_amdgcn_readlane((unsigned)ew, 0)) + TAGK;
    const u64 tagc = (((u64)__builtin_amdgcn_readlane((unsigned)(ew >> 32), 1) << 32) | __builtin_amdgcn_readlane((unsigned)ew, 1)) + TAGK;
    const __amdgpu_buffer_rsrc_t srdW = __builtin_amdgcn_make_buffer_rsrc((void*)wsp, (short)0, (int)WS_END, 0x00020000);
    const __amdgpu_buffer_rsrc_t srdWo = __builtin_amdgcn_make_buffer_rsrc((void*)(wop), (short)0, 1024 * 2048, 0x00020000);
    const int ct = hp & 7, th = hp >> 3;
    const unsigned sW = ct * 256 + (half * 512 + w4 * 8) * 2048;
    auto wo_steps = [&](int s0, int s1) {
        const int ln = lane_id();
        const unsigned voffW0 = (ln >> 4) * 2048 + (((ln & 15) ^ ((ln >> 4) << 1)) << 4), voffW1 = (4 + (ln >> 4)) * 2048 + (((ln & 15) ^ ((4 + (ln >> 4)) << 1)) << 4);
#pragma unroll
        for (int s = s0; s < s1; ++s) { const unsigned d = ldsb + ((s + 8) % 10) * 16384 + wid * 2048; dma16(d, voffW0, srdWo, sW + s * 65536); dma16(d + 1024, voffW1, srdWo, sW + s * 65536); } };
    bf16x8 qf[2][2];
    {
        const unsigned kpos = (g4 >> 1) * 1024 + (g4 & 1) * 512 + r16 * 16;
        const unsigned vpos = 2048 + ((g4 & 1) * 32 + (g4 >> 1) * 16 + r16) * 16;
        const unsigned gkv = (unsigned)WS_KV + me * 65536 + wc * 32768;
        u32x4 kv[2][2], vv[2][2];
#pragma unroll
        for (int mi = 0; mi < 2; ++mi)
#pragma unroll
            for (int s = 0; s < 2; ++s) {
                const f32x4 k0 = acc[2 * mi + s][2], k1 = acc[2 * mi + s][3], v0 = acc[2 * mi + s][4], v1 = acc[2 * mi + s][5];
                kv[mi][s] = (u32x4){cvtpk(k0[0], k0[1]), cvtpk(k0[2], k0[3]), cvtpk(k1[0], k1[1]), cvtpk(k1[2], k1[3])};
                const auto ra = __builtin_amdgcn_permlane32_swap(cvtpk(v0[0], v0[1]), cvtpk(v1[0], v1[1]), false, false);
                const auto rbb = __builtin_amdgcn_permlane32_swap(cvtpk(v0[2], v0[3]), cvtpk(v1[2], v1[3]), false, false);
                vv[mi][s] = (u32x4){ra[0], rbb[0], ra[1], rbb[1]};
                const unsigned blk = wc * 65536 + 32768 + (mi ? 7 - wr : wr) * 4096;
                *(LAS u32x4*)(lds + blk + kpos + s * 256) = kv[mi][s];
                *(LAS u32x4*)(lds + blk + vpos + s * 1024) = vv[mi][s];
            }
        if (rb < 7) {
#pragma unroll
            for (int mi = 0; mi < 2; ++mi)
#pragma unroll
                for (int s = 0; s < 2; ++s) { const unsigned blk = (mi ? 7 - wr : wr) * 4096;
                    __builtin_amdgcn_raw_buffer_store_b128(kv[mi][s], srdW, gkv + blk + kpos + s * 256, 0, 16);
                    __builtin_amdgcn_raw_buffer_store_b128(vv[mi][s], srdW, gkv + blk + vpos + s * 1024, 0, 16); }
        }
#pragma unroll
        for (int mi = 0; mi < 2; ++mi) {
            u32x4 x[2], y0, y1;
#pragma unroll
            for (int s = 0; s < 2; ++s) { const f32x4 q0 = acc[2 * mi + s][0] * QSCALE, q1 = acc[2 * mi + s][1] * QSCALE;
                x[s] = (u32x4){cvtpk(q0[0], q0[1]), cvtpk(q0[2], q0[3]), cvtpk(q1[0], q1[1]), cvtpk(q1[2], q1[3])}; }
#pragma unroll
            for (int i = 0; i < 4; ++i) { const auto u = __builtin_amdgcn_permlane16_swap(x[0][i], x[1][i], false, false);
                const auto v = __builtin_amdgcn_permlane32_swap(u[0], u[1], false, false); y0[i] = v[0]; y1[i] = v[1]; }
            qf[mi][0] = __builtin_bit_cast(bf16x8, y0); qf[mi][1] = __builtin_bit_cast(bf16x8, y1);
        }
    }
    f32x16 o[2], nm[2]; f32x4 l[2];
    LAS const char* kvb = lds + wc * 65536 + lane * 16;
    bf16x8 ones;
    { const short v = ((lane ^ (lane >> 4)) & 1) ? (short)0 : (short)0x3f80;
#pragma unroll
      for (int i = 0; i < 8; ++i) ones[i] = v; }
    auto jmax = [&](const f32x16& s, float m) {
#pragma unroll
        for (int r = 0; r < 16; r += 2) m = max3f(m, s[r], s[r + 1]);
        return m; };
    auto xmax = [&](float m) { const auto r = __builtin_amdgcn_permlane32_swap(__float_as_uint(m), __float_as_uint(m), false, false); return __builtin_fmaxf(__uint_as_float(r[0]), __uint_as_float(r[1])); };
    auto expo = [&](const f32x16& s, bf16x8& p0, bf16x8& p1) {
        float t[16];
#pragma unroll
        for (int r = 0; r < 16; ++r) t[r] = __builtin_amdgcn_exp2f(s[r]);
        p0 = pack8(t[0], t[1], t[2], t[3], t[4], t[5], t[6], t[7]); p1 = pack8(t[8], t[9], t[10], t[11], t[12], t[13], t[14], t[15]); };
    auto flip = [&](const f32x16& m) { f32x16 c;
#pragma unroll
        for (int r = 0; r < 16; ++r) c[r] = __uint_as_float(__float_as_uint(m[r]) ^ 0xff800000u);
        return c; };
    {
        f32x16 zero16, s[2];
#pragma unroll
        for (int r = 0; r < 16; ++r) zero16[r] = 0.f;
#pragma unroll
        for (int d = 0; d < 2; ++d) {
            LAS const char* p = kvb + (8 + (d ? 7 - wr : wr)) * 4096;
            s[d] = MFMA32(ldsfrag(p), qf[d][0], mk); s[d] = MFMA32(ldsfrag(p + 1024), qf[d][1], s[d]);
        }
        wo_steps(0, 2);
        const float m0 = xmax(jmax(s[0], -INFINITY)), m1 = xmax(jmax(s[1], -INFINITY));
#pragma unroll
        for (int d = 0; d < 2; ++d) {
            LAS const char* p = kvb + (8 + (d ? 7 - wr : wr)) * 4096;
            const float m = d ? m1 : m0;
#pragma unroll
            for (int r = 0; r < 16; ++r) { nm[d][r] = -m; s[d][r] = fsub_s(s[d][r], m); }
            bf16x8 p0, p1;
            expo(s[d], p0, p1);
            o[d] = MFMA32(ldsfrag(p + 2048), p0, zero16); l[d] = MFMA16(ones, p0, ((f32x4){0.f, 0.f, 0.f, 0.f}));
            o[d] = MFMA32(ldsfrag(p + 3072), p1, o[d]); l[d] = MFMA16(ones, p1, l[d]);
        }
    }
    mk = flip(mk); PIN(mk);
    waitv<4>(); WAITL0(); BAR();
    if (wid == 0 && lane == 0 && rb < 7) __hip_atomic_store((GAS u64*)(wsp + WS_FKV + (size_t)me * 128), tagp, __ATOMIC_RELAXED, __HIP_MEMORY_SCOPE_AGENT);
    STAMP(3);
    auto attention = [&](auto WC_) {
        constexpr int WR = decltype(WC_)::value;
        u32x4 pld[8];
        auto jobs = [&](auto PART) {
            constexpr int P2 = decltype(PART)::value;
            constexpr int NJ = P2 ? 9 : 7;
            auto TOF = [](int j) { return P2 ? (j < 7 ? (j < 7 - WR ? 0 : 1) : j - 7) : (j < WR ? 0 : 1); };
            auto BOF = [](int j) { return P2 ? (j < 7 ? (j < 7 - WR ? WR + 1 + j : j + 1) : (j == 7 ? WR : 7 - WR)) : 8 + (j < WR ? j : j - WR); };
            f32x16 S[2], cm; bf16x8 kf[2][2], vf[2][2]; u32x4 p0, p1;
            auto rdk = [&](int j) { kf[j & 1][0] = ldsfrag(kvb + BOF(j) * 4096); kf[j & 1][1] = ldsfrag(kvb + BOF(j) * 4096 + 1024); };
            auto cof = [&](int j) -> const f32x16& { return (P2 && j >= 7) ? cm : nm[TOF(j)]; };
            auto mkc = [&](int j) { const f32x16& mi = mk;
#pragma unroll
                for (int r = 0; r < 16; ++r) cm[r] = fadd_s(mi[r], nm[TOF(j)][r]); };
            rdk(0); rdk(1); vf[0][0] = ldsfrag(kvb + BOF(0) * 4096 + 2048);
            S[0] = MFMA32(kf[0][0], qf[TOF(0)][0], cof(0)); S[0] = MFMA32(kf[0][1], qf[TOF(0)][1], S[0]);
            SBAR();
#pragma unroll
            for (int j = 0; j < NJ; ++j) {
                const int T = TOF(j), TP = j > 0 ? TOF(j - 1) : 0, TN = j + 1 < NJ ? TOF(j + 1) : 0;
                f32x16& s = S[j & 1];
#define EX(i) s[i] = __builtin_amdgcn_exp2f(s[i])
                if (P2 && j + 1 >= 7 && j + 1 < NJ) { mkc(j + 1); PIN(cm); }
                if (j + 1 < NJ) S[(j + 1) & 1] = MFMA32(kf[(j + 1) & 1][0], qf[TN][0], cof(j + 1));
                SBAR();
                vf[j & 1][1] = ldsfrag(kvb + BOF(j) * 4096 + 3072);
                EX(0); EX(1); EX(2); PIN(s); SBAR();
                if (j > 0) o[TP] = MFMA32(vf[(j - 1) & 1][1], __builtin_bit_cast(bf16x8, p1), o[TP]);
                SBAR();
                EX(3); EX(4); EX(5); PIN(s); SBAR();
                if (j > 0) l[TP] = MFMA16(ones, __builtin_bit_cast(bf16x8, p1), l[TP]);
                SBAR();
                EX(6); EX(7);
                p0 = (u32x4){cvtpk(s[0], s[1]), cvtpk(s[2], s[3]), cvtpk(s[4], s[5]), cvtpk(s[6], s[7])}; PIN(p0); SBAR();
                if (j + 1 < NJ) S[(j + 1) & 1] = MFMA32(kf[(j + 1) & 1][1], qf[TN][1], S[(j + 1) & 1]);
                SBAR();
                if (j + 1 < NJ) vf[(j + 1) & 1][0] = ldsfrag(kvb + BOF(j + 1) * 4096 + 2048);
                EX(8); EX(9); EX(10); PIN(s); SBAR();
                o[T] = MFMA32(vf[j & 1][0], __builtin_bit_cast(bf16x8, p0), o[T]);
                SBAR();
                if (j + 2 < NJ) rdk(j + 2);
                EX(11); EX(12); EX(13); PIN(s); SBAR();
                l[T] = MFMA16(ones, __builtin_bit_cast(bf16x8, p0), l[T]);
                SBAR();
                EX(14); EX(15);
                p1 = (u32x4){cvtpk(s[8], s[9]), cvtpk(s[10], s[11]), cvtpk(s[12], s[13]), cvtpk(s[14], s[15])}; PIN(p1); SBAR();
#undef EX
                if (j == NJ - 1) { o[T] = MFMA32(vf[j & 1][1], __builtin_bit_cast(bf16x8, p1), o[T]); l[T] = MFMA16(ones, __builtin_bit_cast(bf16x8, p1), l[T]); }
                if (!P2 && j == POLJ && rb > 0) {
                    if (WR == 0 && wid == 0) {
                        GAS u64* f = (GAS u64*)(wsp + WS_FKV + (size_t)(me - 16) * 128);
#pragma nounroll
                        for (int spin = 0; spin < SPIN_CAP; ++spin) {
                            const u64 v = __hip_atomic_load(f, __ATOMIC_RELAXED, __HIP_MEMORY_SCOPE_AGENT);
                            if (rfl((unsigned)v) == (unsigned)tagc && rfl((unsigned)(v >> 32)) == (unsigned)(tagc >> 32)) break;
                            __builtin_amdgcn_s_sleep(1);
                        }
                        if (lane == 0) __hip_atomic_store((GAS u64*)(wsp + WS_EKV + (size_t)(me - 16) * 128), tagc, __ATOMIC_RELAXED, __HIP_MEMORY_SCOPE_AGENT);
                    }
                    BAR();
                    STAMP(5);
#pragma unroll
                    for (int k = 0; k < 8; ++k) pld[k] = __builtin_amdgcn_raw_buffer_load_b128(srdW, (unsigned)WS_KV + (me - 16) * 65536 + wid * 8192 + k * 1024 + lane * 16, 0, 16);
                    SBAR();
                }
            }
        };
        jobs(std::integral_constant<int, 0>{});
        STAMP(4);
        if (rb > 0) {
#pragma unroll
            for (int i = 0; i < 8; ++i) *(LAS u32x4*)(lds + half * 65536 + w4 * 8192 + i * 1024 + lane * 16) = pld[i];
            WAITL0(); BAR();
            STAMP(6);
            jobs(std::integral_constant<int, 1>{});
        }
    };
#define ATT(W) attention(std::integral_constant<int, W>{})
    if (wr == 0) ATT(0); else if (wr == 1) ATT(1); else if (wr == 2) ATT(2); else ATT(3);
#pragma unroll
    for (int d = 0; d < 2; ++d) {
        const int gt = d ? 7 - wr : wr;
        float lt = (lane & 16) ? l[d][1] : l[d][0];
        if (__builtin_expect(__any(!(lt <= LSAFE)), 0)) {
            const f32x16 mhi = mk, mlo = flip(mk);
            const int b0 = rb > 0 ? gt : 8, b1 = 8 + gt;
            auto sc = [&](int b, float ref) { f32x16 c;
#pragma unroll
                for (int r = 0; r < 16; ++r) c[r] = (b == b1 ? mlo[r] : b == gt ? mhi[r] : 0.f) - ref;
                c = MFMA32(ldsfrag(kvb + b * 4096), qf[d][0], c); return MFMA32(ldsfrag(kvb + b * 4096 + 1024), qf[d][1], c); };
            float m = -INFINITY;
#pragma nounroll
            for (int b = b0; b <= b1; ++b) m = jmax(sc(b, 0.f), m);
            m = xmax(m);
            f32x4 ls = {0.f, 0.f, 0.f, 0.f};
#pragma unroll
            for (int r = 0; r < 16; ++r) o[d][r] = 0.f;
#pragma nounroll
            for (int b = b0; b <= b1; ++b) { bf16x8 p0, p1; expo(sc(b, m), p0, p1);
                o[d] = MFMA32(ldsfrag(kvb + b * 4096 + 2048), p0, o[d]); o[d] = MFMA32(ldsfrag(kvb + b * 4096 + 3072), p1, o[d]);
                ls = MFMA16(ones, p0, ls); ls = MFMA16(ones, p1, ls); }
            lt = (lane & 16) ? ls[1] : ls[0];
        }
        const float inv = __builtin_amdgcn_rcpf(lt);
        const unsigned base = (unsigned)WS_O + (((br * 128 + rb * 16 + gt * 2 + (r32 >> 4)) * 32) + hp * 2 + wc) * 1024 + hh * 256 + (r32 & 15) * 16;
#pragma unroll
        for (int e = 0; e < 2; ++e) {
            const f32x16& x = o[d];
            const u32x4 v = {cvtpk(fmul_s(x[8 * e], inv), fmul_s(x[8 * e + 1], inv)), cvtpk(fmul_s(x[8 * e + 2], inv), fmul_s(x[8 * e + 3], inv)), cvtpk(fmul_s(x[8 * e + 4], inv), fmul_s(x[8 * e + 5], inv)), cvtpk(fmul_s(x[8 * e + 6], inv), fmul_s(x[8 * e + 7], inv))};
            __builtin_amdgcn_raw_buffer_store_b128(v, srdW, base + e * 512, 0, 16);
        }
    }
    STAMP(7);
    auto phase3 = [&](auto HC) {
        constexpr int H = decltype(HC)::value;
        const int lane = lane_id(), r16 = lane & 15, g4 = lane >> 4, q4 = (lane & 15) >> 2, p4 = lane & 3;
        GAS char* ep = (GAS char*)wsp + WS_EO + (lane < 16 ? (size_t)(grp + lane) * 128 + hp * 8 : (size_t)me * 128 + (lane & 15) * 8);
        u64 eo;
        asm volatile("global_load_dwordx2 %0, %1, off sc1" : "=v"(eo) : "v"(ep) : "memory");
        unsigned woff[8];
        { const int row = 16 * (g4 >> 1) + 4 * (g4 & 1) + q4;
#pragma unroll
          for (int n = 0; n < 8; ++n) woff[n] = 131072 + H * 8192 + row * 256 + ((n ^ (row & 7)) << 5) + p4 * 8; }
        bf16x8 wf[2][8];
        auto wread = [&](int s, int n) {
            if (s == 2 || s == 12) { woff[n] -= 131072; PIN(woff[n]); } else if (s == 6 || s == 10) { woff[n] += 65536; PIN(woff[n]); }
            LAS const char* p = lds + woff[n] + (((s + 8) % 10) & 3) * 16384;
            const s16x4 lo = trread(p), hi = trread(p + 2048); wf[s & 1][n] = __builtin_shufflevector(lo, hi, 0, 1, 2, 3, 4, 5, 6, 7); };
        waitv<5>(); WAITL0(); BAR();
        wo_steps(2, 2 + WPRE);
#pragma unroll
        for (int n = 0; n < 8; ++n) { wread(0, n); wread(1, n); }
        waitv<2 * WPRE>(); PIN(eo); WAITL0(); BAR();
        if (H == 0 && wid == 0) {
            const u64 tag = eo + TAGK;
            if (lane < 16) __hip_atomic_store((GAS u64*)(ep - WS_EO + WS_FO), tag, __ATOMIC_RELAXED, __HIP_MEMORY_SCOPE_AGENT);
        }
        wo_steps(2 + WPRE, 12);
        if (H == 0 && wid == 0) {
            const u64 tag = eo + TAGK;
            GAS u64* f = (GAS u64*)(wsp + WS_FO + (size_t)me * 128 + (lane & 15) * 8);
            const bool mine = lane >= 16 && lane < 32;
#pragma nounroll
            for (int spin = 0; spin < SPIN_CAP; ++spin) {
                const u64 v = __hip_atomic_load(f, __ATOMIC_RELAXED, __HIP_MEMORY_SCOPE_AGENT);
                if (__all(!mine || v == tag)) break;
                __builtin_amdgcn_s_sleep(1);
            }
            if (mine) __hip_atomic_store((GAS u64*)ep, tag, __ATOMIC_RELAXED, __HIP_MEMORY_SCOPE_AGENT);
        }
        waitv<0>(); BAR();
        STAMP(8);
        bf16x8 of[OW][2];
        const unsigned obase = (unsigned)WS_O + ((br * 128 + rb * 16 + th * 8 + w4 * 2) * 32 + H * 16) * 1024 + lane * 16;
        auto oload = [&](int s, int t) { of[s % OW][t] = __builtin_bit_cast(bf16x8, __builtin_amdgcn_raw_buffer_load_b128(srdW, obase + t * 32768 + s * 1024, 0, 16)); };
#pragma unroll
        for (int s = 0; s < OW - 1; ++s) { oload(s, 0); oload(s, 1); SBAR(); }
        f32x4 c2[2][8];
#pragma unroll
        for (int t = 0; t < 2; ++t)
#pragma unroll
            for (int n = 0; n < 8; ++n) c2[t][n] = (f32x4){0.f, 0.f, 0.f, 0.f};
        const unsigned voffW0 = (lane >> 4) * 2048 + (((lane & 15) ^ ((lane >> 4) << 1)) << 4), voffW1 = (4 + (lane >> 4)) * 2048 + (((lane & 15) ^ ((4 + (lane >> 4)) << 1)) << 4);
        {
            auto step = [&](int s) {
#pragma unroll
                for (int n = 0; n < 8; ++n) {
                    c2[0][n] = MFMA16(wf[s & 1][n], of[s % OW][0], c2[0][n]); c2[1][n] = MFMA16(wf[s & 1][n], of[s % OW][1], c2[1][n]);
                    SBAR();
                    if (s + 2 < 16) wread(s + 2, n);
                    if (n == PA0 + 2 * H && s + OW - 1 < 16) oload(s + OW - 1, 0);
                    if (n == PA0 + 4 + 2 * H && s + OW - 1 < 16) oload(s + OW - 1, 1);
                    if (s >= 4 && s < 8 && (n == PA0 + 2 - 2 * H || n == PA0 + 6 - 2 * H)) dma16(ldsb + ((s + 16) % 10) * 16384 + wid * 2048 + (n >= 4) * 1024, n >= 4 ? voffW1 : voffW0, srdWo, sW + (s + 8) * 65536);
                    SBAR();
                } };
#pragma unroll
            for (int s = 0; s < 4; ++s) { step(s); if (s == 0) STAMP(11); }
            WAITL0(); BAR();
#pragma unroll
            for (int s = 4; s < 10; ++s) step(s);
            STAMP(13);
            waitv<4>(); BAR();
            STAMP(14);
#pragma unroll
            for (int s = 10; s < 15; ++s) step(s);
#pragma unroll
            for (int n = 4 - 4 * H; n < 8 - 4 * H; ++n) { c2[0][n] = MFMA16(wf[1][n], of[15 % OW][0], c2[0][n]); c2[1][n] = MFMA16(wf[1][n], of[15 % OW][1], c2[1][n]); }
            SBAR();
#pragma unroll
            for (int n = 0; n < 4; ++n) {
                c2[0][4 * H + n] = MFMA16(wf[1][4 * H + n], of[15 % OW][0], c2[0][4 * H + n]); c2[1][4 * H + n] = MFMA16(wf[1][4 * H + n], of[15 % OW][1], c2[1][4 * H + n]);
                SBAR();
#pragma unroll
                for (int t = 0; t < 2; ++t) *(LAS f32x4*)(lds + 65536 + wid * 8192 + (t * 4 + n) * 1024 + lane * 16) = c2[t][4 - 4 * H + n];
                SBAR();
            }
            STAMP(9);
            WAITL0(); BAR();
            STAMP(15);
            GAS u16* outp = (GAS u16*)outb + (size_t)(rb * 256 + th * 128 + w4 * 32 + r16) * 2048 + br * 1024 + ct * 128 + H * 64 + (g4 & 1) * 16 + (g4 >> 1) * 8;
#pragma unroll
            for (int t = 0; t < 2; ++t)
#pragma unroll
                for (int n = 0; n < 4; n += 2) {
                    const f32x4 x = c2[t][4 * H + n] + *(LAS const f32x4*)(lds + 65536 + (wid ^ 4) * 8192 + (t * 4 + n) * 1024 + lane * 16);
                    const f32x4 y = c2[t][4 * H + n + 1] + *(LAS const f32x4*)(lds + 65536 + (wid ^ 4) * 8192 + (t * 4 + n + 1) * 1024 + lane * 16);
                    const auto u0 = __builtin_amdgcn_permlane16_swap(cvtpk(x[0], x[1]), cvtpk(y[0], y[1]), false, false);
                    const auto u1 = __builtin_amdgcn_permlane16_swap(cvtpk(x[2], x[3]), cvtpk(y[2], y[3]), false, false);
                    { const u32x4 vv = {u0[0], u1[0], u0[1], u1[1]}; GAS u16* pp = outp + (size_t)t * 16 * 2048 + n * 16; asm volatile("global_store_dwordx4 %0, %1, off sc0 sc1\n\ts_nop 1" :: "v"(pp), "v"(vv) : "memory"); }
                }
        }
    };
    if (half == 0) phase3(std::integral_constant<int, 0>{}); else phase3(std::integral_constant<int, 1>{});
}

extern "C" void kernel_launch(void* const* d_in, const int* in_sizes, int n_in, void* d_out, int out_size, void* d_ws, size_t ws_size, hipStream_t stream) {
    Args a;
    a.x[0] = (const u16*)d_in[0]; a.w[0] = (const u16*)d_in[1]; a.wo[0] = (const u16*)d_in[2];
    a.x[1] = (const u16*)d_in[3]; a.w[1] = (const u16*)d_in[4]; a.wo[1] = (const u16*)d_in[5];
    a.out = (u16*)d_out; a.ws = (char*)d_ws;
    fused_kernel<<<256, 512, 0, stream>>>(a);
}
```

```cpp
#define MCLK __builtin_amdgcn_s_memrealtime
#define MDIV 1
#define MA 6
#define MB 7
#include <hip/hip_runtime.h>
#include <hip/hip_bf16.h>
#include <type_traits>
typedef short bf16x8 __attribute__((ext_vector_type(8)));
typedef short s16x4 __attribute__((ext_vector_type(4)));
typedef float f32x16 __attribute__((ext_vector_type(16)));
typedef float f32x4 __attribute__((ext_vector_type(4)));
typedef float f32x2 __attribute__((ext_vector_type(2)));
typedef __bf16 bf16x2 __attribute__((ext_vector_type(2)));
typedef unsigned u32x4 __attribute__((ext_vector_type(4)));
typedef double f64x8 __attribute__((ext_vector_type(8)));
typedef double f64x2 __attribute__((ext_vector_type(2)));
typedef unsigned short u16;
typedef unsigned long long u64;
#define LAS __attribute__((address_space(3)))
#define GAS __attribute__((address_space(1)))
#define DI __device__ __forceinline__
#define MFMA16(a, b, c) __builtin_amdgcn_mfma_f32_16x16x32_bf16((a), (b), (c), 0, 0, 0)
#define MFMA32(a, b, c) __builtin_amdgcn_mfma_f32_32x32x16_bf16((a), (b), (c), 0, 0, 0)
#define SBAR() __builtin_amdgcn_sched_barrier(0)
#define BAR() do { SBAR(); asm volatile("s_barrier" ::: "memory"); SBAR(); } while (0)
#define PIN(x) asm volatile("" : "+v"(x))
#define WAITL0() __builtin_amdgcn_s_waitcnt(0xC07F)
template <int N> DI void waitv() { asm volatile("s_waitcnt vmcnt(%0)" :: "i"(N) : "memory"); }
DI unsigned rfl(unsigned x) { return __builtin_amdgcn_readfirstlane(x); }
DI int lane_id() { int l; asm volatile("v_mbcnt_lo_u32_b32 %0, -1, 0\n\tv_mbcnt_hi_u32_b32 %0, -1, %0" : "=v"(l)); return l; }
DI unsigned cvtpk(float lo, float hi) { f32x2 v = {lo, hi}; bf16x2 b = __builtin_convertvector(v, bf16x2); return __builtin_bit_cast(unsigned, b); }
DI void dma16(unsigned m0v, unsigned voff, __amdgpu_buffer_rsrc_t srd, unsigned soff) {
    asm volatile("s_mov_b32 m0, %0\n\ts_nop 0\n\tbuffer_load_dwordx4 %1, %2, %3 offen lds" :: "s"(m0v), "v"(voff), "s"(srd), "s"(soff) : "memory");
}
DI void dma4(unsigned m0v, unsigned voff, __amdgpu_buffer_rsrc_t srd, unsigned soff) {
    asm volatile("s_mov_b32 m0, %0\n\ts_nop 0\n\tbuffer_load_dword %1, %2, %3 offen lds" :: "s"(m0v), "v"(voff), "s"(srd), "s"(soff) : "memory");
}
DI s16x4 trread(LAS const char* p) { return __builtin_amdgcn_ds_read_tr16_b64_v4i16((LAS s16x4*)p); }
DI bf16x8 ldsfrag(LAS const char* p) { return *(LAS const bf16x8*)p; }
DI float max3f(float a, float b, float c) { return __builtin_fmaxf(__builtin_fmaxf(a, b), c); }
DI float fadd_s(float a, float b) { return a + b; }
DI float fsub_s(float a, float b) { return a - b; }
DI float fmul_s(float a, float b) { return a * b; }
DI bf16x8 pack8(float a, float b, float c, float d, float e, float f, float g, float h) { u32x4 v = {cvtpk(a, b), cvtpk(c, d), cvtpk(e, f), cvtpk(g, h)}; return __builtin_bit_cast(bf16x8, v); }

constexpr int LDS_A0 = 0, LDS_A1 = 32768, LDS_B = 81920, A_SLOT = 16384, B_SLOT = 24576;
constexpr int LDS_TOTAL = 163840;
constexpr size_t WS_FKV = 0, WS_EKV = 32768, WS_FO = 65536, WS_EO = 98304, WS_KV = 1 << 20, WS_O = 17 << 20, WS_END = 25 << 20, WS_ST = 32 << 20;
constexpr u64 TAGK = 0x9E3779B97F4A7C15ull;
constexpr int SPIN_CAP = 400000;
constexpr float QSCALE = 0.17677669529663687f * 1.4426950408889634f;

#define PUBJ 3
#define POLJ 1
#define SPJ 4
#define OW 5
#define PA0 1
#define WPRE 4
#define LSAFE 1.152921504606847e18f
#define KROT 0
#define CODEPF 48
#define TOUCH 1
#define PD 5
constexpr int LDS_SCR = 155648;
#define PT3W 0
#define PT3X 0
#define KVT 0
#define MAP84 0
#define PGT 0
#define NEARLY 7
struct Args { const u16* x[2]; const u16* w[2]; const u16* wo[2]; u16* out; char* ws; };

#define STAMP(i) do { if ((i) == MA) mta = MCLK(); if ((i) == MB) mtb = MCLK(); } while (0)
__global__ __launch_bounds__(512, 2) void fused_kernel(Args a) {
    __shared__ __attribute__((aligned(16))) char smem[LDS_TOTAL];
    LAS char* lds = (LAS char*)smem;
    const unsigned ldsb = (unsigned)(size_t)smem;
    const int tid = threadIdx.x, lane0 = tid & 63;
    u64 pc0; asm volatile("s_getpc_b64 %0" : "=s"(pc0));
    const int wid = rfl(tid >> 6), half = wid >> 2, w4 = wid & 3;
    const int bid = blockIdx.x, g = bid & 7, j = bid >> 3;
    const int br = g >> 2, rb = MAP84 ? (j & 7) : ((g >> 1) & 1) * 4 + (j & 3), hp = MAP84 ? (g & 3) * 4 + (j >> 3) : (g & 1) * 8 + (j >> 2);
    const int me = (br * 8 + rb) * 16 + hp, grp = me & ~15;
    u64 mta = 0, mtb = 0;
    STAMP(0);
    char* wsp = a.ws; const u16* wop = a.wo[br]; u16* outb = a.out; const u16* xp = a.x[br]; const u16* wp = a.w[br];
    asm volatile("" : "+s"(wsp), "+s"(wop), "+s"(outb), "+s"(xp), "+s"(wp));
    const char* ea = wsp + WS_EKV + (size_t)(lane0 == 0 || rb == 0 ? me : me - 16) * 128;
    if (PGT) ea = lane0 == 2 ? wsp + WS_KV + (size_t)me * 65536 : lane0 == 3 ? wsp + WS_O + (size_t)(br * 128 + rb * 16) * 32768 : lane0 == 4 ? (const char*)outb + (size_t)rb * 256 * 4096
                : lane0 == 5 ? (const char*)wop + hp * 131072 : ea;
    u64 ew = __hip_atomic_load((GAS u64*)ea, __ATOMIC_RELAXED, __HIP_MEMORY_SCOPE_AGENT);
    if (CODEPF && wid == 7) {
        const __amdgpu_buffer_rsrc_t srdC = __builtin_amdgcn_make_buffer_rsrc((void*)pc0, (short)0, CODEPF * 1024, 0x00020000);
        if (lane0 < 13) dma4(ldsb + LDS_SCR, (j * 13 + lane0) * 128, srdC, 0);
    }
    const __amdgpu_buffer_rsrc_t srdA = __builtin_amdgcn_make_buffer_rsrc((void*)(xp + (size_t)rb * 256 * 1024), (short)0, 256 * 2048, 0x00020000);
    const __amdgpu_buffer_rsrc_t srdB = __builtin_amdgcn_make_buffer_rsrc((void*)wp, (short)0, 1024 * 6144, 0x00020000);
    const int wr = 2 * half + (w4 >> 1), wc = w4 & 1;
    const unsigned voffE = (lane0 >> 3) * 2048 + (((lane0 & 7) ^ (lane0 >> 4)) << 4);
    const unsigned voffO = (8 + (lane0 >> 3)) * 2048 + (((lane0 & 7) ^ (4 + (lane0 >> 4))) << 4);
    const unsigned voffB0 = (lane0 >> 3) * 6144 + (((lane0 & 7) ^ (((lane0 >> 4) & 1) << 1)) << 4);
    const unsigned voffB1 = (lane0 >> 3) * 6144 + (((lane0 & 7) ^ ((((lane0 >> 4) & 1) << 1) | 4)) << 4);
    const int gt0 = (w4 & 1) ? 7 - (w4 >> 1) : (w4 >> 1);
    const int gt1 = (w4 & 1) ? 5 - (w4 >> 1) : 2 + (w4 >> 1);
    const unsigned sA0 = gt0 * 32 * 2048, sA1 = gt1 * 32 * 2048;
    const unsigned sB = hp * 128 + (2 * w4) * 8 * 6144;
    auto piece = [&](auto HC, int kt, int s2, int s3i, int i) {
        constexpr int H = decltype(HC)::value;
        const unsigned soB = sB + kt * (64 * 6144), dB = ldsb + LDS_B + s3i * B_SLOT + w4 * 2048;
        const unsigned soA0 = sA0 + kt * 128, dA0 = ldsb + LDS_A0 + s2 * A_SLOT + w4 * 4096;
        const unsigned soA1 = sA1 + kt * 128, dA1 = ldsb + LDS_A1 + s3i * A_SLOT + w4 * 4096;
        if (H == 0) {
            if (i == 6) dma16(dA1 + 3 * 1024, voffO, srdA, soA1 + 32768);
            else dma16(dB + (i >> 1) * 8192 + (i & 1) * 1024, (i & 1) ? voffB1 : voffB0, srdB, soB + (i >> 1) * 2048 + (i & 1) * 8 * 6144);
        } else dma16((i < 4 ? dA0 : dA1) + (i & 3) * 1024, (i & 1) ? voffO : voffE, srdA, (i < 4 ? soA0 : soA1) + ((i >> 1) & 1) * 32768);
    };
    if (half == 0) {
#pragma unroll
        for (int i = 0; i < NEARLY; ++i) piece(std::integral_constant<int, 0>{}, i / 7, i / 7, i / 7, i % 7);
    } else {
#pragma unroll
        for (int i = 0; i < NEARLY; ++i) piece(std::integral_constant<int, 1>{}, i / 7, i / 7, i / 7, i % 7);
    }
    SBAR();
    const int lane = lane_id();
    const int r16 = lane & 15, g4 = lane >> 4, q4 = (lane & 15) >> 2, p4 = lane & 3;
    f32x4 acc[4][6];
    const int r32 = lane & 31, hh = lane >> 5;
    f32x16 mk;
#pragma unroll
    for (int r = 0; r < 16; ++r) mk[r] = ((r & 3) + 8 * (r >> 2) <= r32 - 4 * hh) ? 0.f : -INFINITY;
    {
        unsigned aoff[2], boff[2];
#pragma unroll
        for (int s = 0; s < 2; ++s) aoff[s] = (half ? LDS_A1 : LDS_A0) + ((w4 >> 1) * 64 + r16) * 128 + (((4 * s + g4) ^ ((r16 >> 1) & 7)) << 4);
#pragma unroll
        for (int c = 0; c < 2; ++c) boff[c] = LDS_B + (8 * g4 + q4) * 128 + (((wc * 2 + c) ^ ((q4 >> 1) | ((g4 & 1) << 1))) << 5) + p4 * 8;
#pragma unroll
        for (int i = 0; i < 4; ++i)
#pragma unroll
            for (int s = 0; s < 6; ++s) acc[i][s] = (f32x4){0.f, 0.f, 0.f, 0.f};
        bf16x8 af[4][2], bfr[6][2];
        const unsigned voffT = (16 * (rb & 3) + 4 * w4 + lane / 3) * 6144 + (lane % 3) * 2048 + hp * 128;
        const bool slowc = ((((size_t)wp >> 7) + hp) & 7) == 3;
        auto run = [&](auto HC, auto TC) {
            constexpr int H = decltype(HC)::value;
            constexpr int TW = decltype(TC)::value;
            auto touch = [&](int kt) { if (lane < 12) dma4(ldsb + LDS_SCR, voffT, srdB, kt * (64 * 6144)); };
#pragma unroll
            for (int i = 0; i < 4; ++i) asm volatile("" : "+v"(acc[i][0]), "+v"(acc[i][1]), "+v"(acc[i][2]), "+v"(acc[i][3]), "+v"(acc[i][4]), "+v"(acc[i][5]));
            PIN(mk); PIN(aoff[0]); PIN(aoff[1]); PIN(boff[0]); PIN(boff[1]);
            SBAR();
#pragma unroll
            for (int i = NEARLY; i < 14; ++i) piece(HC, i / 7, i / 7, i / 7, i % 7);
            if (H == 0) waitv<7>(); else waitv<10>();
            BAR();
            if (TW) {
#pragma unroll
                for (int kt = 3; kt < PD; ++kt) touch(kt);
            }
            if (H == 1) { waitv<7>(); BAR(); }
            STAMP(1);
            int s3 = 0;
            auto Lseg = [&](int t, bool issue) {
                const unsigned aslot = H ? s3 * A_SLOT : (t & 1) * A_SLOT;
                const unsigned bslot = s3 * B_SLOT;
                const int s3n = (s3 == 0) ? 2 : s3 - 1;
                auto rdA = [&](int i) { LAS const char* pa = lds + aoff[i >> 1] + aslot + (i & 1) * 4096; af[(i & 1) * 2][i >> 1] = ldsfrag(pa); af[(i & 1) * 2 + 1][i >> 1] = ldsfrag(pa + 2048); };
                auto rdB = [&](int i) { const int s = i / 6, seg = (i % 6) >> 1, c = i & 1; LAS const char* pb = lds + boff[c] + bslot;
                    s16x4 lo = trread(pb + seg * 8192 + s * 4096), hi = trread(pb + seg * 8192 + s * 4096 + 512);
                    bfr[seg * 2 + c][s] = __builtin_shufflevector(lo, hi, 0, 1, 2, 3, 4, 5, 6, 7); };
                if (TW && TOUCH == 2 && issue) touch(t + PD);
#pragma unroll
                for (int i = 0; i < 16; ++i) {
                    if (issue && (i & 1) == 0 && i / 2 < 7) piece(HC, t + 2, t & 1, s3n, i / 2);
                    if (TW && TOUCH == 3 && issue && i == 13) touch(t + PD);
                    if (i < 4) rdA(i); else rdB(i - 4);
                }
                if (TW && TOUCH == 1 && issue) touch(t + PD);
                s3 = (s3 == 2) ? 0 : s3 + 1;
            };
            auto Mseg = [&]() {
                __builtin_amdgcn_s_setprio(1);
#pragma unroll
                for (int s = 0; s < 2; ++s)
#pragma unroll
                    for (int m = 0; m < 4; ++m)
#pragma unroll
                        for (int n = 0; n < 6; ++n) acc[m][n] = n < 4 ? MFMA16(bfr[n][s], af[m][s], acc[m][n]) : MFMA16(af[m][s], bfr[n][s], acc[m][n]);
                __builtin_amdgcn_s_setprio(0);
            };
            if (TW && TOUCH != 2) { Lseg(0, true); WAITL0(); BAR(); Mseg(); waitv<8 + PD - 3>(); BAR(); }
            for (int t = (TW && TOUCH != 2) ? 1 : 0; t < 14; ++t) {
                Lseg(t, true);
                if (H == 1) waitv<10>();
                WAITL0(); BAR();
                Mseg();
                waitv<7 + (TW ? (TOUCH == 2 ? 1 : 2) : 0)>();
                BAR();
            }
            Lseg(14, false); if (H == 1) waitv<3>(); WAITL0(); BAR(); Mseg(); waitv<0>(); BAR();
            Lseg(15, false); WAITL0(); BAR(); Mseg(); if (H == 0) BAR();
        };
        if (half == 1) run(std::integral_constant<int, 1>{}, std::integral_constant<int, 0>{});
        else if (TOUCH && slowc) run(std::integral_constant<int, 0>{}, std::integral_constant<int, 1>{});
        else run(std::integral_constant<int, 0>{}, std::integral_constant<int, 0>{});
    }
    STAMP(2);
    const u64 tagp = (((u64)__builtin_amdgcn_readlane((unsigned)(ew >> 32), 0) << 32) | __builtin_amdgcn_readlane((unsigned)ew, 0)) + TAGK;
    const u64 tagc = (((u64)__builtin_amdgcn_readlane((unsigned)(ew >> 32), 1) << 32) | __builtin_amdgcn_readlane((unsigned)ew, 1)) + TAGK;
    const __amdgpu_buffer_rsrc_t srdW = __builtin_amdgcn_make_buffer_rsrc((void*)wsp, (short)0, (int)WS_END, 0x00020000);
    const __amdgpu_buffer_rsrc_t srdWo = __builtin_amdgcn_make_buffer_rsrc((void*)(wop), (short)0, 1024 * 2048, 0x00020000);
    const int ct = hp & 7, th = hp >> 3;
    const unsigned sW = ct * 256 + (half * 512 + w4 * 8) * 2048;
    auto wo_steps = [&](int s0, int s1) {
        const int ln = lane_id();
        const unsigned voffW0 = (ln >> 4) * 2048 + (((ln & 15) ^ ((ln >> 4) << 1)) << 4), voffW1 = (4 + (ln >> 4)) * 2048 + (((ln & 15) ^ ((4 + (ln >> 4)) << 1)) << 4);
#pragma unroll
        for (int s = s0; s < s1; ++s) { const unsigned d = ldsb + ((s + 8) % 10) * 16384 + wid * 2048; dma16(d, voffW0, srdWo, sW + s * 65536); dma16(d + 1024, voffW1, srdWo, sW + s * 65536); } };
    bf16x8 qf[2][2];
    {
        const unsigned kpos = (g4 >> 1) * 1024 + (g4 & 1) * 512 + r16 * 16;
        const unsigned vpos = 2048 + ((g4 & 1) * 32 + (g4 >> 1) * 16 + r16) * 16;
        const unsigned gkv = (unsigned)WS_KV + me * 65536 + wc * 32768;
        u32x4 kv[2][2], vv[2][2];
#pragma unroll
        for (int mi = 0; mi < 2; ++mi)
#pragma unroll
            for (int s = 0; s < 2; ++s) {
                const f32x4 k0 = acc[2 * mi + s][2], k1 = acc[2 * mi + s][3], v0 = acc[2 * mi + s][4], v1 = acc[2 * mi + s][5];
                kv[mi][s] = (u32x4){cvtpk(k0[0], k0[1]), cvtpk(k0[2], k0[3]), cvtpk(k1[0], k1[1]), cvtpk(k1[2], k1[3])};
                const auto ra = __builtin_amdgcn_permlane32_swap(cvtpk(v0[0], v0[1]), cvtpk(v1[0], v1[1]), false, false);
                const auto rbb = __builtin_amdgcn_permlane32_swap(cvtpk(v0[2], v0[3]), cvtpk(v1[2], v1[3]), false, false);
                vv[mi][s] = (u32x4){ra[0], rbb[0], ra[1], rbb[1]};
                const unsigned blk = wc * 65536 + 32768 + (mi ? 7 - wr : wr) * 4096;
                *(LAS u32x4*)(lds + blk + kpos + s * 256) = kv[mi][s];
                *(LAS u32x4*)(lds + blk + vpos + s * 1024) = vv[mi][s];
            }
        if (rb < 7) {
#pragma unroll
            for (int mi = 0; mi < 2; ++mi)
#pragma unroll
                for (int s = 0; s < 2; ++s) { const unsigned blk = (mi ? 7 - wr : wr) * 4096;
                    __builtin_amdgcn_raw_buffer_store_b128(kv[mi][s], srdW, gkv + blk + kpos + s * 256, 0, 16);
                    __builtin_amdgcn_raw_buffer_store_b128(vv[mi][s], srdW, gkv + blk + vpos + s * 1024, 0, 16); }
        }
#pragma unroll
        for (int mi = 0; mi < 2; ++mi) {
            u32x4 x[2], y0, y1;
#pragma unroll
            for (int s = 0; s < 2; ++s) { const f32x4 q0 = acc[2 * mi + s][0] * QSCALE, q1 = acc[2 * mi + s][1] * QSCALE;
                x[s] = (u32x4){cvtpk(q0[0], q0[1]), cvtpk(q0[2], q0[3]), cvtpk(q1[0], q1[1]), cvtpk(q1[2], q1[3])}; }
#pragma unroll
            for (int i = 0; i < 4; ++i) { const auto u = __builtin_amdgcn_permlane16_swap(x[0][i], x[1][i], false, false);
                const auto v = __builtin_amdgcn_permlane32_swap(u[0], u[1], false, false); y0[i] = v[0]; y1[i] = v[1]; }
            qf[mi][0] = __builtin_bit_cast(bf16x8, y0); qf[mi][1] = __builtin_bit_cast(bf16x8, y1);
        }
    }
    f32x16 o[2], nm[2]; f32x4 l[2];
    LAS const char* kvb = lds + wc * 65536 + lane * 16;
    bf16x8 ones;
    { const short v = ((lane ^ (lane >> 4)) & 1) ? (short)0 : (short)0x3f80;
#pragma unroll
      for (int i = 0; i < 8; ++i) ones[i] = v; }
    auto jmax = [&](const f32x16& s, float m) {
#pragma unroll
        for (int r = 0; r < 16; r += 2) m = max3f(m, s[r], s[r + 1]);
        return m; };
    auto xmax = [&](float m) { const auto r = __builtin_amdgcn_permlane32_swap(__float_as_uint(m), __float_as_uint(m), false, false); return __builtin_fmaxf(__uint_as_float(r[0]), __uint_as_float(r[1])); };
    auto expo = [&](const f32x16& s, bf16x8& p0, bf16x8& p1) {
        float t[16];
#pragma unroll
        for (int r = 0; r < 16; ++r) t[r] = __builtin_amdgcn_exp2f(s[r]);
        p0 = pack8(t[0], t[1], t[2], t[3], t[4], t[5], t[6], t[7]); p1 = pack8(t[8], t[9], t[10], t[11], t[12], t[13], t[14], t[15]); };
    auto flip = [&](const f32x16& m) { f32x16 c;
#pragma unroll
        for (int r = 0; r < 16; ++r) c[r] = __uint_as_float(__float_as_uint(m[r]) ^ 0xff800000u);
        return c; };
    {
        f32x16 zero16, s[2];
#pragma unroll
        for (int r = 0; r < 16; ++r) zero16[r] = 0.f;
#pragma unroll
        for (int d = 0; d < 2; ++d) {
            LAS const char* p = kvb + (8 + (d ? 7 - wr : wr)) * 4096;
            s[d] = MFMA32(ldsfrag(p), qf[d][0], mk); s[d] = MFMA32(ldsfrag(p + 1024), qf[d][1], s[d]);
        }
        wo_steps(0, 2);
        const float m0 = xmax(jmax(s[0], -INFINITY)), m1 = xmax(jmax(s[1], -INFINITY));
#pragma unroll
        for (int d = 0; d < 2; ++d) {
            LAS const char* p = kvb + (8 + (d ? 7 - wr : wr)) * 4096;
            const float m = d ? m1 : m0;
#pragma unroll
            for (int r = 0; r < 16; ++r) { nm[d][r] = -m; s[d][r] = fsub_s(s[d][r], m); }
            bf16x8 p0, p1;
            expo(s[d], p0, p1);
            o[d] = MFMA32(ldsfrag(p + 2048), p0, zero16); l[d] = MFMA16(ones, p0, ((f32x4){0.f, 0.f, 0.f, 0.f}));
            o[d] = MFMA32(ldsfrag(p + 3072), p1, o[d]); l[d] = MFMA16(ones, p1, l[d]);
        }
    }
    mk = flip(mk); PIN(mk);
    waitv<4>(); WAITL0(); BAR();
    if (wid == 0 && lane == 0 && rb < 7) __hip_atomic_store((GAS u64*)(wsp + WS_FKV + (size_t)me * 128), tagp, __ATOMIC_RELAXED, __HIP_MEMORY_SCOPE_AGENT);
    STAMP(3);
    auto attention = [&](auto WC_) {
        constexpr int WR = decltype(WC_)::value;
        u32x4 pld[8];
        auto jobs = [&](auto PART) {
            constexpr int P2 = decltype(PART)::value;
            constexpr int NJ = P2 ? 9 : 7;
            auto TOF = [](int j) { return P2 ? (j < 7 ? (j < 7 - WR ? 0 : 1) : j - 7) : (j < WR ? 0 : 1); };
            auto BOF = [](int j) { return P2 ? (j < 7 ? (j < 7 - WR ? WR + 1 + j : j + 1) : (j == 7 ? WR : 7 - WR)) : 8 + (j < WR ? j : j - WR); };
            f32x16 S[2], cm; bf16x8 kf[2][2], vf[2][2]; u32x4 p0, p1;
            auto rdk = [&](int j) { kf[j & 1][0] = ldsfrag(kvb + BOF(j) * 4096); kf[j & 1][1] = ldsfrag(kvb + BOF(j) * 4096 + 1024); };
            auto cof = [&](int j) -> const f32x16& { return (P2 && j >= 7) ? cm : nm[TOF(j)]; };
            auto mkc = [&](int j) { const f32x16& mi = mk;
#pragma unroll
                for (int r = 0; r < 16; ++r) cm[r] = fadd_s(mi[r], nm[TOF(j)][r]); };
            rdk(0); rdk(1); vf[0][0] = ldsfrag(kvb + BOF(0) * 4096 + 2048);
            S[0] = MFMA32(kf[0][0], qf[TOF(0)][0], cof(0)); S[0] = MFMA32(kf[0][1], qf[TOF(0)][1], S[0]);
            SBAR();
#pragma unroll
            for (int j = 0; j < NJ; ++j) {
                const int T = TOF(j), TP = j > 0 ? TOF(j - 1) : 0, TN = j + 1 < NJ ? TOF(j + 1) : 0;
                f32x16& s = S[j & 1];
#define EX(i) s[i] = __builtin_amdgcn_exp2f(s[i])
                if (P2 && j + 1 >= 7 && j + 1 < NJ) { mkc(j + 1); PIN(cm); }
                if (j + 1 < NJ) S[(j + 1) & 1] = MFMA32(kf[(j + 1) & 1][0], qf[TN][0], cof(j + 1));
                SBAR();
                vf[j & 1][1] = ldsfrag(kvb + BOF(j) * 4096 + 3072);
                EX(0); EX(1); EX(2); PIN(s); SBAR();
                if (j > 0) o[TP] = MFMA32(vf[(j - 1) & 1][1], __builtin_bit_cast(bf16x8, p1), o[TP]);
                SBAR();
                EX(3); EX(4); EX(5); PIN(s); SBAR();
                if (j > 0) l[TP] = MFMA16(ones, __builtin_bit_cast(bf16x8, p1), l[TP]);
                SBAR();
                EX(6); EX(7);
                p0 = (u32x4){cvtpk(s[0], s[1]), cvtpk(s[2], s[3]), cvtpk(s[4], s[5]), cvtpk(s[6], s[7])}; PIN(p0); SBAR();
                if (j + 1 < NJ) S[(j + 1) & 1] = MFMA32(kf[(j + 1) & 1][1], qf[TN][1], S[(j + 1) & 1]);
                SBAR();
                if (j + 1 < NJ) vf[(j + 1) & 1][0] = ldsfrag(kvb + BOF(j + 1) * 4096 + 2048);
                EX(8); EX(9); EX(10); PIN(s); SBAR();
                o[T] = MFMA32(vf[j & 1][0], __builtin_bit_cast(bf16x8, p0), o[T]);
                SBAR();
                if (j + 2 < NJ) rdk(j + 2);
                EX(11); EX(12); EX(13); PIN(s); SBAR();
                l[T] = MFMA16(ones, __builtin_bit_cast(bf16x8, p0), l[T]);
                SBAR();
                EX(14); EX(15);
                p1 = (u32x4){cvtpk(s[8], s[9]), cvtpk(s[10], s[11]), cvtpk(s[12], s[13]), cvtpk(s[14], s[15])}; PIN(p1); SBAR();
#undef EX
                if (j == NJ - 1) { o[T] = MFMA32(vf[j & 1][1], __builtin_bit_cast(bf16x8, p1), o[T]); l[T] = MFMA16(ones, __builtin_bit_cast(bf16x8, p1), l[T]); }
                if (!P2 && j == POLJ && rb > 0) {
                    if (WR == 0 && wid == 0) {
                        GAS u64* f = (GAS u64*)(wsp + WS_FKV + (size_t)(me - 16) * 128);
#pragma nounroll
                        for (int spin = 0; spin < SPIN_CAP; ++spin) {
                            const u64 v = __hip_atomic_load(f, __ATOMIC_RELAXED, __HIP_MEMORY_SCOPE_AGENT);
                            if (rfl((unsigned)v) == (unsigned)tagc && rfl((unsigned)(v >> 32)) == (unsigned)(tagc >> 32)) break;
                            __builtin_amdgcn_s_sleep(1);
                        }
                        if (lane == 0) __hip_atomic_store((GAS u64*)(wsp + WS_EKV + (size_t)(me - 16) * 128), tagc, __ATOMIC_RELAXED, __HIP_MEMORY_SCOPE_AGENT);
                    }
                    BAR();
                    STAMP(5);
#pragma unroll
                    for (int k = 0; k < 8; ++k) pld[k] = __builtin_amdgcn_raw_buffer_load_b128(srdW, (unsigned)WS_KV + (me - 16) * 65536 + wid * 8192 + k * 1024 + lane * 16, 0, 16);
                    SBAR();
                }
            }
        };
        jobs(std::integral_constant<int, 0>{});
        STAMP(4);
        if (rb > 0) {
#pragma unroll
            for (int i = 0; i < 8; ++i) *(LAS u32x4*)(lds + half * 65536 + w4 * 8192 + i * 1024 + lane * 16) = pld[i];
            WAITL0(); BAR();
            STAMP(6);
            jobs(std::integral_constant<int, 1>{});
        }
    };
#define ATT(W) attention(std::integral_constant<int, W>{})
    if (wr == 0) ATT(0); else if (wr == 1) ATT(1); else if (wr == 2) ATT(2); else ATT(3);
#pragma unroll
    for (int d = 0; d < 2; ++d) {
        const int gt = d ? 7 - wr : wr;
        float lt = (lane & 16) ? l[d][1] : l[d][0];
        if (__builtin_expect(__any(!(lt <= LSAFE)), 0)) {
            const f32x16 mhi = mk, mlo = flip(mk);
            const int b0 = rb > 0 ? gt : 8, b1 = 8 + gt;
            auto sc = [&](int b, float ref) { f32x16 c;
#pragma unroll
                for (int r = 0; r < 16; ++r) c[r] = (b == b1 ? mlo[r] : b == gt ? mhi[r] : 0.f) - ref;
                c = MFMA32(ldsfrag(kvb + b * 4096), qf[d][0], c); return MFMA32(ldsfrag(kvb + b * 4096 + 1024), qf[d][1], c); };
            float m = -INFINITY;
#pragma nounroll
            for (int b = b0; b <= b1; ++b) m = jmax(sc(b, 0.f), m);
            m = xmax(m);
            f32x4 ls = {0.f, 0.f, 0.f, 0.f};
#pragma unroll
            for (int r = 0; r < 16; ++r) o[d][r] = 0.f;
#pragma nounroll
            for (int b = b0; b <= b1; ++b) { bf16x8 p0, p1; expo(sc(b, m), p0, p1);
                o[d] = MFMA32(ldsfrag(kvb + b * 4096 + 2048), p0, o[d]); o[d] = MFMA32(ldsfrag(kvb + b * 4096 + 3072), p1, o[d]);
                ls = MFMA16(ones, p0, ls); ls = MFMA16(ones, p1, ls); }
            lt = (lane & 16) ? ls[1] : ls[0];
        }
        const float inv = __builtin_amdgcn_rcpf(lt);
        const unsigned base = (unsigned)WS_O + (((br * 128 + rb * 16 + gt * 2 + (r32 >> 4)) * 32) + hp * 2 + wc) * 1024 + hh * 256 + (r32 & 15) * 16;
#pragma unroll
        for (int e = 0; e < 2; ++e) {
            const f32x16& x = o[d];
            const u32x4 v = {cvtpk(fmul_s(x[8 * e], inv), fmul_s(x[8 * e + 1], inv)), cvtpk(fmul_s(x[8 * e + 2], inv), fmul_s(x[8 * e + 3], inv)), cvtpk(fmul_s(x[8 * e + 4], inv), fmul_s(x[8 * e + 5], inv)), cvtpk(fmul_s(x[8 * e + 6], inv), fmul_s(x[8 * e + 7], inv))};
            __builtin_amdgcn_raw_buffer_store_b128(v, srdW, base + e * 512, 0, 16);
        }
    }
    STAMP(7);
    auto phase3 = [&](auto HC) {
        constexpr int H = decltype(HC)::value;
        const int lane = lane_id(), r16 = lane & 15, g4 = lane >> 4, q4 = (lane & 15) >> 2, p4 = lane & 3;
        GAS char* ep = (GAS char*)wsp + WS_EO + (lane < 16 ? (size_t)(grp + lane) * 128 + hp * 8 : (size_t)me * 128 + (lane & 15) * 8);
        u64 eo;
        asm volatile("global_load_dwordx2 %0, %1, off sc1" : "=v"(eo) : "v"(ep) : "memory");
        unsigned woff[8];
        { const int row = 16 * (g4 >> 1) + 4 * (g4 & 1) + q4;
#pragma unroll
          for (int n = 0; n < 8; ++n) woff[n] = 131072 + H * 8192 + row * 256 + ((n ^ (row & 7)) << 5) + p4 * 8; }
        bf16x8 wf[2][8];
        auto wread = [&](int s, int n) {
            if (s == 2 || s == 12) { woff[n] -= 131072; PIN(woff[n]); } else if (s == 6 || s == 10) { woff[n] += 65536; PIN(woff[n]); }
            LAS const char* p = lds + woff[n] + (((s + 8) % 10) & 3) * 16384;
            const s16x4 lo = trread(p), hi = trread(p + 2048); wf[s & 1][n] = __builtin_shufflevector(lo, hi, 0, 1, 2, 3, 4, 5, 6, 7); };
        waitv<5>(); WAITL0(); BAR();
        wo_steps(2, 2 + WPRE);
#pragma unroll
        for (int n = 0; n < 8; ++n) { wread(0, n); wread(1, n); }
        waitv<2 * WPRE>(); PIN(eo); WAITL0(); BAR();
        if (H == 0 && wid == 0) {
            const u64 tag = eo + TAGK;
            if (lane < 16) __hip_atomic_store((GAS u64*)(ep - WS_EO + WS_FO), tag, __ATOMIC_RELAXED, __HIP_MEMORY_SCOPE_AGENT);
        }
        wo_steps(2 + WPRE, 12);
        if (H == 0 && wid == 0) {
            const u64 tag = eo + TAGK;
            GAS u64* f = (GAS u64*)(wsp + WS_FO + (size_t)me * 128 + (lane & 15) * 8);
            const bool mine = lane >= 16 && lane < 32;
#pragma nounroll
            for (int spin = 0; spin < SPIN_CAP; ++spin) {
                const u64 v = __hip_atomic_load(f, __ATOMIC_RELAXED, __HIP_MEMORY_SCOPE_AGENT);
                if (__all(!mine || v == tag)) break;
                __builtin_amdgcn_s_sleep(1);
            }
            if (mine) __hip_atomic_store((GAS u64*)ep, tag, __ATOMIC_RELAXED, __HIP_MEMORY_SCOPE_AGENT);
        }
        waitv<0>(); BAR();
        STAMP(8);
        bf16x8 of[OW][2];
        const unsigned obase = (unsigned)WS_O + ((br * 128 + rb * 16 + th * 8 + w4 * 2) * 32 + H * 16) * 1024 + lane * 16;
        auto oload = [&](int s, int t) { of[s % OW][t] = __builtin_bit_cast(bf16x8, __builtin_amdgcn_raw_buffer_load_b128(srdW, obase + t * 32768 + s * 1024, 0, 16)); };
#pragma unroll
        for (int s = 0; s < OW - 1; ++s) { oload(s, 0); oload(s, 1); SBAR(); }
        f32x4 c2[2][8];
#pragma unroll
        for (int t = 0; t < 2; ++t)
#pragma unroll
            for (int n = 0; n < 8; ++n) c2[t][n] = (f32x4){0.f, 0.f, 0.f, 0.f};
        const unsigned voffW0 = (lane >> 4) * 2048 + (((lane & 15) ^ ((lane >> 4) << 1)) << 4), voffW1 = (4 + (lane >> 4)) * 2048 + (((lane & 15) ^ ((4 + (lane >> 4)) << 1)) << 4);
        {
            auto step = [&](int s) {
#pragma unroll
                for (int n = 0; n < 8; ++n) {
                    c2[0][n] = MFMA16(wf[s & 1][n], of[s % OW][0], c2[0][n]); c2[1][n] = MFMA16(wf[s & 1][n], of[s % OW][1], c2[1][n]);
                    SBAR();
                    if (s + 2 < 16) wread(s + 2, n);
                    if (n == PA0 + 2 * H && s + OW - 1 < 16) oload(s + OW - 1, 0);
                    if (n == PA0 + 4 + 2 * H && s + OW - 1 < 16) oload(s + OW - 1, 1);
                    if (s >= 4 && s < 8 && (n == PA0 + 2 - 2 * H || n == PA0 + 6 - 2 * H)) dma16(ldsb + ((s + 16) % 10) * 16384 + wid * 2048 + (n >= 4) * 1024, n >= 4 ? voffW1 : voffW0, srdWo, sW + (s + 8) * 65536);
                    SBAR();
                } };
#pragma unroll
            for (int s = 0; s < 4; ++s) { step(s); if (s == 0) STAMP(11); }
            WAITL0(); BAR();
#pragma unroll
            for (int s = 4; s < 10; ++s) step(s);
            STAMP(13);
            waitv<4>(); BAR();
            STAMP(14);
#pragma unroll
            for (int s = 10; s < 15; ++s) step(s);
#pragma unroll
            for (int n = 4 - 4 * H; n < 8 - 4 * H; ++n) { c2[0][n] = MFMA16(wf[1][n], of[15 % OW][0], c2[0][n]); c2[1][n] = MFMA16(wf[1][n], of[15 % OW][1], c2[1][n]); }
            SBAR();
#pragma unroll
            for (int n = 0; n < 4; ++n) {
                c2[0][4 * H + n] = MFMA16(wf[1][4 * H + n], of[15 % OW][0], c2[0][4 * H + n]); c2[1][4 * H + n] = MFMA16(wf[1][4 * H + n], of[15 % OW][1], c2[1][4 * H + n]);
                SBAR();
#pragma unroll
                for (int t = 0; t < 2; ++t) *(LAS f32x4*)(lds + 65536 + wid * 8192 + (t * 4 + n) * 1024 + lane * 16) = c2[t][4 - 4 * H + n];
                SBAR();
            }
            STAMP(9);
            WAITL0(); BAR();
            STAMP(15);
            GAS u16* outp = (GAS u16*)outb + (size_t)(rb * 256 + th * 128 + w4 * 32 + r16) * 2048 + br * 1024 + ct * 128 + H * 64 + (g4 & 1) * 16 + (g4 >> 1) * 8;
#pragma unroll
            for (int t = 0; t < 2; ++t)
#pragma unroll
                for (int n = 0; n < 4; n += 2) {
                    const f32x4 x = c2[t][4 * H + n] + *(LAS const f32x4*)(lds + 65536 + (wid ^ 4) * 8192 + (t * 4 + n) * 1024 + lane * 16);
                    const f32x4 y = c2[t][4 * H + n + 1] + *(LAS const f32x4*)(lds + 65536 + (wid ^ 4) * 8192 + (t * 4 + n + 1) * 1024 + lane * 16);
                    const auto u0 = __builtin_amdgcn_permlane16_swap(cvtpk(x[0], x[1]), cvtpk(y[0], y[1]), false, false);
                    const auto u1 = __builtin_amdgcn_permlane16_swap(cvtpk(x[2], x[3]), cvtpk(y[2], y[3]), false, false);
                    { const u32x4 vv = {u0[0], u1[0], u0[1], u1[1]}; GAS u16* pp = outp + (size_t)t * 16 * 2048 + n * 16; asm volatile("global_store_dwordx4 %0, %1, off sc0 sc1\n\ts_nop 1" :: "v"(pp), "v"(vv) : "memory"); }
                }
        }
    };
    if (half == 0) phase3(std::integral_constant<int, 0>{}); else phase3(std::integral_constant<int, 1>{});
    STAMP(10);
    { const u64 d = (mtb - mta) / MDIV, t0 = __builtin_amdgcn_s_memrealtime(); if (d < 10000 && mta != 0) while (__builtin_amdgcn_s_memrealtime() - t0 < 4 * d) __builtin_amdgcn_s_sleep(2); }
}

extern "C" void kernel_launch(void* const* d_in, const int* in_sizes, int n_in, void* d_out, int out_size, void* d_ws, size_t ws_size, hipStream_t stream) {
    Args a;
    a.x[0] = (const u16*)d_in[0]; a.w[0] = (const u16*)d_in[1]; a.wo[0] = (const u16*)d_in[2];
    a.x[1] = (const u16*)d_in[3]; a.w[1] = (const u16*)d_in[4]; a.wo[1] = (const u16*)d_in[5];
    a.out = (u16*)d_out; a.ws = (char*)d_ws;
    fused_kernel<<<256, 512, 0, stream>>>(a);
}
```

```cpp
#include <hip/hip_runtime.h>
#include <hip/hip_bf16.h>
#include <type_traits>
typedef short bf16x8 __attribute__((ext_vector_type(8)));
typedef short s16x4 __attribute__((ext_vector_type(4)));
typedef float f32x16 __attribute__((ext_vector_type(16)));
typedef float f32x4 __attribute__((ext_vector_type(4)));
typedef float f32x2 __attribute__((ext_vector_type(2)));
typedef __bf16 bf16x2 __attribute__((ext_vector_type(2)));
typedef unsigned u32x4 __attribute__((ext_vector_type(4)));
typedef double f64x8 __attribute__((ext_vector_type(8)));
typedef double f64x2 __attribute__((ext_vector_type(2)));
typedef unsigned short u16;
typedef unsigned long long u64;
#define LAS __attribute__((address_space(3)))
#define GAS __attribute__((address_space(1)))
#define DI __device__ __forceinline__
#define MFMA16(a, b, c) __builtin_amdgcn_mfma_f32_16x16x32_bf16((a), (b), (c), 0, 0, 0)
#define MFMA32(a, b, c) __builtin_amdgcn_mfma_f32_32x32x16_bf16((a), (b), (c), 0, 0, 0)
#define SBAR() __builtin_amdgcn_sched_barrier(0)
#define BAR() do { SBAR(); asm volatile("s_barrier" ::: "memory"); SBAR(); } while (0)
#define PIN(x) asm volatile("" : "+v"(x))
#define WAITL0() __builtin_amdgcn_s_waitcnt(0xC07F)
template <int N> DI void waitv() { asm volatile("s_waitcnt vmcnt(%0)" :: "i"(N) : "memory"); }
DI unsigned rfl(unsigned x) { return __builtin_amdgcn_readfirstlane(x); }
DI int lane_id() { int l; asm volatile("v_mbcnt_lo_u32_b32 %0, -1, 0\n\tv_mbcnt_hi_u32_b32 %0, -1, %0" : "=v"(l)); return l; }
DI unsigned cvtpk(float lo, float hi) { f32x2 v = {lo, hi}; bf16x2 b = __builtin_convertvector(v, bf16x2); return __builtin_bit_cast(unsigned, b); }
DI void dma16(unsigned m0v, unsigned voff, __amdgpu_buffer_rsrc_t srd, unsigned soff) {
    asm volatile("s_mov_b32 m0, %0\n\ts_nop 0\n\tbuffer_load_dwordx4 %1, %2, %3 offen lds" :: "s"(m0v), "v"(voff), "s"(srd), "s"(soff) : "memory");
}
DI void dma4(unsigned m0v, unsigned voff, __amdgpu_buffer_rsrc_t srd, unsigned soff) {
    asm volatile("s_mov_b32 m0, %0\n\ts_nop 0\n\tbuffer_load_dword %1, %2, %3 offen lds" :: "s"(m0v), "v"(voff), "s"(srd), "s"(soff) : "memory");
}
DI s16x4 trread(LAS const char* p) { return __builtin_amdgcn_ds_read_tr16_b64_v4i16((LAS s16x4*)p); }
DI bf16x8 ldsfrag(LAS const char* p) { return *(LAS const bf16x8*)p; }
DI float max3f(float a, float b, float c) { return __builtin_fmaxf(__builtin_fmaxf(a, b), c); }
DI float fadd_s(float a, float b) { return a + b; }
DI float fsub_s(float a, float b) { return a - b; }
DI float fmul_s(float a, float b) { return a * b; }
DI bf16x8 pack8(float a, float b, float c, float d, float e, float f, float g, float h) { u32x4 v = {cvtpk(a, b), cvtpk(c, d), cvtpk(e, f), cvtpk(g, h)}; return __builtin_bit_cast(bf16x8, v); }

constexpr int LDS_A0 = 0, LDS_A1 = 32768, LDS_B = 81920, A_SLOT = 16384, B_SLOT = 24576;
constexpr int LDS_TOTAL = 163840;
constexpr size_t WS_FKV = 0, WS_EKV = 32768, WS_FO = 65536, WS_EO = 98304, WS_KV = 1 << 20, WS_O = 17 << 20, WS_END = 33 << 20, WS_ST = 40 << 20;
constexpr u64 TAGK = 0x9E3779B97F4A7C15ull;
constexpr int SPIN_CAP = 400000;
constexpr float QSCALE = 0.17677669529663687f * 1.4426950408889634f;

#define PUBJ 3
#define POLJ 1
#define SPJ 4
#define OW 5
#define PA0 1
#define WPRE 4
#define LSAFE 1.152921504606847e18f
#define KROT 0
#define CODEPF 48
#define TOUCH 1
#define PD 5
constexpr int LDS_SCR = 155648;
#define PT3W 0
#define PT3X 0
#define KVT 0
#define MAP84 0
#define PGT 0
#define NEARLY 7
#define OPAD 0
#define ALLPOLL 1
constexpr unsigned OFS = OPAD ? 2048 : 1024;
#define CROT 0
#define WOT 0
struct Args { const u16* x[2]; const u16* w[2]; const u16* wo[2]; u16* out; char* ws; };

#define STAMP(i) do {} while (0)

__global__ __launch_bounds__(512, 2) void fused_kernel(Args a) {
    __shared__ __attribute__((aligned(16))) char smem[LDS_TOTAL];
    LAS char* lds = (LAS char*)smem;
    const unsigned ldsb = (unsigned)(size_t)smem;
    const int tid = threadIdx.x, lane0 = tid & 63;
    u64 pc0; asm volatile("s_getpc_b64 %0" : "=s"(pc0));
    const int wid = rfl(tid >> 6), half = wid >> 2, w4 = wid & 3;
    const int bid = blockIdx.x, g = bid & 7, j = bid >> 3;
    const int br = g >> 2, rb = MAP84 ? (j & 7) : ((g >> 1) & 1) * 4 + (j & 3), hp = MAP84 ? (g & 3) * 4 + (j >> 3) : (g & 1) * 8 + (j >> 2);
    const int me = (br * 8 + rb) * 16 + hp, grp = me & ~15;
    STAMP(0);
    char* wsp = a.ws; const u16* wop = a.wo[br]; u16* outb = a.out; const u16* xp = a.x[br]; const u16* wp = a.w[br];
    asm volatile("" : "+s"(wsp), "+s"(wop), "+s"(outb), "+s"(xp), "+s"(wp));
    const char* ea = wsp + WS_EKV + (size_t)(lane0 == 0 || rb == 0 ? me : me - 16) * 128;
    if (PGT) ea = lane0 == 2 ? wsp + WS_KV + (size_t)me * 65536 : lane0 == 3 ? wsp + WS_O + (size_t)(br * 128 + rb * 16) * 32768 : lane0 == 4 ? (const char*)outb + (size_t)rb * 256 * 4096
                : lane0 == 5 ? (const char*)wop + hp * 131072 : ea;
    u64 ew = __hip_atomic_load((GAS u64*)ea, __ATOMIC_RELAXED, __HIP_MEMORY_SCOPE_AGENT);
    if (CODEPF && wid == 7) {
        const __amdgpu_buffer_rsrc_t srdC = __builtin_amdgcn_make_buffer_rsrc((void*)pc0, (short)0, CODEPF * 1024, 0x00020000);
        if (lane0 < 13) dma4(ldsb + LDS_SCR, (j * 13 + lane0) * 128, srdC, 0);
    }
    const __amdgpu_buffer_rsrc_t srdA = __builtin_amdgcn_make_buffer_rsrc((void*)(xp + (size_t)rb * 256 * 1024), (short)0, 256 * 2048, 0x00020000);
    const __amdgpu_buffer_rsrc_t srdB = __builtin_amdgcn_make_buffer_rsrc((void*)wp, (short)0, 1024 * 6144, 0x00020000);
    const int wr = 2 * half + (w4 >> 1), wc = w4 & 1;
    const unsigned voffE = (lane0 >> 3) * 2048 + (((lane0 & 7) ^ (lane0 >> 4)) << 4);
    const unsigned voffO = (8 + (lane0 >> 3)) * 2048 + (((lane0 & 7) ^ (4 + (lane0 >> 4))) << 4);
    const unsigned voffB0 = (lane0 >> 3) * 6144 + (((lane0 & 7) ^ (((lane0 >> 4) & 1) << 1)) << 4);
    const unsigned voffB1 = (lane0 >> 3) * 6144 + (((lane0 & 7) ^ ((((lane0 >> 4) & 1) << 1) | 4)) << 4);
    const int gt0 = (w4 & 1) ? 7 - (w4 >> 1) : (w4 >> 1);
    const int gt1 = (w4 & 1) ? 5 - (w4 >> 1) : 2 + (w4 >> 1);
    const unsigned sA0 = gt0 * 32 * 2048, sA1 = gt1 * 32 * 2048;
    const unsigned sB = hp * 128 + (2 * w4) * 8 * 6144;
    const bool slowc = ((((size_t)wp >> 7) + hp) & 7) == 3;
    const int rot = (KROT * g + ((CROT && slowc) ? 4 * (rb & 3) : 0)) & 15;
    auto piece = [&](auto HC, int kt0, int s2, int s3i, int i) {
        constexpr int H = decltype(HC)::value;
        const int kt = (KROT || CROT) ? (kt0 + rot) & 15 : kt0;
        const unsigned soB = sB + kt * (64 * 6144), dB = ldsb + LDS_B + s3i * B_SLOT + w4 * 2048;
        const unsigned soA0 = sA0 + kt * 128, dA0 = ldsb + LDS_A0 + s2 * A_SLOT + w4 * 4096;
        const unsigned soA1 = sA1 + kt * 128, dA1 = ldsb + LDS_A1 + s3i * A_SLOT + w4 * 4096;
        if (H == 0) {
            if (i == 6) dma16(dA1 + 3 * 1024, voffO, srdA, soA1 + 32768);
            else dma16(dB + (i >> 1) * 8192 + (i & 1) * 1024, (i & 1) ? voffB1 : voffB0, srdB, soB + (i >> 1) * 2048 + (i & 1) * 8 * 6144);
        } else dma16((i < 4 ? dA0 : dA1) + (i & 3) * 1024, (i & 1) ? voffO : voffE, srdA, (i < 4 ? soA0 : soA1) + ((i >> 1) & 1) * 32768);
    };
    if (half == 0) {
#pragma unroll
        for (int i = 0; i < NEARLY; ++i) piece(std::integral_constant<int, 0>{}, i / 7, i / 7, i / 7, i % 7);
    } else {
#pragma unroll
        for (int i = 0; i < NEARLY; ++i) piece(std::integral_constant<int, 1>{}, i / 7, i / 7, i / 7, i % 7);
    }
    SBAR();
    const int lane = lane_id();
    const int r16 = lane & 15, g4 = lane >> 4, q4 = (lane & 15) >> 2, p4 = lane & 3;
    f32x4 acc[4][6];
    const int r32 = lane & 31, hh = lane >> 5;
    f32x16 mk;
#pragma unroll
    for (int r = 0; r < 16; ++r) mk[r] = ((r & 3) + 8 * (r >> 2) <= r32 - 4 * hh) ? 0.f : -INFINITY;
    {
        unsigned aoff[2], boff[2];
#pragma unroll
        for (int s = 0; s < 2; ++s) aoff[s] = (half ? LDS_A1 : LDS_A0) + ((w4 >> 1) * 64 + r16) * 128 + (((4 * s + g4) ^ ((r16 >> 1) & 7)) << 4);
#pragma unroll
        for (int c = 0; c < 2; ++c) boff[c] = LDS_B + (8 * g4 + q4) * 128 + (((wc * 2 + c) ^ ((q4 >> 1) | ((g4 & 1) << 1))) << 5) + p4 * 8;
#pragma unroll
        for (int i = 0; i < 4; ++i)
#pragma unroll
            for (int s = 0; s < 6; ++s) acc[i][s] = (f32x4){0.f, 0.f, 0.f, 0.f};
        bf16x8 af[4][2], bfr[6][2];
        const unsigned voffT = (16 * (rb & 3) + 4 * w4 + lane / 3) * 6144 + (lane % 3) * 2048 + hp * 128;
        auto run = [&](auto HC, auto TC) {
            constexpr int H = decltype(HC)::value;
            constexpr int TW = decltype(TC)::value;
            auto touch = [&](int kt) { if (lane < 12) dma4(ldsb + LDS_SCR, voffT, srdB, (KROT ? (kt + rot) & 15 : kt) * (64 * 6144)); };
#pragma unroll
            for (int i = 0; i < 4; ++i) asm volatile("" : "+v"(acc[i][0]), "+v"(acc[i][1]), "+v"(acc[i][2]), "+v"(acc[i][3]), "+v"(acc[i][4]), "+v"(acc[i][5]));
            PIN(mk); PIN(aoff[0]); PIN(aoff[1]); PIN(boff[0]); PIN(boff[1]);
            SBAR();
#pragma unroll
            for (int i = NEARLY; i < 14; ++i) piece(HC, i / 7, i / 7, i / 7, i % 7);
            if (H == 0) waitv<7>(); else waitv<10>();
            BAR();
            if (TW) {
#pragma unroll
                for (int kt = 3; kt < PD; ++kt) touch(kt);
            }
            if (H == 1) { waitv<7>(); BAR(); }
            STAMP(1);
            int s3 = 0;
            auto Lseg = [&](int t, bool issue) {
                const unsigned aslot = H ? s3 * A_SLOT : (t & 1) * A_SLOT;
                const unsigned bslot = s3 * B_SLOT;
                const int s3n = (s3 == 0) ? 2 : s3 - 1;
                auto rdA = [&](int i) { LAS const char* pa = lds + aoff[i >> 1] + aslot + (i & 1) * 4096; af[(i & 1) * 2][i >> 1] = ldsfrag(pa); af[(i & 1) * 2 + 1][i >> 1] = ldsfrag(pa + 2048); };
                auto rdB = [&](int i) { const int s = i / 6, seg = (i % 6) >> 1, c = i & 1; LAS const char* pb = lds + boff[c] + bslot;
                    s16x4 lo = trread(pb + seg * 8192 + s * 4096), hi = trread(pb + seg * 8192 + s * 4096 + 512);
                    bfr[seg * 2 + c][s] = __builtin_shufflevector(lo, hi, 0, 1, 2, 3, 4, 5, 6, 7); };
                if (TW && TOUCH == 2 && issue) touch(t + PD);
#pragma unroll
                for (int i = 0; i < 16; ++i) {
                    if (issue && (i & 1) == 0 && i / 2 < 7) piece(HC, t + 2, t & 1, s3n, i / 2);
                    if (TW && TOUCH == 3 && issue && i == 13) touch(t + PD);
                    if (i < 4) rdA(i); else rdB(i - 4);
                }
                if (TW && TOUCH == 1 && issue) touch(t + PD);
                s3 = (s3 == 2) ? 0 : s3 + 1;
            };
            auto Mseg = [&]() {
                __builtin_amdgcn_s_setprio(1);
#pragma unroll
                for (int s = 0; s < 2; ++s)
#pragma unroll
                    for (int m = 0; m < 4; ++m)
#pragma unroll
                        for (int n = 0; n < 6; ++n) acc[m][n] = n < 4 ? MFMA16(bfr[n][s], af[m][s], acc[m][n]) : MFMA16(af[m][s], bfr[n][s], acc[m][n]);
                __builtin_amdgcn_s_setprio(0);
            };
            if (TW && TOUCH != 2) { Lseg(0, true); WAITL0(); BAR(); Mseg(); waitv<8 + PD - 3>(); BAR(); }
            for (int t = (TW && TOUCH != 2) ? 1 : 0; t < 14; ++t) {
                Lseg(t, true);
                if (H == 1) waitv<10>();
                WAITL0(); BAR();
                Mseg();
                waitv<7 + (TW ? (TOUCH == 2 ? 1 : 2) : 0)>();
                BAR();
            }
            Lseg(14, false); if (H == 1) waitv<3>(); WAITL0(); BAR(); Mseg(); waitv<0>(); BAR();
            Lseg(15, false); WAITL0(); BAR(); Mseg(); if (H == 0) BAR();
        };
        if (half == 1) run(std::integral_constant<int, 1>{}, std::integral_constant<int, 0>{});
        else if (TOUCH && slowc) run(std::integral_constant<int, 0>{}, std::integral_constant<int, 1>{});
        else run(std::integral_constant<int, 0>{}, std::integral_constant<int, 0>{});
    }
    STAMP(2);
    const u64 tagp = (((u64)__builtin_amdgcn_readlane((unsigned)(ew >> 32), 0) << 32) | __builtin_amdgcn_readlane((unsigned)ew, 0)) + TAGK;
    const u64 tagc = (((u64)__builtin_amdgcn_readlane((unsigned)(ew >> 32), 1) << 32) | __builtin_amdgcn_readlane((unsigned)ew, 1)) + TAGK;
    const __amdgpu_buffer_rsrc_t srdW = __builtin_amdgcn_make_buffer_rsrc((void*)wsp, (short)0, (int)WS_END, 0x00020000);
    const __amdgpu_buffer_rsrc_t srdWo = __builtin_amdgcn_make_buffer_rsrc((void*)(wop), (short)0, 1024 * 2048, 0x00020000);
    const int ct = hp & 7, th = hp >> 3;
    const unsigned sW = ct * 256 + (half * 512 + w4 * 8) * 2048;
    auto wo_steps = [&](int s0, int s1) {
        const int ln = lane_id();
        const unsigned voffW0 = (ln >> 4) * 2048 + (((ln & 15) ^ ((ln >> 4) << 1)) << 4), voffW1 = (4 + (ln >> 4)) * 2048 + (((ln & 15) ^ ((4 + (ln >> 4)) << 1)) << 4);
#pragma unroll
        for (int s = s0; s < s1; ++s) { const unsigned d = ldsb + ((s + 8) % 10) * 16384 + wid * 2048; dma16(d, voffW0, srdWo, sW + s * 65536); dma16(d + 1024, voffW1, srdWo, sW + s * 65536); } };
    bf16x8 qf[2][2];
    {
        const unsigned kpos = (g4 >> 1) * 1024 + (g4 & 1) * 512 + r16 * 16;
        const unsigned vpos = 2048 + ((g4 & 1) * 32 + (g4 >> 1) * 16 + r16) * 16;
        const unsigned gkv = (unsigned)WS_KV + me * 65536 + wc * 32768;
        u32x4 kv[2][2], vv[2][2];
#pragma unroll
        for (int mi = 0; mi < 2; ++mi)
#pragma unroll
            for (int s = 0; s < 2; ++s) {
                const f32x4 k0 = acc[2 * mi + s][2], k1 = acc[2 * mi + s][3], v0 = acc[2 * mi + s][4], v1 = acc[2 * mi + s][5];
                kv[mi][s] = (u32x4){cvtpk(k0[0], k0[1]), cvtpk(k0[2], k0[3]), cvtpk(k1[0], k1[1]), cvtpk(k1[2], k1[3])};
                const auto ra = __builtin_amdgcn_permlane32_swap(cvtpk(v0[0], v0[1]), cvtpk(v1[0], v1[1]), false, false);
                const auto rbb = __builtin_amdgcn_permlane32_swap(cvtpk(v0[2], v0[3]), cvtpk(v1[2], v1[3]), false, false);
                vv[mi][s] = (u32x4){ra[0], rbb[0], ra[1], rbb[1]};
                const unsigned blk = wc * 65536 + 32768 + (mi ? 7 - wr : wr) * 4096;
                *(LAS u32x4*)(lds + blk + kpos + s * 256) = kv[mi][s];
                *(LAS u32x4*)(lds + blk + vpos + s * 1024) = vv[mi][s];
            }
        if (rb < 7) {
#pragma unroll
            for (int mi = 0; mi < 2; ++mi)
#pragma unroll
                for (int s = 0; s < 2; ++s) { const unsigned blk = (mi ? 7 - wr : wr) * 4096;
                    __builtin_amdgcn_raw_buffer_store_b128(kv[mi][s], srdW, gkv + blk + kpos + s * 256, 0, 16);
                    __builtin_amdgcn_raw_buffer_store_b128(vv[mi][s], srdW, gkv + blk + vpos + s * 1024, 0, 16); }
        }
#pragma unroll
        for (int mi = 0; mi < 2; ++mi) {
            u32x4 x[2], y0, y1;
#pragma unroll
            for (int s = 0; s < 2; ++s) { const f32x4 q0 = acc[2 * mi + s][0] * QSCALE, q1 = acc[2 * mi + s][1] * QSCALE;
                x[s] = (u32x4){cvtpk(q0[0], q0[1]), cvtpk(q0[2], q0[3]), cvtpk(q1[0], q1[1]), cvtpk(q1[2], q1[3])}; }
#pragma unroll
            for (int i = 0; i < 4; ++i) { const auto u = __builtin_amdgcn_permlane16_swap(x[0][i], x[1][i], false, false);
                const auto v = __builtin_amdgcn_permlane32_swap(u[0], u[1], false, false); y0[i] = v[0]; y1[i] = v[1]; }
            qf[mi][0] = __builtin_bit_cast(bf16x8, y0); qf[mi][1] = __builtin_bit_cast(bf16x8, y1);
        }
    }
    f32x16 o[2], nm[2]; f32x4 l[2];
    LAS const char* kvb = lds + wc * 65536 + lane * 16;
    bf16x8 ones;
    { const short v = ((lane ^ (lane >> 4)) & 1) ? (short)0 : (short)0x3f80;
#pragma unroll
      for (int i = 0; i < 8; ++i) ones[i] = v; }
    auto jmax = [&](const f32x16& s, float m) {
#pragma unroll
        for (int r = 0; r < 16; r += 2) m = max3f(m, s[r], s[r + 1]);
        return m; };
    auto xmax = [&](float m) { const auto r = __builtin_amdgcn_permlane32_swap(__float_as_uint(m), __float_as_uint(m), false, false); return __builtin_fmaxf(__uint_as_float(r[0]), __uint_as_float(r[1])); };
    auto expo = [&](const f32x16& s, bf16x8& p0, bf16x8& p1) {
        float t[16];
#pragma unroll
        for (int r = 0; r < 16; ++r) t[r] = __builtin_amdgcn_exp2f(s[r]);
        p0 = pack8(t[0], t[1], t[2], t[3], t[4], t[5], t[6], t[7]); p1 = pack8(t[8], t[9], t[10], t[11], t[12], t[13], t[14], t[15]); };
    auto flip = [&](const f32x16& m) { f32x16 c;
#pragma unroll
        for (int r = 0; r < 16; ++r) c[r] = __uint_as_float(__float_as_uint(m[r]) ^ 0xff800000u);
        return c; };
    {
        f32x16 zero16, s[2];
#pragma unroll
        for (int r = 0; r < 16; ++r) zero16[r] = 0.f;
#pragma unroll
        for (int d = 0; d < 2; ++d) {
            LAS const char* p = kvb + (8 + (d ? 7 - wr : wr)) * 4096;
            s[d] = MFMA32(ldsfrag(p), qf[d][0], mk); s[d] = MFMA32(ldsfrag(p + 1024), qf[d][1], s[d]);
        }
        wo_steps(0, 2);
        const float m0 = xmax(jmax(s[0], -INFINITY)), m1 = xmax(jmax(s[1], -INFINITY));
#pragma unroll
        for (int d = 0; d < 2; ++d) {
            LAS const char* p = kvb + (8 + (d ? 7 - wr : wr)) * 4096;
            const float m = d ? m1 : m0;
#pragma unroll
            for (int r = 0; r < 16; ++r) { nm[d][r] = -m; s[d][r] = fsub_s(s[d][r], m); }
            bf16x8 p0, p1;
            expo(s[d], p0, p1);
            o[d] = MFMA32(ldsfrag(p + 2048), p0, zero16); l[d] = MFMA16(ones, p0, ((f32x4){0.f, 0.f, 0.f, 0.f}));
            o[d] = MFMA32(ldsfrag(p + 3072), p1, o[d]); l[d] = MFMA16(ones, p1, l[d]);
        }
    }
    mk = flip(mk); PIN(mk);
    waitv<4>(); WAITL0(); BAR();
    if (wid == 0 && lane == 0 && rb < 7) __hip_atomic_store((GAS u64*)(wsp + WS_FKV + (size_t)me * 128), tagp, __ATOMIC_RELAXED, __HIP_MEMORY_SCOPE_AGENT);
    if (WOT && lane < 56) { const int e = ((rb & 3) * 8 + wid) * 56 + lane, row = e >> 1;
        dma4(ldsb + half * 65536 + w4 * 8192, (64 + row + (row >= 448 ? 64 : 0)) * 2048 + ct * 256 + (e & 1) * 128, srdWo, 0); }
    STAMP(3);
    auto attention = [&](auto WC_) {
        constexpr int WR = decltype(WC_)::value;
        u32x4 pld[8];
        auto jobs = [&](auto PART) {
            constexpr int P2 = decltype(PART)::value;
            constexpr int NJ = P2 ? 9 : 7;
            auto TOF = [](int j) { return P2 ? (j < 7 ? (j < 7 - WR ? 0 : 1) : j - 7) : (j < WR ? 0 : 1); };
            auto BOF = [](int j) { return P2 ? (j < 7 ? (j < 7 - WR ? WR + 1 + j : j + 1) : (j == 7 ? WR : 7 - WR)) : 8 + (j < WR ? j : j - WR); };
            f32x16 S[2], cm; bf16x8 kf[2][2], vf[2][2]; u32x4 p0, p1;
            auto rdk = [&](int j) { kf[j & 1][0] = ldsfrag(kvb + BOF(j) * 4096); kf[j & 1][1] = ldsfrag(kvb + BOF(j) * 4096 + 1024); };
            auto cof = [&](int j) -> const f32x16& { return (P2 && j >= 7) ? cm : nm[TOF(j)]; };
            auto mkc = [&](int j) { const f32x16& mi = mk;
#pragma unroll
                for (int r = 0; r < 16; ++r) cm[r] = fadd_s(mi[r], nm[TOF(j)][r]); };
            rdk(0); rdk(1); vf[0][0] = ldsfrag(kvb + BOF(0) * 4096 + 2048);
            S[0] = MFMA32(kf[0][0], qf[TOF(0)][0], cof(0)); S[0] = MFMA32(kf[0][1], qf[TOF(0)][1], S[0]);
            SBAR();
#pragma unroll
            for (int j = 0; j < NJ; ++j) {
                const int T = TOF(j), TP = j > 0 ? TOF(j - 1) : 0, TN = j + 1 < NJ ? TOF(j + 1) : 0;
                f32x16& s = S[j & 1];
#define EX(i) s[i] = __builtin_amdgcn_exp2f(s[i])
                if (P2 && j + 1 >= 7 && j + 1 < NJ) { mkc(j + 1); PIN(cm); }
                if (j + 1 < NJ) S[(j + 1) & 1] = MFMA32(kf[(j + 1) & 1][0], qf[TN][0], cof(j + 1));
                SBAR();
                vf[j & 1][1] = ldsfrag(kvb + BOF(j) * 4096 + 3072);
                EX(0); EX(1); EX(2); PIN(s); SBAR();
                if (j > 0) o[TP] = MFMA32(vf[(j - 1) & 1][1], __builtin_bit_cast(bf16x8, p1), o[TP]);
                SBAR();
                EX(3); EX(4); EX(5); PIN(s); SBAR();
                if (j > 0) l[TP] = MFMA16(ones, __builtin_bit_cast(bf16x8, p1), l[TP]);
                SBAR();
                EX(6); EX(7);
                p0 = (u32x4){cvtpk(s[0], s[1]), cvtpk(s[2], s[3]), cvtpk(s[4], s[5]), cvtpk(s[6], s[7])}; PIN(p0); SBAR();
                if (j + 1 < NJ) S[(j + 1) & 1] = MFMA32(kf[(j + 1) & 1][1], qf[TN][1], S[(j + 1) & 1]);
                SBAR();
                if (j + 1 < NJ) vf[(j + 1) & 1][0] = ldsfrag(kvb + BOF(j + 1) * 4096 + 2048);
                EX(8); EX(9); EX(10); PIN(s); SBAR();
                o[T] = MFMA32(vf[j & 1][0], __builtin_bit_cast(bf16x8, p0), o[T]);
                SBAR();
                if (j + 2 < NJ) rdk(j + 2);
                EX(11); EX(12); EX(13); PIN(s); SBAR();
                l[T] = MFMA16(ones, __builtin_bit_cast(bf16x8, p0), l[T]);
                SBAR();
                EX(14); EX(15);
                p1 = (u32x4){cvtpk(s[8], s[9]), cvtpk(s[10], s[11]), cvtpk(s[12], s[13]), cvtpk(s[14], s[15])}; PIN(p1); SBAR();
#undef EX
                if (j == NJ - 1) { o[T] = MFMA32(vf[j & 1][1], __builtin_bit_cast(bf16x8, p1), o[T]); l[T] = MFMA16(ones, __builtin_bit_cast(bf16x8, p1), l[T]); }
                if (!P2 && j == POLJ && rb > 0) {
                    if (WR == 0 && wid == 0) {
                        GAS u64* f = (GAS u64*)(wsp + WS_FKV + (size_t)(me - 16) * 128);
#pragma nounroll
                        for (int spin = 0; spin < SPIN_CAP; ++spin) {
                            const u64 v = __hip_atomic_load(f, __ATOMIC_RELAXED, __HIP_MEMORY_SCOPE_AGENT);
                            if (rfl((unsigned)v) == (unsigned)tagc && rfl((unsigned)(v >> 32)) == (unsigned)(tagc >> 32)) break;
                            __builtin_amdgcn_s_sleep(1);
                        }
                        if (lane == 0) __hip_atomic_store((GAS u64*)(wsp + WS_EKV + (size_t)(me - 16) * 128), tagc, __ATOMIC_RELAXED, __HIP_MEMORY_SCOPE_AGENT);
                    }
                    BAR();
                    STAMP(5);
#pragma unroll
                    for (int k = 0; k < 8; ++k) pld[k] = __builtin_amdgcn_raw_buffer_load_b128(srdW, (unsigned)WS_KV + (me - 16) * 65536 + wid * 8192 + k * 1024 + lane * 16, 0, 16);
                    SBAR();
                }
            }
        };
        jobs(std::integral_constant<int, 0>{});
        STAMP(4);
        if (rb > 0) {
#pragma unroll
            for (int i = 0; i < 8; ++i) *(LAS u32x4*)(lds + half * 65536 + w4 * 8192 + i * 1024 + lane * 16) = pld[i];
            WAITL0(); BAR();
            STAMP(6);
            jobs(std::integral_constant<int, 1>{});
        }
    };
#define ATT(W) attention(std::integral_constant<int, W>{})
    if (wr == 0) ATT(0); else if (wr == 1) ATT(1); else if (wr == 2) ATT(2); else ATT(3);
#pragma unroll
    for (int d = 0; d < 2; ++d) {
        const int gt = d ? 7 - wr : wr;
        float lt = (lane & 16) ? l[d][1] : l[d][0];
        if (__builtin_expect(__any(!(lt <= LSAFE)), 0)) {
            const f32x16 mhi = mk, mlo = flip(mk);
            const int b0 = rb > 0 ? gt : 8, b1 = 8 + gt;
            auto sc = [&](int b, float ref) { f32x16 c;
#pragma unroll
                for (int r = 0; r < 16; ++r) c[r] = (b == b1 ? mlo[r] : b == gt ? mhi[r] : 0.f) - ref;
                c = MFMA32(ldsfrag(kvb + b * 4096), qf[d][0], c); return MFMA32(ldsfrag(kvb + b * 4096 + 1024), qf[d][1], c); };
            float m = -INFINITY;
#pragma nounroll
            for (int b = b0; b <= b1; ++b) m = jmax(sc(b, 0.f), m);
            m = xmax(m);
            f32x4 ls = {0.f, 0.f, 0.f, 0.f};
#pragma unroll
            for (int r = 0; r < 16; ++r) o[d][r] = 0.f;
#pragma nounroll
            for (int b = b0; b <= b1; ++b) { bf16x8 p0, p1; expo(sc(b, m), p0, p1);
                o[d] = MFMA32(ldsfrag(kvb + b * 4096 + 2048), p0, o[d]); o[d] = MFMA32(ldsfrag(kvb + b * 4096 + 3072), p1, o[d]);
                ls = MFMA16(ones, p0, ls); ls = MFMA16(ones, p1, ls); }
            lt = (lane & 16) ? ls[1] : ls[0];
        }
        const float inv = __builtin_amdgcn_rcpf(lt);
        const unsigned base = (unsigned)WS_O + (((br * 128 + rb * 16 + gt * 2 + (r32 >> 4)) * 32) + hp * 2 + wc) * OFS + hh * 256 + (r32 & 15) * 16;
#pragma unroll
        for (int e = 0; e < 2; ++e) {
            const f32x16& x = o[d];
            const u32x4 v = {cvtpk(fmul_s(x[8 * e], inv), fmul_s(x[8 * e + 1], inv)), cvtpk(fmul_s(x[8 * e + 2], inv), fmul_s(x[8 * e + 3], inv)), cvtpk(fmul_s(x[8 * e + 4], inv), fmul_s(x[8 * e + 5], inv)), cvtpk(fmul_s(x[8 * e + 6], inv), fmul_s(x[8 * e + 7], inv))};
            __builtin_amdgcn_raw_buffer_store_b128(v, srdW, base + e * 512 + ((OPAD && (e || (lane & 40) == 40)) ? 128 : 0), 0, 16);
        }
    }
    STAMP(7);
    auto phase3 = [&](auto HC) {
        constexpr int H = decltype(HC)::value;
        const int lane = lane_id(), r16 = lane & 15, g4 = lane >> 4, q4 = (lane & 15) >> 2, p4 = lane & 3;
        GAS char* ep = (GAS char*)wsp + WS_EO + (lane < 16 ? (size_t)(grp + lane) * 128 + hp * 8 : (size_t)me * 128 + (lane & 15) * 8);
        u64 eo;
        asm volatile("global_load_dwordx2 %0, %1, off sc1" : "=v"(eo) : "v"(ep) : "memory");
        unsigned woff[8];
        { const int row = 16 * (g4 >> 1) + 4 * (g4 & 1) + q4;
#pragma unroll
          for (int n = 0; n < 8; ++n) woff[n] = 131072 + H * 8192 + row * 256 + ((n ^ (row & 7)) << 5) + p4 * 8; }
        bf16x8 wf[2][8];
        auto wread = [&](int s, int n) {
            if (s == 2 || s == 12) { woff[n] -= 131072; PIN(woff[n]); } else if (s == 6 || s == 10) { woff[n] += 65536; PIN(woff[n]); }
            LAS const char* p = lds + woff[n] + (((s + 8) % 10) & 3) * 16384;
            const s16x4 lo = trread(p), hi = trread(p + 2048); wf[s & 1][n] = __builtin_shufflevector(lo, hi, 0, 1, 2, 3, 4, 5, 6, 7); };
        waitv<5>(); WAITL0(); BAR();
        wo_steps(2, 2 + WPRE);
#pragma unroll
        for (int n = 0; n < 8; ++n) { wread(0, n); wread(1, n); }
        waitv<2 * WPRE>(); PIN(eo); WAITL0(); BAR();
        if (H == 0 && wid == 0) {
            const u64 tag = eo + TAGK;
            if (lane < 16) __hip_atomic_store((GAS u64*)(ep - WS_EO + WS_FO), tag, __ATOMIC_RELAXED, __HIP_MEMORY_SCOPE_AGENT);
        }
        wo_steps(2 + WPRE, 12);
        if (ALLPOLL) { waitv<0>(); BAR(); }
        if (ALLPOLL || (H == 0 && wid == 0)) {
            const u64 tag = eo + TAGK;
            GAS u64* f = (GAS u64*)(wsp + WS_FO + (size_t)me * 128 + (lane & 15) * 8);
            const bool mine = lane >= 16 && lane < 32;
#pragma nounroll
            for (int spin = 0; spin < SPIN_CAP; ++spin) {
                const u64 v = __hip_atomic_load(f, __ATOMIC_RELAXED, __HIP_MEMORY_SCOPE_AGENT);
                if (__all(!mine || v == tag)) break;
                __builtin_amdgcn_s_sleep(1);
            }
            if (H == 0 && wid == 0 && mine) __hip_atomic_store((GAS u64*)ep, tag, __ATOMIC_RELAXED, __HIP_MEMORY_SCOPE_AGENT);
        }
        if (!ALLPOLL) { waitv<0>(); BAR(); }
        STAMP(8);
        bf16x8 of[OW][2];
        const unsigned obase = (unsigned)WS_O + ((br * 128 + rb * 16 + th * 8 + w4 * 2) * 32 + H * 16) * OFS + lane * 16 + ((OPAD && lane >= 24) ? 128 : 0);
        auto oload = [&](int s, int t) { of[s % OW][t] = __builtin_bit_cast(bf16x8, __builtin_amdgcn_raw_buffer_load_b128(srdW, obase + t * 32 * OFS + s * OFS, 0, 16)); };
#pragma unroll
        for (int s = 0; s < OW - 1; ++s) { oload(s, 0); oload(s, 1); SBAR(); }
        f32x4 c2[2][8];
#pragma unroll
        for (int t = 0; t < 2; ++t)
#pragma unroll
            for (int n = 0; n < 8; ++n) c2[t][n] = (f32x4){0.f, 0.f, 0.f, 0.f};
        const unsigned voffW0 = (lane >> 4) * 2048 + (((lane & 15) ^ ((lane >> 4) << 1)) << 4), voffW1 = (4 + (lane >> 4)) * 2048 + (((lane & 15) ^ ((4 + (lane >> 4)) << 1)) << 4);
        {
            auto step = [&](int s) {
#pragma unroll
                for (int n = 0; n < 8; ++n) {
                    c2[0][n] = MFMA16(wf[s & 1][n], of[s % OW][0], c2[0][n]); c2[1][n] = MFMA16(wf[s & 1][n], of[s % OW][1], c2[1][n]);
                    SBAR();
                    if (s + 2 < 16) wread(s + 2, n);
                    if (n == PA0 + 2 * H && s + OW - 1 < 16) oload(s + OW - 1, 0);
                    if (n == PA0 + 4 + 2 * H && s + OW - 1 < 16) oload(s + OW - 1, 1);
                    if (s >= 4 && s < 8 && (n == PA0 + 2 - 2 * H || n == PA0 + 6 - 2 * H)) dma16(ldsb + ((s + 16) % 10) * 16384 + wid * 2048 + (n >= 4) * 1024, n >= 4 ? voffW1 : voffW0, srdWo, sW + (s + 8) * 65536);
                    SBAR();
                } };
#pragma unroll
            for (int s = 0; s < 4; ++s) { step(s); if (s == 0) STAMP(11); }
            WAITL0(); BAR();
#pragma unroll
            for (int s = 4; s < 10; ++s) step(s);
            STAMP(13);
            waitv<4>(); BAR();
            STAMP(14);
#pragma unroll
            for (int s = 10; s < 15; ++s) step(s);
#pragma unroll
            for (int n = 4 - 4 * H; n < 8 - 4 * H; ++n) { c2[0][n] = MFMA16(wf[1][n], of[15 % OW][0], c2[0][n]); c2[1][n] = MFMA16(wf[1][n], of[15 % OW][1], c2[1][n]); }
            SBAR();
#pragma unroll
            for (int n = 0; n < 4; ++n) {
                c2[0][4 * H + n] = MFMA16(wf[1][4 * H + n], of[15 % OW][0], c2[0][4 * H + n]); c2[1][4 * H + n] = MFMA16(wf[1][4 * H + n], of[15 % OW][1], c2[1][4 * H + n]);
                SBAR();
#pragma unroll
                for (int t = 0; t < 2; ++t) *(LAS f32x4*)(lds + 65536 + wid * 8192 + (t * 4 + n) * 1024 + lane * 16) = c2[t][4 - 4 * H + n];
                SBAR();
            }
            STAMP(9);
            WAITL0(); BAR();
            STAMP(15);
            GAS u16* outp = (GAS u16*)outb + (size_t)(rb * 256 + th * 128 + w4 * 32 + r16) * 2048 + br * 1024 + ct * 128 + H * 64 + (g4 & 1) * 16 + (g4 >> 1) * 8;
#pragma unroll
            for (int t = 0; t < 2; ++t)
#pragma unroll
                for (int n = 0; n < 4; n += 2) {
                    const f32x4 x = c2[t][4 * H + n] + *(LAS const f32x4*)(lds + 65536 + (wid ^ 4) * 8192 + (t * 4 + n) * 1024 + lane * 16);
                    const f32x4 y = c2[t][4 * H + n + 1] + *(LAS const f32x4*)(lds + 65536 + (wid ^ 4) * 8192 + (t * 4 + n + 1) * 1024 + lane * 16);
                    const auto u0 = __builtin_amdgcn_permlane16_swap(cvtpk(x[0], x[1]), cvtpk(y[0], y[1]), false, false);
                    const auto u1 = __builtin_amdgcn_permlane16_swap(cvtpk(x[2], x[3]), cvtpk(y[2], y[3]), false, false);
                    { const u32x4 vv = {u0[0], u1[0], u0[1], u1[1]}; GAS u16* pp = outp + (size_t)t * 16 * 2048 + n * 16; asm volatile("global_store_dwordx4 %0, %1, off sc0 sc1\n\ts_nop 1" :: "v"(pp), "v"(vv) : "memory"); }
                }
        }
    };
    if (half == 0) phase3(std::integral_constant<int, 0>{}); else phase3(std::integral_constant<int, 1>{});
}

extern "C" void kernel_launch(void* const* d_in, const int* in_sizes, int n_in, void* d_out, int out_size, void* d_ws, size_t ws_size, hipStream_t stream) {
    Args a;
    a.x[0] = (const u16*)d_in[0]; a.w[0] = (const u16*)d_in[1]; a.wo[0] = (const u16*)d_in[2];
    a.x[1] = (const u16*)d_in[3]; a.w[1] = (const u16*)d_in[4]; a.wo[1] = (const u16*)d_in[5];
    a.out = (u16*)d_out; a.ws = (char*)(((size_t)d_ws + 4095) & ~(size_t)4095);
    fused_kernel<<<256, 512, 0, stream>>>(a);
}
```

```cpp
#include <hip/hip_runtime.h>
#include <hip/hip_bf16.h>
#include <type_traits>
typedef short bf16x8 __attribute__((ext_vector_type(8)));
typedef short s16x4 __attribute__((ext_vector_type(4)));
typedef float f32x16 __attribute__((ext_vector_type(16)));
typedef float f32x4 __attribute__((ext_vector_type(4)));
typedef float f32x2 __attribute__((ext_vector_type(2)));
typedef __bf16 bf16x2 __attribute__((ext_vector_type(2)));
typedef unsigned u32x4 __attribute__((ext_vector_type(4)));
typedef unsigned short u16;
typedef unsigned long long u64;
#define LAS __attribute__((address_space(3)))
#define GAS __attribute__((address_space(1)))
#define DI __device__ __forceinline__
#define MFMA16(a, b, c) __builtin_amdgcn_mfma_f32_16x16x32_bf16((a), (b), (c), 0, 0, 0)
#define MFMA32(a, b, c) __builtin_amdgcn_mfma_f32_32x32x16_bf16((a), (b), (c), 0, 0, 0)
#define SBAR() __builtin_amdgcn_sched_barrier(0)
#define BAR() do { SBAR(); asm volatile("s_barrier" ::: "memory"); SBAR(); } while (0)
#define PIN(x) asm volatile("" : "+v"(x))
#define WAITL0() __builtin_amdgcn_s_waitcnt(0xC07F)
template <int N> DI void waitv() { asm volatile("s_waitcnt vmcnt(%0)" :: "i"(N) : "memory"); }
DI unsigned rfl(unsigned x) { return __builtin_amdgcn_readfirstlane(x); }
DI int lane_id() { int l; asm volatile("v_mbcnt_lo_u32_b32 %0, -1, 0\n\tv_mbcnt_hi_u32_b32 %0, -1, %0" : "=v"(l)); return l; }
DI unsigned cvtpk(float lo, float hi) { f32x2 v = {lo, hi}; bf16x2 b = __builtin_convertvector(v, bf16x2); return __builtin_bit_cast(unsigned, b); }
DI void dma16(unsigned m0v, unsigned voff, __amdgpu_buffer_rsrc_t srd, unsigned soff) {
    asm volatile("s_mov_b32 m0, %0\n\ts_nop 0\n\tbuffer_load_dwordx4 %1, %2, %3 offen lds" :: "s"(m0v), "v"(voff), "s"(srd), "s"(soff) : "memory");
}
DI void dma4(unsigned m0v, unsigned voff, __amdgpu_buffer_rsrc_t srd, unsigned soff) {
    asm volatile("s_mov_b32 m0, %0\n\ts_nop 0\n\tbuffer_load_dword %1, %2, %3 offen lds" :: "s"(m0v), "v"(voff), "s"(srd), "s"(soff) : "memory");
}
DI void ld4(unsigned& d, unsigned voff, __amdgpu_buffer_rsrc_t srd, unsigned soff) {
    asm volatile("buffer_load_dword %0, %1, %2, %3 offen" : "+v"(d) : "v"(voff), "s"(srd), "s"(soff) : "memory");
}
DI s16x4 trread(LAS const char* p) { return __builtin_amdgcn_ds_read_tr16_b64_v4i16((LAS s16x4*)p); }
DI bf16x8 ldsfrag(LAS const char* p) { return *(LAS const bf16x8*)p; }
DI float max3f(float a, float b, float c) { return __builtin_fmaxf(__builtin_fmaxf(a, b), c); }
DI bf16x8 pack8(float a, float b, float c, float d, float e, float f, float g, float h) { u32x4 v = {cvtpk(a, b), cvtpk(c, d), cvtpk(e, f), cvtpk(g, h)}; return __builtin_bit_cast(bf16x8, v); }

constexpr int LDS_A0 = 0, LDS_A1 = 32768, LDS_B = 81920, A_SLOT = 16384, B_SLOT = 24576;
constexpr int LDS_TOTAL = 163840;
constexpr size_t WS_FKV = 0, WS_EKV = 32768, WS_FO = 65536, WS_EO = 98304, WS_KV = 1 << 20, WS_O = 17 << 20, WS_END = 25 << 20;
constexpr u64 TAGK = 0x9E3779B97F4A7C15ull;
constexpr int SPIN_CAP = 400000;
constexpr float QSCALE = 0.17677669529663687f * 1.4426950408889634f;

constexpr int LDS_SCR = 155648;
constexpr int POLJ = 1;
constexpr int OW = 4;
constexpr int PD = 4;
constexpr int CODE_KB = 48;
#define LSAFE 1.152921504606847e18f
struct Args { const u16* x[2]; const u16* w[2]; const u16* wo[2]; u16* out; char* ws; };

__global__ __launch_bounds__(512, 2) void concat_heads_sliding_block_kernel(Args a) {
    __shared__ __attribute__((aligned(16))) char smem[LDS_TOTAL];
    LAS char* lds = (LAS char*)smem;
    const unsigned ldsb = (unsigned)(size_t)smem;
    const int tid = threadIdx.x, lane0 = tid & 63;
    u64 pc0; asm volatile("s_getpc_b64 %0" : "=s"(pc0));
    const int wid = rfl(tid >> 6), half = wid >> 2, w4 = wid & 3;
    const int bid = blockIdx.x, g = bid & 7, j = bid >> 3;
    const int br = g >> 2, rb = ((g >> 1) & 1) * 4 + (j & 3), hp = (g & 1) * 8 + (j >> 2);
    const int me = (br * 8 + rb) * 16 + hp, grp = me & ~15;
    char* wsp = a.ws; const u16* wop = a.wo[br]; u16* outb = a.out; const u16* xp = a.x[br]; const u16* wp = a.w[br];
    asm volatile("" : "+s"(wsp), "+s"(wop), "+s"(outb), "+s"(xp), "+s"(wp));
    const u64 ew = __hip_atomic_load((GAS u64*)(wsp + WS_EKV + (size_t)(lane0 == 0 || rb == 0 ? me : me - 16) * 128), __ATOMIC_RELAXED, __HIP_MEMORY_SCOPE_AGENT);
    if (wid == 7) {
        const __amdgpu_buffer_rsrc_t srdC = __builtin_amdgcn_make_buffer_rsrc((void*)pc0, (short)0, CODE_KB * 1024, 0x00020000);
        if (lane0 < 13) dma4(ldsb + LDS_SCR, (j * 13 + lane0) * 128, srdC, 0);
    }
    const __amdgpu_buffer_rsrc_t srdA = __builtin_amdgcn_make_buffer_rsrc((void*)(xp + (size_t)rb * 256 * 1024), (short)0, 256 * 2048, 0x00020000);
    const __amdgpu_buffer_rsrc_t srdB = __builtin_amdgcn_make_buffer_rsrc((void*)wp, (short)0, 1024 * 6144, 0x00020000);
    const int wr = 2 * half + (w4 >> 1), wc = w4 & 1;
    const unsigned voffE = (lane0 >> 3) * 2048 + (((lane0 & 7) ^ (lane0 >> 4)) << 4);
    const unsigned voffO = (8 + (lane0 >> 3)) * 2048 + (((lane0 & 7) ^ (4 + (lane0 >> 4))) << 4);
    const unsigned voffB0 = (lane0 >> 3) * 6144 + (((lane0 & 7) ^ (((lane0 >> 4) & 1) << 1)) << 4);
    const unsigned voffB1 = (lane0 >> 3) * 6144 + (((lane0 & 7) ^ ((((lane0 >> 4) & 1) << 1) | 4)) << 4);
    const int gt0 = (w4 & 1) ? 7 - (w4 >> 1) : (w4 >> 1);
    const int gt1 = (w4 & 1) ? 5 - (w4 >> 1) : 2 + (w4 >> 1);
    const unsigned sA0 = gt0 * 32 * 2048, sA1 = gt1 * 32 * 2048;
    const unsigned sB = hp * 128 + (2 * w4) * 8 * 6144;
    const bool slowc = ((((size_t)wp >> 7) + hp) & 7) == 3;
    auto piece = [&](auto HC, int kt, int s2, int s3i, int i) {
        constexpr int H = decltype(HC)::value;
        const unsigned soB = sB + kt * (64 * 6144), dB = ldsb + LDS_B + s3i * B_SLOT + w4 * 2048;
        const unsigned soA0 = sA0 + kt * 128, dA0 = ldsb + LDS_A0 + s2 * A_SLOT + w4 * 4096;
        const unsigned soA1 = sA1 + kt * 128, dA1 = ldsb + LDS_A1 + s3i * A_SLOT + w4 * 4096;
        if (H == 0) {
            if (i == 6) dma16(dA1 + 3 * 1024, voffO, srdA, soA1 + 32768);
            else dma16(dB + (i >> 1) * 8192 + (i & 1) * 1024, (i & 1) ? voffB1 : voffB0, srdB, soB + (i >> 1) * 2048 + (i & 1) * 8 * 6144);
        } else dma16((i < 4 ? dA0 : dA1) + (i & 3) * 1024, (i & 1) ? voffO : voffE, srdA, (i < 4 ? soA0 : soA1) + ((i >> 1) & 1) * 32768);
    };
    if (half == 0) {
#pragma unroll
        for (int i = 0; i < 7; ++i) piece(std::integral_constant<int, 0>{}, 0, 0, 0, i);
    } else {
#pragma unroll
        for (int i = 0; i < 7; ++i) piece(std::integral_constant<int, 1>{}, 0, 0, 0, i);
    }
    SBAR();
    const int lane = lane_id();
    const int r16 = lane & 15, g4 = lane >> 4, q4 = (lane & 15) >> 2, p4 = lane & 3;
    f32x4 acc[4][6];
    const int r32 = lane & 31, hh = lane >> 5;
    f32x16 mk;
#pragma unroll
    for (int r = 0; r < 16; ++r) mk[r] = ((r & 3) + 8 * (r >> 2) <= r32 - 4 * hh) ? 0.f : -INFINITY;
    unsigned aoff[2], boff[2];
    {
#pragma unroll
        for (int s = 0; s < 2; ++s) aoff[s] = (half ? LDS_A1 : LDS_A0) + ((w4 >> 1) * 64 + r16) * 128 + (((4 * s + g4) ^ ((r16 >> 1) & 7)) << 4);
#pragma unroll
        for (int c = 0; c < 2; ++c) boff[c] = LDS_B + (8 * g4 + q4) * 128 + (((wc * 2 + c) ^ ((q4 >> 1) | ((g4 & 1) << 1))) << 5) + p4 * 8;
#pragma unroll
        for (int i = 0; i < 4; ++i)
#pragma unroll
            for (int s = 0; s < 6; ++s) acc[i][s] = (f32x4){0.f, 0.f, 0.f, 0.f};
        bf16x8 af[4][2], bfr[6][2];
        const unsigned voffT = (16 * (rb & 3) + 4 * w4 + lane / 3) * 6144 + (lane % 3) * 2048 + hp * 128;
        auto run = [&](auto HC, auto TC) {
            constexpr int H = decltype(HC)::value;
            constexpr int TW = decltype(TC)::value;
            unsigned sink = 0;
            auto touch = [&](int kt) { if (lane < 12) ld4(sink, voffT, srdB, kt * (64 * 6144)); };
#pragma unroll
            for (int i = 0; i < 4; ++i) asm volatile("" : "+v"(acc[i][0]), "+v"(acc[i][1]), "+v"(acc[i][2]), "+v"(acc[i][3]), "+v"(acc[i][4]), "+v"(acc[i][5]));
            PIN(mk); PIN(aoff[0]); PIN(aoff[1]); PIN(boff[0]); PIN(boff[1]);
            SBAR();
#pragma unroll
            for (int i = 0; i < 7; ++i) piece(HC, 1, 1, 1, i);
            if (H == 0) waitv<7>(); else waitv<10>();
            BAR();
            if (TW) {
#pragma unroll
                for (int kt = 3; kt < PD; ++kt) touch(kt);
            }
            if (H == 1) { waitv<7>(); BAR(); }
            int s3 = 0;
            auto Lseg = [&](int t, bool issue) {
                const unsigned aslot = H ? s3 * A_SLOT : (t & 1) * A_SLOT;
                const unsigned bslot = s3 * B_SLOT;
                const int s3n = (s3 == 0) ? 2 : s3 - 1;
                auto rdA = [&](int i) { LAS const char* pa = lds + aoff[i >> 1] + aslot + (i & 1) * 4096; af[(i & 1) * 2][i >> 1] = ldsfrag(pa); af[(i & 1) * 2 + 1][i >> 1] = ldsfrag(pa + 2048); };
                auto rdB = [&](int i) { const int s = i / 6, seg = (i % 6) >> 1, c = i & 1; LAS const char* pb = lds + boff[c] + bslot;
                    s16x4 lo = trread(pb + seg * 8192 + s * 4096), hi = trread(pb + seg * 8192 + s * 4096 + 512);
                    bfr[seg * 2 + c][s] = __builtin_shufflevector(lo, hi, 0, 1, 2, 3, 4, 5, 6, 7); };
#pragma unroll
                for (int i = 0; i < 16; ++i) {
                    if (issue && (i & 1) == 0 && i / 2 < 7) piece(HC, t + 2, t & 1, s3n, i / 2);
                    if (i < 4) rdA(i); else if (issue || (i - 4) % 6 >= 2) rdB(i - 4);
                }
                if (TW && issue) touch(t + PD);
                s3 = (s3 == 2) ? 0 : s3 + 1;
            };
            auto Mseg = [&](int n0) {
                __builtin_amdgcn_s_setprio(1);
#pragma unroll
                for (int s = 0; s < 2; ++s)
#pragma unroll
                    for (int m = 0; m < 4; ++m)
#pragma unroll
                        for (int n = n0; n < 6; ++n) acc[m][n] = n < 4 ? MFMA16(bfr[n][s], af[m][s], acc[m][n]) : MFMA16(af[m][s], bfr[n][s], acc[m][n]);
                __builtin_amdgcn_s_setprio(0);
            };
            if (TW) { Lseg(0, true); WAITL0(); BAR(); Mseg(0); waitv<8 + PD - 3>(); BAR(); }
            for (int t = TW; t < 14; ++t) {
                Lseg(t, true);
                if (H == 1 && __builtin_expect(t == 6, 0)) {
                    const char* pa = lane == 0 ? wsp + WS_KV + (size_t)me * 65536 : lane == 1 ? wsp + WS_O + (size_t)((br * 128 + rb * 16) * 32 + hp * 2) * 1024
                                   : lane == 2 ? (const char*)(outb + (size_t)(rb * 256 + (hp >> 3) * 128) * 2048 + br * 1024 + (hp & 7) * 128) : (const char*)wop + (lane - 3) * 1048576;
                    if (w4 == 0 && lane < 5) asm volatile("global_load_dword %0, %1, off" : "+v"(sink) : "v"(pa) : "memory");
                }
                if (H == 1) waitv<10>();
                WAITL0(); BAR();
                Mseg(0);
                waitv<7 + 2 * TW>();
                BAR();
            }
            Lseg(14, false); if (H == 1) waitv<3>(); WAITL0(); BAR(); Mseg(2); waitv<0>(); asm volatile("" :: "v"(sink)); BAR();
            Lseg(15, false); WAITL0(); BAR(); Mseg(2); if (H == 0) BAR();
        };
        if (half == 1) run(std::integral_constant<int, 1>{}, std::integral_constant<int, 0>{});
        else if (slowc) run(std::integral_constant<int, 0>{}, std::integral_constant<int, 1>{});
        else run(std::integral_constant<int, 0>{}, std::integral_constant<int, 0>{});
    }
    const u64 tagp = (((u64)__builtin_amdgcn_readlane((unsigned)(ew >> 32), 0) << 32) | __builtin_amdgcn_readlane((unsigned)ew, 0)) + TAGK;
    const u64 tagc = (((u64)__builtin_amdgcn_readlane((unsigned)(ew >> 32), 1) << 32) | __builtin_amdgcn_readlane((unsigned)ew, 1)) + TAGK;
    const __amdgpu_buffer_rsrc_t srdW = __builtin_amdgcn_make_buffer_rsrc((void*)wsp, (short)0, (int)WS_END, 0x00020000);
    const __amdgpu_buffer_rsrc_t srdWo = __builtin_amdgcn_make_buffer_rsrc((void*)(wop), (short)0, 1024 * 2048, 0x00020000);
    const int ct = hp & 7, th = hp >> 3;
    const unsigned sW = ct * 256 + (half * 512 + w4 * 8) * 2048;
    auto wo_steps = [&](int s0, int s1) {
        const int ln = lane_id();
        const unsigned voffW0 = (ln >> 4) * 2048 + (((ln & 15) ^ ((ln >> 4) << 1)) << 4), voffW1 = (4 + (ln >> 4)) * 2048 + (((ln & 15) ^ ((4 + (ln >> 4)) << 1)) << 4);
#pragma unroll
        for (int s = s0; s < s1; ++s) { const unsigned d = ldsb + ((s + 8) % 10) * 16384 + wid * 2048; dma16(d, voffW0, srdWo, sW + s * 65536); dma16(d + 1024, voffW1, srdWo, sW + s * 65536); } };
    bf16x8 qf[2][2];
    {
        const unsigned kpos = (g4 >> 1) * 1024 + (g4 & 1) * 512 + r16 * 16;
        const unsigned vpos = 2048 + ((g4 & 1) * 32 + (g4 >> 1) * 16 + r16) * 16;
        const unsigned gkv = (unsigned)WS_KV + me * 65536 + wc * 32768;
        u32x4 kv[2][2], vv[2][2];
#pragma unroll
        for (int mi = 0; mi < 2; ++mi)
#pragma unroll
            for (int s = 0; s < 2; ++s) {
                const f32x4 k0 = acc[2 * mi + s][2], k1 = acc[2 * mi + s][3], v0 = acc[2 * mi + s][4], v1 = acc[2 * mi + s][5];
                kv[mi][s] = (u32x4){cvtpk(k0[0], k0[1]), cvtpk(k0[2], k0[3]), cvtpk(k1[0], k1[1]), cvtpk(k1[2], k1[3])};
                const auto ra = __builtin_amdgcn_permlane32_swap(cvtpk(v0[0], v0[1]), cvtpk(v1[0], v1[1]), false, false);
                const auto rbb = __builtin_amdgcn_permlane32_swap(cvtpk(v0[2], v0[3]), cvtpk(v1[2], v1[3]), false, false);
                vv[mi][s] = (u32x4){ra[0], rbb[0], ra[1], rbb[1]};
            }
        auto kvout = [&]() {
#pragma unroll
            for (int mi = 0; mi < 2; ++mi)
#pragma unroll
                for (int s = 0; s < 2; ++s) { const unsigned blk = (mi ? 7 - wr : wr) * 4096;
                    __builtin_amdgcn_raw_buffer_store_b128(kv[mi][s], srdW, gkv + blk + kpos + s * 256, 0, 16);
                    __builtin_amdgcn_raw_buffer_store_b128(vv[mi][s], srdW, gkv + blk + vpos + s * 1024, 0, 16); }
        };
        if (rb < 7 && half == 0) kvout();
        SBAR();
#pragma unroll
        for (int u = 0; u < 2; ++u) {
            const unsigned aslot = u ? (half ? 0 : A_SLOT) : (half ? 2 * A_SLOT : 0), bslot = u ? 0 : 2 * B_SLOT;
            bf16x8 af[4][2], bq[2][2];
#pragma unroll
            for (int s = 0; s < 2; ++s) {
#pragma unroll
                for (int m = 0; m < 4; ++m) af[m][s] = ldsfrag(lds + aoff[s] + aslot + m * 2048);
#pragma unroll
                for (int c = 0; c < 2; ++c) { const s16x4 lo = trread(lds + boff[c] + bslot + s * 4096), hi = trread(lds + boff[c] + bslot + s * 4096 + 512);
                    bq[c][s] = __builtin_shufflevector(lo, hi, 0, 1, 2, 3, 4, 5, 6, 7); }
            }
#pragma unroll
            for (int s = 0; s < 2; ++s)
#pragma unroll
                for (int m = 0; m < 4; ++m)
#pragma unroll
                    for (int c = 0; c < 2; ++c) acc[m][c] = MFMA16(bq[c][s], af[m][s], acc[m][c]);
        }
        WAITL0(); BAR();
        if (rb < 7 && half == 1) kvout();
#pragma unroll
        for (int mi = 0; mi < 2; ++mi)
#pragma unroll
            for (int s = 0; s < 2; ++s) { const unsigned blk = wc * 65536 + 32768 + (mi ? 7 - wr : wr) * 4096;
                *(LAS u32x4*)(lds + blk + kpos + s * 256) = kv[mi][s];
                *(LAS u32x4*)(lds + blk + vpos + s * 1024) = vv[mi][s]; }
#pragma unroll
        for (int mi = 0; mi < 2; ++mi) {
            u32x4 x[2], y0, y1;
#pragma unroll
            for (int s = 0; s < 2; ++s) { const f32x4 q0 = acc[2 * mi + s][0] * QSCALE, q1 = acc[2 * mi + s][1] * QSCALE;
                x[s] = (u32x4){cvtpk(q0[0], q0[1]), cvtpk(q0[2], q0[3]), cvtpk(q1[0], q1[1]), cvtpk(q1[2], q1[3])}; }
#pragma unroll
            for (int i = 0; i < 4; ++i) { const auto u = __builtin_amdgcn_permlane16_swap(x[0][i], x[1][i], false, false);
                const auto v = __builtin_amdgcn_permlane32_swap(u[0], u[1], false, false); y0[i] = v[0]; y1[i] = v[1]; }
            qf[mi][0] = __builtin_bit_cast(bf16x8, y0); qf[mi][1] = __builtin_bit_cast(bf16x8, y1);
        }
    }
    f32x16 o[2], nm[2]; f32x4 l[2];
    LAS const char* kvb = lds + wc * 65536 + lane * 16;
    bf16x8 ones;
    { const short v = ((lane ^ (lane >> 4)) & 1) ? (short)0 : (short)0x3f80;
#pragma unroll
      for (int i = 0; i < 8; ++i) ones[i] = v; }
    auto jmax = [&](const f32x16& s, float m) {
#pragma unroll
        for (int r = 0; r < 16; r += 2) m = max3f(m, s[r], s[r + 1]);
        return m; };
    auto xmax = [&](float m) { const auto r = __builtin_amdgcn_permlane32_swap(__float_as_uint(m), __float_as_uint(m), false, false); return __builtin_fmaxf(__uint_as_float(r[0]), __uint_as_float(r[1])); };
    auto expo = [&](const f32x16& s, bf16x8& p0, bf16x8& p1) {
        float t[16];
#pragma unroll
        for (int r = 0; r < 16; ++r) t[r] = __builtin_amdgcn_exp2f(s[r]);
        p0 = pack8(t[0], t[1], t[2], t[3], t[4], t[5], t[6], t[7]); p1 = pack8(t[8], t[9], t[10], t[11], t[12], t[13], t[14], t[15]); };
    auto flip = [&](const f32x16& m) { f32x16 c;
#pragma unroll
        for (int r = 0; r < 16; ++r) c[r] = __uint_as_float(__float_as_uint(m[r]) ^ 0xff800000u);
        return c; };
    {
        f32x16 zero16, s[2];
#pragma unroll
        for (int r = 0; r < 16; ++r) zero16[r] = 0.f;
#pragma unroll
        for (int d = 0; d < 2; ++d) {
            LAS const char* p = kvb + (8 + (d ? 7 - wr : wr)) * 4096;
            s[d] = MFMA32(ldsfrag(p), qf[d][0], mk); s[d] = MFMA32(ldsfrag(p + 1024), qf[d][1], s[d]);
        }
        const float m0 = xmax(jmax(s[0], -INFINITY)), m1 = xmax(jmax(s[1], -INFINITY));
#pragma unroll
        for (int d = 0; d < 2; ++d) {
            LAS const char* p = kvb + (8 + (d ? 7 - wr : wr)) * 4096;
            const float m = d ? m1 : m0;
#pragma unroll
            for (int r = 0; r < 16; ++r) { nm[d][r] = -m; s[d][r] = s[d][r] - m; }
            bf16x8 p0, p1;
            expo(s[d], p0, p1);
            o[d] = MFMA32(ldsfrag(p + 2048), p0, zero16); l[d] = MFMA16(ones, p0, ((f32x4){0.f, 0.f, 0.f, 0.f}));
            o[d] = MFMA32(ldsfrag(p + 3072), p1, o[d]); l[d] = MFMA16(ones, p1, l[d]);
        }
    }
    mk = flip(mk); PIN(mk);
    waitv<0>(); WAITL0(); BAR();
    if (wid == 0 && lane == 0 && rb < 7) __hip_atomic_store((GAS u64*)(wsp + WS_FKV + (size_t)me * 128), tagp, __ATOMIC_RELAXED, __HIP_MEMORY_SCOPE_AGENT);
    wo_steps(0, 2);
    unsigned wot = 0;
    auto wotouch = [&]() { const int e = ((rb & 3) * 8 + wid) * 56 + lane, row = e >> 1;
        if (lane < 56) wot = __builtin_amdgcn_raw_buffer_load_b32(srdWo, (64 + row + (row >= 448 ? 64 : 0)) * 2048 + ct * 256 + (e & 1) * 128, 0, 0);
        SBAR(); };
    auto attention = [&](auto WC_) {
        constexpr int WR = decltype(WC_)::value;
        u32x4 pld[8]; u64 pv = ~tagc;
        auto jobs = [&](auto PART) {
            constexpr int P2 = decltype(PART)::value;
            constexpr int NJ = P2 ? 9 : 7;
            auto TOF = [](int j) { return P2 ? (j < 2 ? j : (j - 2 < WR ? 1 : 0)) : (j < WR ? 0 : 1); };
            auto BOF = [](int j) { return P2 ? (j < 2 ? (j ? 7 - WR : WR) : (j - 2 < WR ? 6 - WR + j : j - 1)) : 8 + (j < WR ? j : j - WR); };
            f32x16 S[2], cm; bf16x8 kf[2][2], vf[2][2]; u32x4 p0, p1;
            auto rdk = [&](int j) { kf[j & 1][0] = ldsfrag(kvb + BOF(j) * 4096); kf[j & 1][1] = ldsfrag(kvb + BOF(j) * 4096 + 1024); };
            auto cof = [&](int j) -> const f32x16& { return (P2 && j < 2) ? cm : nm[TOF(j)]; };
            auto mkc = [&](int j) {
#pragma unroll
                for (int r = 0; r < 16; ++r) cm[r] = mk[r] + nm[TOF(j)][r]; };
            if (P2) {
                mkc(0);
#pragma unroll
                for (int k = 0; k < 4; ++k) *(LAS u32x4*)(lds + wc * 65536 + WR * 4096 + k * 1024 + lane * 16) = pld[k];
            }
            rdk(0); vf[0][0] = ldsfrag(kvb + BOF(0) * 4096 + 2048);
            S[0] = MFMA32(kf[0][0], qf[TOF(0)][0], cof(0)); S[0] = MFMA32(kf[0][1], qf[TOF(0)][1], S[0]);
            if (P2) PIN(S[0]);
            SBAR();
            if (P2) {
#pragma unroll
                for (int k = 0; k < 4; ++k) *(LAS u32x4*)(lds + wc * 65536 + (7 - WR) * 4096 + k * 1024 + lane * 16) = pld[4 + k];
            }
            rdk(1);
            SBAR();
#pragma unroll
            for (int j = 0; j < NJ; ++j) {
                const int T = TOF(j), TP = j > 0 ? TOF(j - 1) : 0, TN = j + 1 < NJ ? TOF(j + 1) : 0;
                f32x16& s = S[j & 1];
#define EX(i) s[i] = __builtin_amdgcn_exp2f(s[i])
                if (!P2 && j == POLJ && WR == 0) pv = __hip_atomic_load((GAS u64*)(wsp + WS_FKV + (size_t)(rb > 0 ? me - 16 : me) * 128), __ATOMIC_RELAXED, __HIP_MEMORY_SCOPE_AGENT);
                if (P2 && j == 0) { mkc(1); PIN(cm); }
                if (j + 1 < NJ) S[(j + 1) & 1] = MFMA32(kf[(j + 1) & 1][0], qf[TN][0], cof(j + 1));
                SBAR();
                vf[j & 1][1] = ldsfrag(kvb + BOF(j) * 4096 + 3072);
                EX(0); EX(1); EX(2); PIN(s); SBAR();
                if (j > 0) o[TP] = MFMA32(vf[(j - 1) & 1][1], __builtin_bit_cast(bf16x8, p1), o[TP]);
                SBAR();
                EX(3); EX(4); EX(5); PIN(s); SBAR();
                if (j > 0) l[TP] = MFMA16(ones, __builtin_bit_cast(bf16x8, p1), l[TP]);
                SBAR();
                EX(6); EX(7);
                p0 = (u32x4){cvtpk(s[0], s[1]), cvtpk(s[2], s[3]), cvtpk(s[4], s[5]), cvtpk(s[6], s[7])}; PIN(p0); SBAR();
                if (j + 1 < NJ) S[(j + 1) & 1] = MFMA32(kf[(j + 1) & 1][1], qf[TN][1], S[(j + 1) & 1]);
                SBAR();
                if (j + 1 < NJ) vf[(j + 1) & 1][0] = ldsfrag(kvb + BOF(j + 1) * 4096 + 2048);
                EX(8); EX(9); EX(10); PIN(s); SBAR();
                o[T] = MFMA32(vf[j & 1][0], __builtin_bit_cast(bf16x8, p0), o[T]);
                SBAR();
                if (P2 && j == 0) { WAITL0(); BAR(); wotouch(); }
                if (j + 2 < NJ) rdk(j + 2);
                EX(11); EX(12); EX(13); PIN(s); SBAR();
                l[T] = MFMA16(ones, __builtin_bit_cast(bf16x8, p0), l[T]);
                SBAR();
                EX(14); EX(15);
                p1 = (u32x4){cvtpk(s[8], s[9]), cvtpk(s[10], s[11]), cvtpk(s[12], s[13]), cvtpk(s[14], s[15])}; PIN(p1); SBAR();
#undef EX
                if (j == NJ - 1) { o[T] = MFMA32(vf[j & 1][1], __builtin_bit_cast(bf16x8, p1), o[T]); l[T] = MFMA16(ones, __builtin_bit_cast(bf16x8, p1), l[T]); }
                if (!P2 && j == POLJ && rb > 0) {
                    if (WR == 0 && wid == 0) {
                        GAS u64* f = (GAS u64*)(wsp + WS_FKV + (size_t)(me - 16) * 128);
#pragma nounroll
                        for (int spin = 0; spin < SPIN_CAP; ++spin) {
                            if (rfl((unsigned)pv) == (unsigned)tagc && rfl((unsigned)(pv >> 32)) == (unsigned)(tagc >> 32)) break;
                            __builtin_amdgcn_s_sleep(1);
                            pv = __hip_atomic_load(f, __ATOMIC_RELAXED, __HIP_MEMORY_SCOPE_AGENT);
                        }
                        if (lane == 0) __hip_atomic_store((GAS u64*)(wsp + WS_EKV + (size_t)(me - 16) * 128), tagc, __ATOMIC_RELAXED, __HIP_MEMORY_SCOPE_AGENT);
                    }
                    BAR();
#pragma unroll
                    for (int k = 0; k < 8; ++k) pld[k] = __builtin_amdgcn_raw_buffer_load_b128(srdW, (unsigned)(lane * 16 + (k & 3) * 1024), (unsigned)WS_KV + (me - 16) * 65536 + wc * 32768 + (k < 4 ? WR : 7 - WR) * 4096, 16);
                    SBAR();
                }
            }
        };
        jobs(std::integral_constant<int, 0>{});
        if (rb > 0) {
            jobs(std::integral_constant<int, 1>{});
        }
    };
#define ATT(W) attention(std::integral_constant<int, W>{})
    if (wr == 0) ATT(0); else if (wr == 1) ATT(1); else if (wr == 2) ATT(2); else ATT(3);
    asm volatile("" :: "v"(wot));
#pragma unroll
    for (int d = 0; d < 2; ++d) {
        const int gt = d ? 7 - wr : wr;
        float lt = (lane & 16) ? l[d][1] : l[d][0];
        if (__builtin_expect(__any(!(lt <= LSAFE)), 0)) {
            const f32x16 mhi = mk, mlo = flip(mk);
            const int b0 = rb > 0 ? gt : 8, b1 = 8 + gt;
            auto sc = [&](int b, float ref) { f32x16 c;
#pragma unroll
                for (int r = 0; r < 16; ++r) c[r] = (b == b1 ? mlo[r] : b == gt ? mhi[r] : 0.f) - ref;
                c = MFMA32(ldsfrag(kvb + b * 4096), qf[d][0], c); return MFMA32(ldsfrag(kvb + b * 4096 + 1024), qf[d][1], c); };
            float m = -INFINITY;
#pragma nounroll
            for (int b = b0; b <= b1; ++b) m = jmax(sc(b, 0.f), m);
            m = xmax(m);
            f32x4 ls = {0.f, 0.f, 0.f, 0.f};
#pragma unroll
            for (int r = 0; r < 16; ++r) o[d][r] = 0.f;
#pragma nounroll
            for (int b = b0; b <= b1; ++b) { bf16x8 p0, p1; expo(sc(b, m), p0, p1);
                o[d] = MFMA32(ldsfrag(kvb + b * 4096 + 2048), p0, o[d]); o[d] = MFMA32(ldsfrag(kvb + b * 4096 + 3072), p1, o[d]);
                ls = MFMA16(ones, p0, ls); ls = MFMA16(ones, p1, ls); }
            lt = (lane & 16) ? ls[1] : ls[0];
        }
        const float inv = __builtin_amdgcn_rcpf(lt);
        const unsigned base = (unsigned)WS_O + (((br * 128 + rb * 16 + gt * 2 + (r32 >> 4)) * 32) + hp * 2 + wc) * 1024 + hh * 256 + (r32 & 15) * 16;
#pragma unroll
        for (int e = 0; e < 2; ++e) {
            const f32x16& x = o[d];
            const u32x4 v = {cvtpk(x[8 * e] * inv, x[8 * e + 1] * inv), cvtpk(x[8 * e + 2] * inv, x[8 * e + 3] * inv), cvtpk(x[8 * e + 4] * inv, x[8 * e + 5] * inv), cvtpk(x[8 * e + 6] * inv, x[8 * e + 7] * inv)};
            __builtin_amdgcn_raw_buffer_store_b128(v, srdW, base + e * 512, 0, 16);
        }
    }
    auto phase3 = [&](auto HC) {
        constexpr int H = decltype(HC)::value;
        const int lane = lane_id(), r16 = lane & 15, g4 = lane >> 4, q4 = (lane & 15) >> 2, p4 = lane & 3;
        GAS char* ep = (GAS char*)wsp + WS_EO + (lane < 16 ? (size_t)(grp + lane) * 128 + hp * 8 : (size_t)me * 128 + (lane & 15) * 8);
        u64 eo;
        asm volatile("global_load_dwordx2 %0, %1, off sc1" : "=v"(eo) : "v"(ep) : "memory");
        unsigned woff[8];
        { const int row = 16 * (g4 >> 1) + 4 * (g4 & 1) + q4;
#pragma unroll
          for (int n = 0; n < 8; ++n) woff[n] = 131072 + H * 8192 + row * 256 + ((n ^ (row & 7)) << 5) + p4 * 8; }
        bf16x8 wf[2][8];
        auto wread = [&](int s, int n) {
            if (s == 2 || s == 12) { woff[n] -= 131072; PIN(woff[n]); } else if (s == 6 || s == 10) { woff[n] += 65536; PIN(woff[n]); }
            LAS const char* p = lds + woff[n] + (((s + 8) % 10) & 3) * 16384;
            const s16x4 lo = trread(p), hi = trread(p + 2048); wf[s & 1][n] = __builtin_shufflevector(lo, hi, 0, 1, 2, 3, 4, 5, 6, 7); };
        if (rb == 0) { waitv<5>(); BAR(); }
#pragma unroll
        for (int n = 0; n < 8; ++n) { wread(0, n); wread(1, n); }
        waitv<0>(); PIN(eo); WAITL0(); BAR();
        if (H == 0 && wid == 0) {
            const u64 tag = eo + TAGK;
            if (lane < 16) __hip_atomic_store((GAS u64*)(ep - WS_EO + WS_FO), tag, __ATOMIC_RELAXED, __HIP_MEMORY_SCOPE_AGENT);
        }
        wo_steps(2, 12);
        if (H == 0 && wid == 0) {
            const u64 tag = eo + TAGK;
            GAS u64* f = (GAS u64*)(wsp + WS_FO + (size_t)me * 128 + (lane & 15) * 8);
            const bool mine = lane >= 16 && lane < 32;
#pragma nounroll
            for (int spin = 0; spin < SPIN_CAP; ++spin) {
                const u64 v = __hip_atomic_load(f, __ATOMIC_RELAXED, __HIP_MEMORY_SCOPE_AGENT);
                if (__all(!mine || v == tag)) break;
                __builtin_amdgcn_s_sleep(1);
            }
            if (mine) __hip_atomic_store((GAS u64*)ep, tag, __ATOMIC_RELAXED, __HIP_MEMORY_SCOPE_AGENT);
        }
        waitv<0>(); BAR();
        bf16x8 of[OW][2];
        const unsigned obase = (unsigned)WS_O + ((br * 128 + rb * 16 + th * 8 + w4 * 2) * 32 + H * 16) * 1024 + lane * 16;
        auto oload = [&](int s, int t) { of[s % OW][t] = __builtin_bit_cast(bf16x8, __builtin_amdgcn_raw_buffer_load_b128(srdW, obase + t * 32768 + s * 1024, 0, 16)); };
#pragma unroll
        for (int s = 0; s < OW - 1; ++s) { oload(s, 0); oload(s, 1); SBAR(); }
        f32x4 c2[2][8];
#pragma unroll
        for (int t = 0; t < 2; ++t)
#pragma unroll
            for (int n = 0; n < 8; ++n) c2[t][n] = (f32x4){0.f, 0.f, 0.f, 0.f};
        const unsigned voffW0 = (lane >> 4) * 2048 + (((lane & 15) ^ ((lane >> 4) << 1)) << 4), voffW1 = (4 + (lane >> 4)) * 2048 + (((lane & 15) ^ ((4 + (lane >> 4)) << 1)) << 4);
        {
            auto step = [&](int s) {
#pragma unroll
                for (int n = 0; n < 8; ++n) {
                    c2[0][n] = MFMA16(wf[s & 1][n], of[s % OW][0], c2[0][n]); c2[1][n] = MFMA16(wf[s & 1][n], of[s % OW][1], c2[1][n]);
                    SBAR();
                    if (s + 2 < 16) wread(s + 2, n);
                    if (n == 1 + 2 * H && s + OW - 1 < 16) oload(s + OW - 1, 0);
                    if (n == 5 + 2 * H && s + OW - 1 < 16) oload(s + OW - 1, 1);
                    if (s >= 4 && s < 8 && (n == 3 - 2 * H || n == 7 - 2 * H)) dma16(ldsb + ((s + 16) % 10) * 16384 + wid * 2048 + (n >= 4) * 1024, n >= 4 ? voffW1 : voffW0, srdWo, sW + (s + 8) * 65536);
                    SBAR();
                } };
#pragma unroll
            for (int s = 0; s < 4; ++s) step(s);
            WAITL0(); BAR();
#pragma unroll
            for (int s = 4; s < 10; ++s) step(s);
            waitv<4>(); BAR();
#pragma unroll
            for (int s = 10; s < 15; ++s) step(s);
#pragma unroll
            for (int n = 4 - 4 * H; n < 8 - 4 * H; ++n) { c2[0][n] = MFMA16(wf[1][n], of[15 % OW][0], c2[0][n]); c2[1][n] = MFMA16(wf[1][n], of[15 % OW][1], c2[1][n]); }
            SBAR();
#pragma unroll
            for (int n = 0; n < 4; ++n) {
                c2[0][4 * H + n] = MFMA16(wf[1][4 * H + n], of[15 % OW][0], c2[0][4 * H + n]); c2[1][4 * H + n] = MFMA16(wf[1][4 * H + n], of[15 % OW][1], c2[1][4 * H + n]);
                SBAR();
#pragma unroll
                for (int t = 0; t < 2; ++t) *(LAS f32x4*)(lds + 65536 + wid * 8192 + (t * 4 + n) * 1024 + lane * 16) = c2[t][4 - 4 * H + n];
                SBAR();
            }
            WAITL0(); BAR();
            GAS u16* outp = (GAS u16*)outb + (size_t)(rb * 256 + th * 128 + w4 * 32 + r16) * 2048 + br * 1024 + ct * 128 + H * 64 + (g4 & 1) * 16 + (g4 >> 1) * 8;
#pragma unroll
            for (int t = 0; t < 2; ++t)
#pragma unroll
                for (int n = 0; n < 4; n += 2) {
                    const f32x4 x = c2[t][4 * H + n] + *(LAS const f32x4*)(lds + 65536 + (wid ^ 4) * 8192 + (t * 4 + n) * 1024 + lane * 16);
                    const f32x4 y = c2[t][4 * H + n + 1] + *(LAS const f32x4*)(lds + 65536 + (wid ^ 4) * 8192 + (t * 4 + n + 1) * 1024 + lane * 16);
                    const auto u0 = __builtin_amdgcn_permlane16_swap(cvtpk(x[0], x[1]), cvtpk(y[0], y[1]), false, false);
                    const auto u1 = __builtin_amdgcn_permlane16_swap(cvtpk(x[2], x[3]), cvtpk(y[2], y[3]), false, false);
                    { const u32x4 vv = {u0[0], u1[0], u0[1], u1[1]}; GAS u16* pp = outp + (size_t)t * 16 * 2048 + n * 16; asm volatile("global_store_dwordx4 %0, %1, off sc0 sc1\n\ts_nop 1" :: "v"(pp), "v"(vv) : "memory"); }
                }
        }
    };
    if (half == 0) phase3(std::integral_constant<int, 0>{}); else phase3(std::integral_constant<int, 1>{});
}

extern "C" void kernel_launch(void* const* d_in, const int* in_sizes, int n_in, void* d_out, int out_size, void* d_ws, size_t ws_size, hipStream_t stream) {
    Args a;
    a.x[0] = (const u16*)d_in[0]; a.w[0] = (const u16*)d_in[1]; a.wo[0] = (const u16*)d_in[2];
    a.x[1] = (const u16*)d_in[3]; a.w[1] = (const u16*)d_in[4]; a.wo[1] = (const u16*)d_in[5];
    a.out = (u16*)d_out; a.ws = (char*)(((size_t)d_ws + 4095) & ~(size_t)4095);
    concat_heads_sliding_block_kernel<<<256, 512, 0, stream>>>(a);
}
```
